```python
import torch

def _softmax(x, axis=-1):
    return torch.softmax(x, dim=axis)
def _gelu(x):
    return torch.nn.functional.gelu(x, approximate="tanh")
def _silu(x):
    return torch.nn.functional.silu(x)
def _sigmoid(x):
    return torch.sigmoid(x)
def _layernorm(x, axis=-1, eps=1e-5):
    m = x.mean(dim=axis, keepdim=True)
    v = x.var(dim=axis, keepdim=True, unbiased=False)
    return (x - m) / torch.sqrt(v + eps)
def _scatter_add(tgt, idx, src, axis=0):
    return tgt.clone().index_add_(axis, idx, src)
def _segment_sum(src, seg, n):
    out = torch.zeros((n,) + src.shape[1:], dtype=src.dtype)
    return out.index_add_(0, seg, src)
def _pad(x, pw):
    flat = tuple(v for pair in reversed(pw) for v in pair)
    return torch.nn.functional.pad(x, flat)

kernel_name = 'interleave_layers__gqa_block_cached__glu_gated_reglu__decode'

def setup_inputs():

    _w = (lambda a: (lambda t: t.to(torch.bfloat16) if t.is_floating_point() else t)(torch.as_tensor(a)))
    def _A_setup():
        B, Sq, D = 1, 1, 512
        Hq, Hkv, Sk, Dh = 32, 8, 65536, 128
        assert Hq % Hkv == 0, (Hq, Hkv)
        return {
            "x": _w((lambda *s: torch.randn(*s, dtype=torch.bfloat16) * 0.05)(B, Sq, D)),
            "k_cache": _w((lambda *s: torch.randn(*s, dtype=torch.bfloat16) * 0.8)(B, Hkv, Sk, Dh)),
            "v_cache": _w((lambda *s: torch.randn(*s, dtype=torch.bfloat16) * 1.6)(B, Hkv, Sk, Dh)),
            "wq": _w((lambda *s: torch.randn(*s, dtype=torch.bfloat16) * 0.8)(D, Hq * Dh) * (D ** -0.5)),
            "wo": _w((lambda *s: torch.randn(*s, dtype=torch.bfloat16) * 1.6)(Hq * Dh, D) * ((Hq * Dh) ** -0.5)),
        }
    _A_in = _A_setup()

    def _B_setup():
        B, S, D, F = 1, 1, 512, 1792
        return {
            "x": _w((lambda *s: torch.randn(*s, dtype=torch.bfloat16) * 0.05)(B, S, D)),
            "W_gate": _w((lambda *s: torch.randn(*s, dtype=torch.bfloat16) * 0.2)(D, F) * (2.0 / D) ** 0.5),
            "W_up": _w((lambda *s: torch.randn(*s, dtype=torch.bfloat16) * 0.2)(D, F) * (2.0 / D) ** 0.5),
            "W_down": _w((lambda *s: torch.randn(*s, dtype=torch.bfloat16) * 0.2)(F, D) * (1.0 / F) ** 0.5),
        }
    _B_in = _B_setup()

    return {
        "A_x": _A_in["x"],
        "A_k_cache": _A_in["k_cache"],
        "A_v_cache": _A_in["v_cache"],
        "A_wq": _A_in["wq"],
        "A_wo": _A_in["wo"],
        "B_x": _B_in["x"],
        "B_W_gate": _B_in["W_gate"],
        "B_W_up": _B_in["W_up"],
        "B_W_down": _B_in["W_down"],
    }

def reference(A_x, A_k_cache, A_v_cache, A_wq, A_wo, B_x, B_W_gate, B_W_up, B_W_down):
    def _A_ref(x, k_cache, v_cache, wq, wo):
        B, Sq, D = x.shape
        Hq, Dh = 32, 128
        Hkv = k_cache.shape[1]
        rep = Hq // Hkv
        q = (lambda x, *d: x.permute(*d))((x @ wq).reshape(B, Sq, Hq, Dh), 0, 2, 1, 3)
        k = (lambda x, n, axis: torch.repeat_interleave(x, n, dim=axis))(k_cache, rep, axis=1)
        v = (lambda x, n, axis: torch.repeat_interleave(x, n, dim=axis))(v_cache, rep, axis=1)
        scores = torch.einsum("bhqd,bhkd->bhqk", q, k) * (Dh ** -0.5)
        p = _softmax(scores, axis=-1)
        o = torch.einsum("bhqk,bhkd->bhqd", p, v)
        return (lambda x, *d: x.permute(*d))(o, 0, 2, 1, 3).reshape(B, Sq, Hq * Dh) @ wo

    def _B_ref(x, W_gate, W_up, W_down):
        gate = torch.einsum("bsd,df->bsf", x, W_gate)
        up = torch.einsum("bsd,df->bsf", x, W_up)
        gate = gate * (lambda x, dt: x.to(dt))(gate > 0, gate.dtype)
        h = gate * up
        return torch.einsum("bsf,fd->bsd", h, W_down)

    x = A_x
    x = x + _A_ref(_layernorm(x, axis=-1), A_k_cache, A_v_cache, A_wq, A_wo)
    x = x + _B_ref(_layernorm(x, axis=-1), B_W_gate, B_W_up, B_W_down)
    x = x + _A_ref(_layernorm(x, axis=-1), A_k_cache, A_v_cache, A_wq, A_wo)
    x = x + _B_ref(_layernorm(x, axis=-1), B_W_gate, B_W_up, B_W_down)
    return x
```

```cpp
#include <hip/hip_runtime.h>
#include <hip/hip_bf16.h>
#include <stdint.h>

#define AUX_P1 2
#define AUX_P2 2
#define REV2 1

typedef unsigned v4u __attribute__((ext_vector_type(4)));
typedef float f32x4 __attribute__((ext_vector_type(4)));
typedef short bf16x8 __attribute__((ext_vector_type(8)));
typedef unsigned short u16;

constexpr int D = 512, HQ = 32, HKV = 8, SK = 65536, DH = 128, F = 1792;
constexpr int NCU = 256, KEYS_CU = 2048, KEYS_W = 512, TILE = 32, NT_W = KEYS_W / TILE;
constexpr float C2 = 0.08838834764831845f * 1.4426950408889634f;

__device__ __forceinline__ float bf2f(unsigned b) { return __builtin_bit_cast(float, b << 16); }
__device__ __forceinline__ unsigned f2bf(float f) { return (unsigned)__builtin_bit_cast(u16, (__bf16)f); }
__device__ __forceinline__ float rbf(float f) { return bf2f(f2bf(f)); }
__device__ __forceinline__ unsigned pk2(float lo, float hi) { return f2bf(lo) | (f2bf(hi) << 16); }
template <int CTRL> __device__ __forceinline__ float dppf(float x) {
  return __builtin_bit_cast(float, __builtin_amdgcn_mov_dpp(__builtin_bit_cast(int, x), CTRL, 0xf, 0xf, true));
}
__device__ __forceinline__ float xrow_max(float x) {
  auto s = __builtin_amdgcn_permlane16_swap(__float_as_uint(x), __float_as_uint(x), false, false);
  x = fmaxf(__uint_as_float(s[0]), __uint_as_float(s[1]));
  auto t = __builtin_amdgcn_permlane32_swap(__float_as_uint(x), __float_as_uint(x), false, false);
  return fmaxf(__uint_as_float(t[0]), __uint_as_float(t[1]));
}
__device__ __forceinline__ float xrow_sum(float x) {
  auto s = __builtin_amdgcn_permlane16_swap(__float_as_uint(x), __float_as_uint(x), false, false);
  x = __uint_as_float(s[0]) + __uint_as_float(s[1]);
  auto t = __builtin_amdgcn_permlane32_swap(__float_as_uint(x), __float_as_uint(x), false, false);
  return __uint_as_float(t[0]) + __uint_as_float(t[1]);
}
__device__ __forceinline__ float wave_sum(float v) {
#pragma unroll
  for (int o = 32; o >= 1; o >>= 1) v += __shfl_xor(v, o);
  return v;
}

__device__ __forceinline__ void block_ln(const float* xs, float* xn, float* red, int tid) {
  float a = xs[tid], b = xs[tid + 256];
  float s = wave_sum(a + b);
  if ((tid & 63) == 0) red[tid >> 6] = s;
  __syncthreads();
  float mean = rbf((red[0] + red[1] + red[2] + red[3]) * (1.f / 512.f));
  float mf = (red[0] + red[1] + red[2] + red[3]) * (1.f / 512.f);
  float da = a - mf, db = b - mf;
  float v = wave_sum(da * da + db * db);
  __syncthreads();
  if ((tid & 63) == 0) red[tid >> 6] = v;
  __syncthreads();
  float var = rbf((red[0] + red[1] + red[2] + red[3]) * (1.f / 512.f));
  float den = rbf(sqrtf(rbf(var + 1e-5f)));
  xn[tid] = rbf(rbf(a - mean) / den);
  xn[tid + 256] = rbf(rbf(b - mean) / den);
  __syncthreads();
}

__global__ void __launch_bounds__(256) k_qproj(const u16* __restrict__ x, const u16* __restrict__ wq, u16* __restrict__ q) {
  __shared__ float xs[512], xn[512], red[4], part[128][17];
  const int tid = threadIdx.x, c0 = blockIdx.x * 16;
  xs[tid] = bf2f(x[tid]); xs[tid + 256] = bf2f(x[tid + 256]);
  const int half = tid & 1, rg = tid >> 1;
  v4u w[4];
#pragma unroll
  for (int i = 0; i < 4; ++i) w[i] = *(const v4u*)(wq + (size_t)(rg + 128 * i) * 4096 + c0 + half * 8);
  __syncthreads();
  block_ln(xs, xn, red, tid);
  float acc[8] = {0, 0, 0, 0, 0, 0, 0, 0};
#pragma unroll
  for (int i = 0; i < 4; ++i) { float xv = xn[rg + 128 * i];
#pragma unroll
    for (int k = 0; k < 4; ++k) { acc[2 * k] += xv * bf2f(w[i][k] & 0xffff); acc[2 * k + 1] += xv * bf2f(w[i][k] >> 16); } }
#pragma unroll
  for (int k = 0; k < 8; ++k) part[rg][half * 8 + k] = acc[k];
  __syncthreads();
  if (tid < 16) { float s = 0; for (int r = 0; r < 128; ++r) s += part[r][tid]; q[c0 + tid] = (u16)f2bf(s); }
}

template <int AUX, int REV>
__global__ void __launch_bounds__(256, 1) k_attn(const u16* __restrict__ kc, const u16* __restrict__ vc, const u16* __restrict__ q, float* __restrict__ part) {
  __shared__ float sm[4][4], sl[4][4], so[4][4][128];
  const int tid = threadIdx.x, lane = tid & 63, wave = __builtin_amdgcn_readfirstlane(tid >> 6);
  const int kvh = blockIdx.x & 7, split = blockIdx.x >> 3;
  const size_t base = ((size_t)kvh * SK + (size_t)split * KEYS_CU + (size_t)wave * KEYS_W) * DH;
  __amdgpu_buffer_rsrc_t rk = __builtin_amdgcn_make_buffer_rsrc((void*)(kc + base), 0, KEYS_W * 256, 0x00020000);
  __amdgpu_buffer_rsrc_t rv = __builtin_amdgcn_make_buffer_rsrc((void*)(vc + base), 0, KEYS_W * 256, 0x00020000);
  const int g = lane >> 4, a = (lane >> 2) & 3, p = lane & 3;
  const int koff = a * 256 + (4 * g + p) * 16, voff = lane * 16;
  const v4u qf = *(const v4u*)(q + (kvh * 4 + a) * DH + (4 * g + p) * 8);
  f32x4 acc[8];
#pragma unroll
  for (int e = 0; e < 8; ++e) acc[e] = f32x4{0, 0, 0, 0};
  float m = -INFINITY, l = 0.f;
  v4u KA[8], VA[8], KB[8], VB[8];
#define ISSUE(KK, VV, t) do { const int so_ = (REV ? (NT_W - 1 - (t)) : (t)) * 8192; _Pragma("unroll") for (int j = 0; j < 8; ++j) KK[j] = __builtin_amdgcn_raw_buffer_load_b128(rk, koff + j * 1024, so_, AUX); \
    _Pragma("unroll") for (int j = 0; j < 8; ++j) VV[j] = __builtin_amdgcn_raw_buffer_load_b128(rv, voff + j * 1024, so_, AUX); } while (0)
  auto compute = [&](v4u* KK, v4u* VV) {
    float s[8];
#pragma unroll
    for (int j = 0; j < 8; ++j) {
      f32x4 d = __builtin_amdgcn_mfma_f32_16x16x32_bf16(__builtin_bit_cast(__attribute__((ext_vector_type(8))) __bf16, KK[j]), __builtin_bit_cast(__attribute__((ext_vector_type(8))) __bf16, qf), f32x4{0, 0, 0, 0}, 0, 0, 0);
      s[j] = ((dppf<0x00>(d[0]) + dppf<0x55>(d[1])) + (dppf<0xAA>(d[2]) + dppf<0xFF>(d[3]))) * C2;
    }
    float mx = fmaxf(fmaxf(fmaxf(s[0], s[1]), fmaxf(s[2], s[3])), fmaxf(fmaxf(s[4], s[5]), fmaxf(s[6], s[7])));
    mx = xrow_max(mx);
    const float mn = fmaxf(m, mx);
    const float alpha = __builtin_amdgcn_exp2f(m - mn);
    m = mn;
    float ps = 0.f;
#pragma unroll
    for (int j = 0; j < 8; ++j) { s[j] = __builtin_amdgcn_exp2f(s[j] - mn); ps += s[j]; }
    l = l * alpha + ps;
    v4u pf = {pk2(s[0], s[1]), pk2(s[2], s[3]), pk2(s[4], s[5]), pk2(s[6], s[7])};
    const float al = __shfl(alpha, (lane & 48) + 4 * g);
#pragma unroll
    for (int w = 0; w < 4; ++w)
#pragma unroll
      for (int b = 0; b < 2; ++b) {
        v4u vf;
#pragma unroll
        for (int u = 0; u < 4; ++u) vf[u] = __builtin_amdgcn_perm(VV[2 * u + 1][w], VV[2 * u][w], b ? 0x07060302u : 0x05040100u);
        f32x4 c = acc[2 * w + b]; c[0] *= al;
        acc[2 * w + b] = __builtin_amdgcn_mfma_f32_16x16x32_bf16(__builtin_bit_cast(__attribute__((ext_vector_type(8))) __bf16, pf), __builtin_bit_cast(__attribute__((ext_vector_type(8))) __bf16, vf), c, 0, 0, 0);
      }
  };
  ISSUE(KA, VA, 0);
  for (int t = 0; t < NT_W; t += 2) {
    ISSUE(KB, VB, t + 1);
    compute(KA, VA);
    if (t + 2 < NT_W) ISSUE(KA, VA, t + 2);
    compute(KB, VB);
  }
  l = xrow_sum(l);
  if (lane < 16 && p == 0) { sm[wave][a] = m; sl[wave][a] = l; }
#pragma unroll
  for (int e = 0; e < 8; ++e) so[wave][g][(lane & 15) * 8 + e] = acc[e][0];
  __syncthreads();
  {
    const int h = wave;
    float M = fmaxf(fmaxf(sm[0][h], sm[1][h]), fmaxf(sm[2][h], sm[3][h]));
    float L = 0, o0 = 0, o1 = 0;
#pragma unroll
    for (int w = 0; w < 4; ++w) { float wt = __builtin_amdgcn_exp2f(sm[w][h] - M); L += wt * sl[w][h]; o0 += wt * so[w][h][2 * lane]; o1 += wt * so[w][h][2 * lane + 1]; }
    float* dst = part + ((size_t)blockIdx.x * 4 + h) * 132;
    dst[4 + 2 * lane] = o0; dst[4 + 2 * lane + 1] = o1;
    if (lane == 0) { dst[0] = M; dst[1] = L; }
  }
}

__global__ void __launch_bounds__(128) k_combine(const float* __restrict__ part, u16* __restrict__ o) {
  const int h = blockIdx.x, kvh = h >> 2, hh = h & 3, d = threadIdx.x;
  float M = -INFINITY;
  for (int s = 0; s < 32; ++s) M = fmaxf(M, part[((size_t)(s * 8 + kvh) * 4 + hh) * 132]);
  float L = 0, acc = 0;
  for (int s = 0; s < 32; ++s) { const float* pp = part + ((size_t)(s * 8 + kvh) * 4 + hh) * 132; float w = __builtin_amdgcn_exp2f(pp[0] - M); L += w * pp[1]; acc += w * pp[4 + d]; }
  o[h * 128 + d] = (u16)f2bf(acc / L);
}

template <int KD>
__global__ void __launch_bounds__(256) k_proj_res(const u16* __restrict__ v, const u16* __restrict__ W, const u16* __restrict__ xres, u16* __restrict__ y) {
  __shared__ float part[256][9];
  const int tid = threadIdx.x, c0 = blockIdx.x * 8;
  float acc[8] = {0, 0, 0, 0, 0, 0, 0, 0};
#pragma unroll
  for (int i = 0; i < KD / 256; ++i) { const int r = tid + 256 * i; v4u w = *(const v4u*)(W + (size_t)r * 512 + c0); float xv = bf2f(v[r]);
#pragma unroll
    for (int k = 0; k < 4; ++k) { acc[2 * k] += xv * bf2f(w[k] & 0xffff); acc[2 * k + 1] += xv * bf2f(w[k] >> 16); } }
#pragma unroll
  for (int k = 0; k < 8; ++k) { float s = wave_sum(acc[k]); if ((tid & 63) == 0) part[tid >> 6][k] = s; }
  __syncthreads();
  if (tid < 8) { float s = part[0][tid] + part[1][tid] + part[2][tid] + part[3][tid]; y[c0 + tid] = (u16)f2bf(bf2f(xres[c0 + tid]) + rbf(s)); }
}

__global__ void __launch_bounds__(256) k_gateup(const u16* __restrict__ x, const u16* __restrict__ Wg, const u16* __restrict__ Wu, u16* __restrict__ h) {
  __shared__ float xs[512], xn[512], red[4], part[4][16];
  const int tid = threadIdx.x, c0 = blockIdx.x * 8;
  xs[tid] = bf2f(x[tid]); xs[tid + 256] = bf2f(x[tid + 256]);
  v4u wg[2], wu[2];
#pragma unroll
  for (int i = 0; i < 2; ++i) { wg[i] = *(const v4u*)(Wg + (size_t)(tid + 256 * i) * F + c0); wu[i] = *(const v4u*)(Wu + (size_t)(tid + 256 * i) * F + c0); }
  __syncthreads();
  block_ln(xs, xn, red, tid);
  float acc[16];
#pragma unroll
  for (int k = 0; k < 16; ++k) acc[k] = 0;
#pragma unroll
  for (int i = 0; i < 2; ++i) { float xv = xn[tid + 256 * i];
#pragma unroll
    for (int k = 0; k < 4; ++k) { acc[2 * k] += xv * bf2f(wg[i][k] & 0xffff); acc[2 * k + 1] += xv * bf2f(wg[i][k] >> 16); acc[8 + 2 * k] += xv * bf2f(wu[i][k] & 0xffff); acc[8 + 2 * k + 1] += xv * bf2f(wu[i][k] >> 16); } }
#pragma unroll
  for (int k = 0; k < 16; ++k) { float s = wave_sum(acc[k]); if ((tid & 63) == 0) part[tid >> 6][k] = s; }
  __syncthreads();
  if (tid < 8) { float gt = rbf(part[0][tid] + part[1][tid] + part[2][tid] + part[3][tid]); float up = rbf(part[0][8 + tid] + part[1][8 + tid] + part[2][8 + tid] + part[3][8 + tid]);
    gt = gt > 0.f ? gt : gt * 0.f; h[c0 + tid] = (u16)f2bf(gt * up); }
}

template <int AUX>
__global__ void __launch_bounds__(256, 1) k_probe(const v4u* __restrict__ K, const v4u* __restrict__ V, unsigned* sink, int which) {
  const int tid = threadIdx.x, lane = tid & 63, wave = __builtin_amdgcn_readfirstlane(tid >> 6), cu = blockIdx.x;
  constexpr int B = 4, PERW = 128, NTILE = PERW / B;
  const v4u* kb = K + (size_t)cu * 32768 + (size_t)wave * PERW * 64;
  const v4u* vb = V + (size_t)cu * 32768 + (size_t)wave * PERW * 64;
  if (which == 1) { kb = K + (size_t)cu * 32768 + (size_t)wave * PERW * 64; vb = kb + 64 * 64; }
  if (which == 2) { kb = V + (size_t)cu * 32768 + (size_t)wave * PERW * 64; vb = kb + 64 * 64; }
  const int nt = which ? NTILE / 2 : NTILE;
  __amdgpu_buffer_rsrc_t rk = __builtin_amdgcn_make_buffer_rsrc((void*)kb, 0, PERW * 1024, 0x00020000);
  __amdgpu_buffer_rsrc_t rv = __builtin_amdgcn_make_buffer_rsrc((void*)vb, 0, which ? PERW * 512 : PERW * 1024, 0x00020000);
  v4u acc = {0, 0, 0, 0};
  v4u bufA[2 * B], bufB[2 * B];
#define PISSUE(buf, t) do { _Pragma("unroll") for (int j = 0; j < B; ++j) { buf[j] = __builtin_amdgcn_raw_buffer_load_b128(rk, lane * 16 + j * 1024, (t) * B * 1024, AUX); buf[B + j] = __builtin_amdgcn_raw_buffer_load_b128(rv, lane * 16 + j * 1024, (t) * B * 1024, AUX); } } while (0)
  PISSUE(bufA, 0);
  for (int t = 0; t < nt; t += 2) {
    PISSUE(bufB, t + 1);
#pragma unroll
    for (int j = 0; j < 2 * B; ++j) acc ^= bufA[j];
    if (t + 2 < nt) PISSUE(bufA, t + 2);
#pragma unroll
    for (int j = 0; j < 2 * B; ++j) acc ^= bufB[j];
  }
  unsigned s = acc.x ^ acc.y ^ acc.z ^ acc.w;
  sink[cu * 256 + tid] = s;
}
extern "C" void kernel_launch(void* const* d_in, const int* in_sizes, int n_in, void* d_out, int out_size, void* d_ws, size_t ws_size, hipStream_t stream) {
  const u16* x = (const u16*)d_in[0]; const u16* kc = (const u16*)d_in[1]; const u16* vc = (const u16*)d_in[2];
  const u16* wq = (const u16*)d_in[3]; const u16* wo = (const u16*)d_in[4];
  const u16* Wg = (const u16*)d_in[6]; const u16* Wu = (const u16*)d_in[7]; const u16* Wd = (const u16*)d_in[8];
  char* ws = (char*)d_ws;
  u16* q = (u16*)ws; u16* o = (u16*)(ws + 16384); u16* x1 = (u16*)(ws + 32768); u16* h = (u16*)(ws + 36864); u16* x2 = (u16*)(ws + 40960); u16* x3 = (u16*)(ws + 45056);
  float* part = (float*)(ws + 65536);
  k_probe<0><<<256, 256, 0, stream>>>((const v4u*)kc, (const v4u*)vc, (unsigned*)(ws + (1<<20)), 1);
  k_qproj<<<256, 256, 0, stream>>>(x, wq, q);
  k_attn<AUX_P1, 0><<<256, 256, 0, stream>>>(kc, vc, q, part);
  k_combine<<<32, 128, 0, stream>>>(part, o);
  k_proj_res<4096><<<64, 256, 0, stream>>>(o, wo, x, x1);
  k_gateup<<<224, 256, 0, stream>>>(x1, Wg, Wu, h);
  k_proj_res<1792><<<64, 256, 0, stream>>>(h, Wd, x1, x2);
  k_qproj<<<256, 256, 0, stream>>>(x2, wq, q);
  k_attn<AUX_P2, REV2><<<256, 256, 0, stream>>>(kc, vc, q, part);
  k_combine<<<32, 128, 0, stream>>>(part, o);
  k_proj_res<4096><<<64, 256, 0, stream>>>(o, wo, x2, x3);
  k_gateup<<<224, 256, 0, stream>>>(x3, Wg, Wu, h);
  k_proj_res<1792><<<64, 256, 0, stream>>>(h, Wd, x3, (u16*)d_out);
}
```

```cpp
#include <hip/hip_runtime.h>
#include <hip/hip_bf16.h>
#include <stdint.h>

typedef unsigned v4u __attribute__((ext_vector_type(4)));
typedef unsigned v2u __attribute__((ext_vector_type(2)));
typedef float f32x4 __attribute__((ext_vector_type(4)));
typedef float f32x2 __attribute__((ext_vector_type(2)));
typedef __bf16 bf16x8 __attribute__((ext_vector_type(8)));
typedef __bf16 bf16x2 __attribute__((ext_vector_type(2)));
typedef unsigned short u16;
typedef __amdgpu_buffer_rsrc_t rsrc_t;
#define LAS __attribute__((address_space(3)))

constexpr int SK = 65536, FF = 1792;
constexpr float C2 = 0.08838834764831845f * 1.4426950408889634f;
constexpr int AUX_NT = 2, AUX_SC1 = 16;
constexpr int SPIN_CAP = 1 << 17;

constexpr int WS_EPOCH = 0;
constexpr int WS_SH = 1 << 19;
constexpr int WS_Q = 4096, Q_L = 16384;
constexpr int WS_ML = WS_Q + 2 * Q_L, ML_L = 256 * 128;
constexpr int WS_X1 = WS_ML + 2 * ML_L, X_L = 4096;
constexpr int WS_X2 = WS_X1 + 2 * X_L;
constexpr int WS_PART = 1 << 20, BIG_L = 1 << 20;
constexpr int WS_Y1 = WS_PART + 2 * BIG_L;
constexpr int WS_Y2 = WS_Y1 + 2 * BIG_L;
constexpr int WS_END = WS_Y2 + 2 * BIG_L;

constexpr int LDS_VW = 38 * 1024;
constexpr int LDS_SCR = 4 * LDS_VW;
constexpr int LDS_BYTES = LDS_SCR + 8192;

constexpr int CODE_LINES = 402;
constexpr int DLW = 242;
constexpr int DLS = 248;
constexpr int T0OFF = 90;
constexpr int DL1 = 230;
constexpr int NEWL = 0;
constexpr int DLL = 200;
constexpr int DL2 = 230;
constexpr int TPOLL2 = 195;
constexpr int DELAY_A = 15;
constexpr int DELAY_Q = 11;
constexpr int WS_WARM = 1;
constexpr int TPOLL = 180;
constexpr int DELAY_G = 10;
constexpr int RETRY_SLEEP = 2;
constexpr int CPF_MASK = 3;
constexpr int REUSE = 0;

#define LOFF(l) (REUSE ? 0 : (l))

__device__ __forceinline__ float bf2f(unsigned b) { return __builtin_bit_cast(float, b << 16); }
__device__ __forceinline__ float bflo(unsigned w) { return __builtin_bit_cast(float, w << 16); }
__device__ __forceinline__ float bfhi(unsigned w) { return __builtin_bit_cast(float, w & 0xffff0000u); }
__device__ __forceinline__ unsigned pk2(float lo, float hi) { bf16x2 v = {(__bf16)lo, (__bf16)hi}; return __builtin_bit_cast(unsigned, v); }
__device__ __forceinline__ float rbf(float f) { return (float)(__bf16)f; }
__device__ __forceinline__ float asf(unsigned u) { return __builtin_bit_cast(float, u); }
__device__ __forceinline__ unsigned asu(float f) { return __builtin_bit_cast(unsigned, f); }
template <int CTRL> __device__ __forceinline__ float dppf(float x) {
  return __builtin_bit_cast(float, __builtin_amdgcn_mov_dpp(__builtin_bit_cast(int, x), CTRL, 0xf, 0xf, true));
}
constexpr int XOR1 = 0xB1, XOR2 = 0x4E, XOR7 = 0x141, XOR8 = 0x128, ROR4 = 0x124;
__device__ __forceinline__ float x16_max(float x) { auto s = __builtin_amdgcn_permlane16_swap(asu(x), asu(x), false, false); return fmaxf(asf(s[0]), asf(s[1])); }
__device__ __forceinline__ float x32_max(float x) { auto s = __builtin_amdgcn_permlane32_swap(asu(x), asu(x), false, false); return fmaxf(asf(s[0]), asf(s[1])); }
__device__ __forceinline__ float x16_sum(float x) { auto s = __builtin_amdgcn_permlane16_swap(asu(x), asu(x), false, false); return asf(s[0]) + asf(s[1]); }
__device__ __forceinline__ float x32_sum(float x) { auto s = __builtin_amdgcn_permlane32_swap(asu(x), asu(x), false, false); return asf(s[0]) + asf(s[1]); }
__device__ __forceinline__ float row_sum16(float x) { x += dppf<XOR1>(x); x += dppf<XOR2>(x); x += dppf<XOR7>(x); x += dppf<XOR8>(x); return x; }
__device__ __forceinline__ float wave_sum(float x) { return x32_sum(x16_sum(row_sum16(x))); }
__device__ __forceinline__ void wave_sum2(float& a, float& b, int lane) {
  const float ta = a + dppf<XOR1>(a), tb = b + dppf<XOR1>(b);
  float x = (lane & 1) ? tb : ta;
  x += dppf<XOR2>(x); x += dppf<0x124>(x); x += dppf<XOR8>(x);
  x = x32_sum(x16_sum(x));
  a = asf(__builtin_amdgcn_readlane(asu(x), 0)); b = asf(__builtin_amdgcn_readlane(asu(x), 1));
}
__device__ __forceinline__ f32x4 mfma(v4u a, v4u b, f32x4 c) {
  return __builtin_amdgcn_mfma_f32_16x16x32_bf16(__builtin_bit_cast(bf16x8, a), __builtin_bit_cast(bf16x8, b), c, 0, 0, 0);
}
__device__ __forceinline__ float dot2(unsigned a, unsigned b, float c) {
  return __builtin_amdgcn_fdot2_f32_bf16(__builtin_bit_cast(bf16x2, a), __builtin_bit_cast(bf16x2, b), c, false);
}
__device__ __forceinline__ unsigned plo(unsigned b, unsigned a) { return __builtin_amdgcn_perm(b, a, 0x05040100u); }
__device__ __forceinline__ unsigned phi(unsigned b, unsigned a) { return __builtin_amdgcn_perm(b, a, 0x07060302u); }
#define SB() __builtin_amdgcn_sched_barrier(0)
#define CLOB() asm volatile("" ::: "memory")
template <int N> __device__ __forceinline__ void nap() {
#pragma unroll
  for (int i = 0; i < N; ++i) __builtin_amdgcn_s_sleep(1);
}
__device__ __forceinline__ v4u ldg(rsrc_t r, int off) { return __builtin_amdgcn_raw_buffer_load_b128(r, off, 0, AUX_SC1); }
__device__ __forceinline__ void stg(rsrc_t r, int off, unsigned a, unsigned b, unsigned tag) { __builtin_amdgcn_raw_buffer_store_b128(v4u{a, tag, b, tag}, r, off, 0, AUX_SC1); }
__device__ __forceinline__ bool tag_ok(v4u x, unsigned tag) { return x[1] == tag && x[3] == tag; }
#define GLD(k) "buffer_load_dwordx4 %[x" #k "], %[o" #k "], %[r], 0 offen sc1\n\t"
#define GHEAD "s_mov_b64 %[ex], exec\n\ts_mov_b32 %[cnt], 0\n\t.rept %c[nap]\n\ts_sleep 1\n\t.endr\n\t1:\n\t"
#define GTAIL "v_cmp_ne_u32 vcc, %[tag], %[mn]\n\tv_cmp_ne_u32 %[s2], %[tag], %[mx]\n\ts_or_b64 vcc, vcc, %[s2]\n\ts_cbranch_vccz 2f\n\t" \
              "s_add_u32 %[cnt], %[cnt], 1\n\ts_cmp_gt_u32 %[cnt], %[cap]\n\ts_cbranch_scc1 2f\n\ts_mov_b64 exec, vcc\n\ts_sleep %c[rs]\n\ts_branch 1b\n\t2:\n\ts_mov_b64 exec, %[ex]\n\t"
#define GTMP [mn] "=&v"(mn), [mx] "=&v"(mx), [ex] "=&s"(ex), [s2] "=&s"(s2), [cnt] "=&s"(cnt)
#define GIN [r] "s"(r), [tag] "s"(tag), [cap] "s"(SPIN_CAP), [nap] "i"(NAP), [rs] "i"(RETRY_SLEEP)
struct Rec { unsigned a, t0, b, t1; };
template <int NAP> __device__ __forceinline__ void gather2(rsrc_t r, const int (&offs)[2], unsigned tag, v4u (&x)[2]) {
  unsigned mn, mx, cnt; unsigned long long ex, s2;
  asm volatile(GHEAD GLD(0) GLD(1) "s_waitcnt vmcnt(0)\n\t"
      "v_min3_u32 %[mn], v63, v65, v67\n\tv_max3_u32 %[mx], v63, v65, v67\n\tv_min3_u32 %[mn], %[mn], v69, v69\n\tv_max3_u32 %[mx], %[mx], v69, v69\n\t" GTAIL
      : [x0] "=&{v[62:65]}"(x[0]), [x1] "=&{v[66:69]}"(x[1]), GTMP
      : [o0] "v"(offs[0]), [o1] "v"(offs[1]), GIN
      : "memory", "vcc", "scc");
}
template <int NAP> __device__ __forceinline__ void gather4(rsrc_t r, const int (&offs)[4], unsigned tag, v4u (&x)[4]) {
  unsigned mn, mx, cnt; unsigned long long ex, s2;
  asm volatile(GHEAD GLD(0) GLD(1) GLD(2) GLD(3) "s_waitcnt vmcnt(0)\n\t"
      "v_min3_u32 %[mn], v55, v57, v59\n\tv_max3_u32 %[mx], v55, v57, v59\n\tv_min3_u32 %[mn], %[mn], v61, v63\n\tv_max3_u32 %[mx], %[mx], v61, v63\n\tv_min3_u32 %[mn], %[mn], v65, v67\n\tv_max3_u32 %[mx], %[mx], v65, v67\n\tv_min3_u32 %[mn], %[mn], v69, v69\n\tv_max3_u32 %[mx], %[mx], v69, v69\n\t" GTAIL
      : [x0] "=&{v[54:57]}"(x[0]), [x1] "=&{v[58:61]}"(x[1]), [x2] "=&{v[62:65]}"(x[2]), [x3] "=&{v[66:69]}"(x[3]), GTMP
      : [o0] "v"(offs[0]), [o1] "v"(offs[1]), [o2] "v"(offs[2]), [o3] "v"(offs[3]), GIN
      : "memory", "vcc", "scc");
}
template <int NAP> __device__ __forceinline__ void gather8(rsrc_t r, const int (&offs)[8], unsigned tag, v4u (&x)[8]) {
  unsigned mn, mx, cnt; unsigned long long ex, s2;
  asm volatile(GHEAD GLD(0) GLD(1) GLD(2) GLD(3) GLD(4) GLD(5) GLD(6) GLD(7) "s_waitcnt vmcnt(0)\n\t"
      "v_min3_u32 %[mn], v37, v39, v41\n\tv_max3_u32 %[mx], v37, v39, v41\n\tv_min3_u32 %[mn], %[mn], v43, v45\n\tv_max3_u32 %[mx], %[mx], v43, v45\n\tv_min3_u32 %[mn], %[mn], v47, v49\n\tv_max3_u32 %[mx], %[mx], v47, v49\n\tv_min3_u32 %[mn], %[mn], v51, v53\n\tv_max3_u32 %[mx], %[mx], v51, v53\n\tv_min3_u32 %[mn], %[mn], v55, v57\n\tv_max3_u32 %[mx], %[mx], v55, v57\n\tv_min3_u32 %[mn], %[mn], v59, v61\n\tv_max3_u32 %[mx], %[mx], v59, v61\n\tv_min3_u32 %[mn], %[mn], v63, v65\n\tv_max3_u32 %[mx], %[mx], v63, v65\n\tv_min3_u32 %[mn], %[mn], v67, v67\n\tv_max3_u32 %[mx], %[mx], v67, v67\n\t" GTAIL
      : [x0] "=&{v[36:39]}"(x[0]), [x1] "=&{v[40:43]}"(x[1]), [x2] "=&{v[44:47]}"(x[2]), [x3] "=&{v[48:51]}"(x[3]), [x4] "=&{v[52:55]}"(x[4]), [x5] "=&{v[56:59]}"(x[5]), [x6] "=&{v[60:63]}"(x[6]), [x7] "=&{v[64:67]}"(x[7]), GTMP
      : [o0] "v"(offs[0]), [o1] "v"(offs[1]), [o2] "v"(offs[2]), [o3] "v"(offs[3]), [o4] "v"(offs[4]), [o5] "v"(offs[5]), [o6] "v"(offs[6]), [o7] "v"(offs[7]), GIN
      : "memory", "vcc", "scc");
}

struct Att { f32x4 a0, a1; float m, l; };

struct Ctx {
  rsrc_t ws, rwq, rwo, rwg, rwu, rwd, rout, rkc, rvc;
  int cu, tid, lane, wave, cp;
  unsigned tag;
  LAS unsigned char* lds;
};

#define BAR() asm volatile("s_waitcnt lgkmcnt(0)\n\ts_barrier" ::: "memory")
__device__ __forceinline__ float wave_reduce16(float (&v)[16], int l) {
#define TR(H, CTRL, BIT) _Pragma("unroll") for (int h = 0; h < H; ++h) { const float t1 = v[h] + dppf<CTRL>(v[h]), t2 = v[h + H] + dppf<CTRL>(v[h + H]); v[h] = (l & BIT) ? t2 : t1; }
  TR(8, XOR8, 8) TR(4, XOR7, 4) TR(2, XOR2, 2) TR(1, XOR1, 1)
#undef TR
  return x32_sum(x16_sum(v[0]));
}

__device__ __forceinline__ int qcol(int l) { return 8 * (l & 1) + ((l >> 1) & 4) + (l & 2) + ((l >> 2) & 1); }
__device__ __forceinline__ float wave_reduce8(float (&v)[8], int l) {
#define TR(H, CTRL, BIT) _Pragma("unroll") for (int h = 0; h < H; ++h) { const float t1 = v[h] + dppf<CTRL>(v[h]), t2 = v[h + H] + dppf<CTRL>(v[h + H]); v[h] = (l & BIT) ? t2 : t1; }
  TR(4, XOR8, 8) TR(2, XOR2, 2)
#undef TR
  const float t1 = v[0] + dppf<XOR7>(dppf<0x1B>(v[0])), t2 = v[1] + dppf<XOR7>(dppf<0x1B>(v[1]));
  return x32_sum(x16_sum((l & 4) ? t2 : t1));
}

__device__ __forceinline__ void ln_stats(const unsigned (&xp)[4], int lane, float& mean, float& rden) {
  const float sh = bflo(__builtin_amdgcn_readlane(xp[0], 0));
  float s = 0.f, q = 0.f;
#pragma unroll
  for (int i = 0; i < 4; ++i) { const float a = bflo(xp[i]) - sh, b = bfhi(xp[i]) - sh; s += a + b; q = fmaf(a, a, fmaf(b, b, q)); }
  wave_sum2(s, q, lane);
  const float d = s * (1.f / 512.f);
  const float var = rbf(fmaxf(fmaf(-d, d, q * (1.f / 512.f)), 0.f));
  mean = rbf(sh + d);
  rden = __builtin_amdgcn_rcpf(rbf(__builtin_amdgcn_sqrtf(rbf(var + 1e-5f))));
}
__device__ __forceinline__ unsigned ln_apply(unsigned me, float mean, float rden) { return pk2(rbf(bflo(me) - mean) * rden, rbf(bfhi(me) - mean) * rden); }

template <int ST> __device__ __forceinline__ void gather_ln(const Ctx& c, int base) {
  LAS unsigned* xs = (LAS unsigned*)(c.lds + LDS_SCR + 1024);
  int offs[4];
#pragma unroll
  for (int i = 0; i < 4; ++i) offs[i] = base + (c.lane + 64 * i) * 16;
  v4u x[4];
  gather4<DELAY_A>(c.ws, offs, c.tag, x);
  const unsigned xp[4] = {x[0][0], x[1][0], x[2][0], x[3][0]};
#pragma unroll
  for (int i = 0; i < 4; ++i) xs[c.lane + 64 * i] = xp[i];
  float mean, rden;
  ln_stats(xp, c.lane, mean, rden);
  if (c.lane == 0) *(LAS f32x2*)(xs + 256) = f32x2{mean, rden};
}

struct W16 { v4u a0, a1, b0, b1; };
struct P16 { unsigned p[16]; };
__device__ __forceinline__ P16 pair_up(const W16& w) {
  P16 r;
#pragma unroll
  for (int k = 0; k < 4; ++k) {
    r.p[2 * k] = plo(w.b0[k], w.a0[k]); r.p[2 * k + 1] = phi(w.b0[k], w.a0[k]);
    r.p[8 + 2 * k] = plo(w.b1[k], w.a1[k]); r.p[8 + 2 * k + 1] = phi(w.b1[k], w.a1[k]);
  }
  return r;
}
__device__ __forceinline__ int ffn_slot(const Ctx& c) { return (c.cu >> 3) < 28 ? (c.cu & 7) * 28 + (c.cu >> 3) : 0; }
__device__ __forceinline__ void publish_q(const Ctx& c, float r, int layer) {
  const unsigned pr = pk2(r, dppf<XOR1>(r));
  const unsigned nx = asu(dppf<XOR2>(asf(pr)));
  if (c.lane < 16 && (c.lane & 3) == 0) stg(c.ws, WS_Q + layer * Q_L + (c.cu & 7) * 2048 + (c.cu >> 3) * 64 + (c.lane >> 2) * 16, pr, nx, c.tag);
}
__device__ __forceinline__ int q_off(const Ctx& c, int layer) {
  const int g = c.lane >> 4, h = (c.lane >> 2) & 3, p = c.lane & 3;
  return WS_Q + layer * Q_L + (c.cu & 7) * 2048 + (h * 16 + 4 * g + p) * 32;
}
__device__ __forceinline__ int wo_off(const Ctx& c) {
  const int sp = c.cu >> 3, hh = sp >> 3, sub = sp & 7, wq_ = sub >> 1;
  const int row0 = ((c.cu & 7) * 4 + hh) * 128 + 64 * (sub & 1) + 16 * (wq_ & 1) + 4 * (wq_ >> 1);
  return row0 * 1024 + c.tid * 4;
}
#define GEMV16_OUT(v) "=&v"(v[0]), "=&v"(v[1]), "=&v"(v[2]), "=&v"(v[3]), "=&v"(v[4]), "=&v"(v[5]), "=&v"(v[6]), "=&v"(v[7]), \
                      "=&v"(v[8]), "=&v"(v[9]), "=&v"(v[10]), "=&v"(v[11]), "=&v"(v[12]), "=&v"(v[13]), "=&v"(v[14]), "=&v"(v[15])
template <int WHICH> __device__ __forceinline__ float gemv_block(const Ctx& c) {
  const LAS unsigned* xs = (const LAS unsigned*)(c.lds + LDS_SCR + 1024);
  const f32x2 mr = *(const LAS f32x2*)(xs + 256);
  LAS float* red = (LAS float*)(c.lds + LDS_SCR);
  if (WHICH == 0) {
    const unsigned xn = ln_apply(xs[c.tid], mr[0], mr[1]);
    float v[16];
    asm volatile("v_dot2_f32_bf16 %0, v88, %16, 0\n\tv_dot2_f32_bf16 %1, v92, %16, 0\n\tv_dot2_f32_bf16 %2, v89, %16, 0\n\tv_dot2_f32_bf16 %3, v93, %16, 0\n\tv_dot2_f32_bf16 %4, v90, %16, 0\n\tv_dot2_f32_bf16 %5, v94, %16, 0\n\tv_dot2_f32_bf16 %6, v91, %16, 0\n\tv_dot2_f32_bf16 %7, v95, %16, 0\n\tv_dot2_f32_bf16 %8, v96, %16, 0\n\tv_dot2_f32_bf16 %9, v100, %16, 0\n\tv_dot2_f32_bf16 %10, v97, %16, 0\n\tv_dot2_f32_bf16 %11, v101, %16, 0\n\tv_dot2_f32_bf16 %12, v98, %16, 0\n\tv_dot2_f32_bf16 %13, v102, %16, 0\n\tv_dot2_f32_bf16 %14, v99, %16, 0\n\tv_dot2_f32_bf16 %15, v103, %16, 0\n\ts_nop 2" : GEMV16_OUT(v) : "v"(xn));
    const float r = wave_reduce16(v, c.lane);
    if (c.lane < 16) red[c.wave * 16 + c.lane] = r;
  } else {
    const v2u xr = *(const LAS v2u*)(xs + (c.tid & ~1));
    const unsigned x0 = ln_apply(xr[0], mr[0], mr[1]), x1 = ln_apply(xr[1], mr[0], mr[1]);
    float v[8];
    asm volatile("v_dot2_f32_bf16 %0, v104, %8, 0\n\tv_dot2_f32_bf16 %1, v105, %8, 0\n\tv_dot2_f32_bf16 %2, v106, %8, 0\n\tv_dot2_f32_bf16 %3, v107, %8, 0\n\tv_dot2_f32_bf16 %4, v108, %8, 0\n\tv_dot2_f32_bf16 %5, v109, %8, 0\n\tv_dot2_f32_bf16 %6, v110, %8, 0\n\tv_dot2_f32_bf16 %7, v111, %8, 0\n\tv_dot2_f32_bf16 %0, v112, %9, %0\n\tv_dot2_f32_bf16 %1, v113, %9, %1\n\tv_dot2_f32_bf16 %2, v114, %9, %2\n\tv_dot2_f32_bf16 %3, v115, %9, %3\n\tv_dot2_f32_bf16 %4, v116, %9, %4\n\tv_dot2_f32_bf16 %5, v117, %9, %5\n\tv_dot2_f32_bf16 %6, v118, %9, %6\n\tv_dot2_f32_bf16 %7, v119, %9, %7\n\ts_nop 2" : "=&v"(v[0]), "=&v"(v[1]), "=&v"(v[2]), "=&v"(v[3]), "=&v"(v[4]), "=&v"(v[5]), "=&v"(v[6]), "=&v"(v[7]) : "v"(x0), "v"(x1));
    const float r = wave_reduce8(v, c.lane);
    if (c.lane < 16) red[c.wave * 16 + qcol(c.lane)] = r;
  }
  BAR();
  const int i = c.lane & 15;
  return (red[i] + red[16 + i]) + (red[32 + i] + red[48 + i]);
}

__device__ __forceinline__ void lp_touch(const Ctx& c, int n) {
  const int off = (n >> 2) * 131072 + (12 + (n & 1)) * 8192 + c.lane * 128;
  if (n & 2) asm volatile("buffer_load_dword v71, %0, %1, 0 offen" :: "v"(off), "s"(c.rvc) : "memory", "v71");
  else asm volatile("buffer_load_dword v71, %0, %1, 0 offen" :: "v"(off), "s"(c.rkc) : "memory", "v71");
}
template <int STEP> __device__ __forceinline__ void lp_step(const Ctx& c) {
}

template <int LAYER>
__device__ __forceinline__ void middle(const Ctx& c, const Att& st, unsigned& xres, u16* out, unsigned tpoll) {
  const int lane = c.lane, wave = c.wave, tid = c.tid;
  LAS unsigned* bc = (LAS unsigned*)(c.lds + LDS_SCR + 4096);
  {
    const float l = x32_sum(x16_sum(st.l));
    LAS f32x2* so = (LAS f32x2*)(c.lds + LDS_SCR);
    const f32x2 pr[4] = {f32x2{st.a0[0], st.a0[1]}, f32x2{st.a0[2], st.a0[3]}, f32x2{st.a1[0], st.a1[1]}, f32x2{st.a1[2], st.a1[3]}};
    const int wv = tid >> 6;
    const f32x2 m01 = (wv & 1) ? pr[1] : pr[0], m23 = (wv & 1) ? pr[3] : pr[2];
    const f32x2 mine = (wv & 2) ? m23 : m01;
#pragma unroll
    for (int k = 0; k < 4; ++k) so[(wave * 4 + k) * 64 + lane] = (k == wv) ? f32x2{st.m, l} : pr[k];
    BAR();
    f32x2 a[4], ml[4];
#pragma unroll
    for (int w = 0; w < 4; ++w) { a[w] = so[(w * 4 + wave) * 64 + lane]; ml[w] = so[(w * 4 + w) * 64 + lane]; }
    const float M = fmaxf(fmaxf(ml[0][0], ml[1][0]), fmaxf(ml[2][0], ml[3][0]));
    float L = 0.f, o0 = 0.f, o1 = 0.f;
#pragma unroll
    for (int w = 0; w < 4; ++w) {
      const f32x2 av = (w == wv) ? mine : a[w];
      const float wt = __builtin_amdgcn_exp2f((ml[w][0] - M) * C2);
      L = fmaf(wt, ml[w][1], L); o0 = fmaf(wt, av[0], o0); o1 = fmaf(wt, av[1], o1);
    }
    const int G = lane >> 4, h = (lane >> 2) & 3, p = lane & 3;
    const int rec = 64 * h + 16 * wave + 4 * G + p;
    stg(c.ws, WS_PART + LOFF(LAYER) * BIG_L + (NEWL ? (((c.cu & 7) * 32 + (rec >> 3)) * 32 + (c.cu >> 3)) * 128 + (rec & 7) * 16 : c.cu * 4096 + rec * 16), asu(o0), asu(o1), c.tag);
    if (tid < 16 && p == 0) stg(c.ws, WS_ML + LOFF(LAYER) * ML_L + (NEWL ? c.cp : c.cu) * 128 + h * 16, asu(M), asu(L), c.tag);
  }
  if (wave == 0) {
    const int sp = c.cu >> 3, kv = c.cu & 7;
    int offs[8];
#pragma unroll
    for (int i = 0; i < 4; ++i) {
      const int pc = (8 * i + (lane >> 3)) * 8 + kv;
      offs[i] = WS_PART + LOFF(LAYER) * BIG_L + (NEWL ? c.cp * 4096 + i * 1024 + lane * 16 : pc * 4096 + sp * 128 + (lane & 7) * 16);
      offs[4 + i] = WS_ML + LOFF(LAYER) * ML_L + (NEWL ? kv * 32 + 8 * i + (lane >> 3) : pc) * 128 + (sp >> 3) * 16;
    }
    asm volatile("" : "+v"(offs[0]), "+v"(offs[1]), "+v"(offs[2]), "+v"(offs[3]), "+v"(offs[4]), "+v"(offs[5]), "+v"(offs[6]), "+v"(offs[7]));
    nap<DELAY_G>();
    while ((int)((unsigned)__builtin_amdgcn_s_memrealtime() - tpoll) < 0) __builtin_amdgcn_s_sleep(1);
    v4u x[8];
    gather8<0>(c.ws, offs, c.tag, x);
    float M = fmaxf(fmaxf(asf(x[4][0]), asf(x[5][0])), fmaxf(asf(x[6][0]), asf(x[7][0])));
    M = fmaxf(M, dppf<XOR8>(M)); M = x32_max(x16_max(M));
    float L = 0.f, o0 = 0.f, o1 = 0.f;
#pragma unroll
    for (int i = 0; i < 4; ++i) { const float wt = __builtin_amdgcn_exp2f((asf(x[4 + i][0]) - M) * C2); L = fmaf(wt, asf(x[4 + i][2]), L); o0 = fmaf(wt, asf(x[i][0]), o0); o1 = fmaf(wt, asf(x[i][2]), o1); }
    L += dppf<XOR8>(L); o0 += dppf<XOR8>(o0); o1 += dppf<XOR8>(o1);
    L = x32_sum(x16_sum(L)); o0 = x32_sum(x16_sum(o0)); o1 = x32_sum(x16_sum(o1));
    const float rl = __builtin_amdgcn_rcpf(L);
    if (lane < 8) bc[lane] = pk2(o0 * rl, o1 * rl);
  }
  BAR();
  {
    const v4u oa = *(LAS v4u*)bc, ob = *(LAS v4u*)(bc + 4);
    float y0, y1, y2, y3;
    asm volatile("v_dot2_f32_bf16 %0, v72, %4, 0\n\tv_dot2_f32_bf16 %1, v73, %4, 0\n\tv_dot2_f32_bf16 %0, v74, %5, %0\n\tv_dot2_f32_bf16 %1, v75, %5, %1\n\tv_dot2_f32_bf16 %0, v76, %6, %0\n\tv_dot2_f32_bf16 %1, v77, %6, %1\n\tv_dot2_f32_bf16 %0, v78, %7, %0\n\tv_dot2_f32_bf16 %1, v79, %7, %1\n\tv_dot2_f32_bf16 %2, v80, %8, 0\n\tv_dot2_f32_bf16 %3, v81, %8, 0\n\tv_dot2_f32_bf16 %2, v82, %9, %2\n\tv_dot2_f32_bf16 %3, v83, %9, %3\n\tv_dot2_f32_bf16 %2, v84, %10, %2\n\tv_dot2_f32_bf16 %3, v85, %10, %3\n\tv_dot2_f32_bf16 %2, v86, %11, %2\n\tv_dot2_f32_bf16 %3, v87, %11, %3\n\ts_nop 2" : "=&v"(y0), "=&v"(y1), "=&v"(y2), "=&v"(y3)
                 : "v"(oa[0]), "v"(oa[1]), "v"(oa[2]), "v"(oa[3]), "v"(ob[0]), "v"(ob[1]), "v"(ob[2]), "v"(ob[3]));
    stg(c.ws, WS_Y1 + LOFF(LAYER) * BIG_L + (NEWL ? (tid >> 3) * 32768 + c.cu * 128 + (tid & 7) * 16 : c.cu * 4096 + tid * 16), asu(y0 + y2), asu(y1 + y3), c.tag);
  }
  if (LAYER == 0) lp_step<0>(c);
  if (wave == 0) {
    int offs[4];
#pragma unroll
    for (int i = 0; i < 4; ++i) offs[i] = WS_Y1 + LOFF(LAYER) * BIG_L + (NEWL ? (c.cp >> 3) * 32768 + (lane + 64 * i) * 128 + (c.cp & 7) * 16 : (lane + 64 * i) * 4096 + c.cp * 16);
    v4u x[4];
    gather4<DELAY_A>(c.ws, offs, c.tag, x);
    float s0 = (asf(x[0][0]) + asf(x[1][0])) + (asf(x[2][0]) + asf(x[3][0]));
    float s1 = (asf(x[0][2]) + asf(x[1][2])) + (asf(x[2][2]) + asf(x[3][2]));
    wave_sum2(s0, s1, lane);
    xres = __builtin_amdgcn_readfirstlane(pk2(bflo(xres) + rbf(s0), bfhi(xres) + rbf(s1)));
    if (lane == 0) stg(c.ws, WS_X1 + LOFF(LAYER) * X_L + c.cp * 16, xres, xres, c.tag);
    gather_ln<10 + 14 * LAYER>(c, WS_X1 + LOFF(LAYER) * X_L);
  }
  BAR();
  {
    const float r = rbf(gemv_block<0>(c));
    const float up = dppf<XOR8>(r);
    const float gt = r > 0.f ? r : r * 0.f;
    const float hv = gt * up;
    const unsigned hp = pk2(hv, dppf<XOR1>(hv));
    const unsigned h0 = __builtin_amdgcn_readlane(hp, 0), h1 = __builtin_amdgcn_readlane(hp, 2), h2 = __builtin_amdgcn_readlane(hp, 4), h3 = __builtin_amdgcn_readlane(hp, 6);
    float y0, y1;
    asm volatile("s_nop 1\n\tv_dot2_f32_bf16 %0, v120, %2, 0\n\tv_dot2_f32_bf16 %1, v121, %2, 0\n\tv_dot2_f32_bf16 %0, v122, %3, %0\n\tv_dot2_f32_bf16 %1, v123, %3, %1\n\tv_dot2_f32_bf16 %0, v124, %4, %0\n\tv_dot2_f32_bf16 %1, v125, %4, %1\n\tv_dot2_f32_bf16 %0, v126, %5, %0\n\tv_dot2_f32_bf16 %1, v127, %5, %1\n\ts_nop 2" : "=&v"(y0), "=&v"(y1) : "s"(h0), "s"(h1), "s"(h2), "s"(h3));
    if ((c.cu >> 3) < 28) stg(c.ws, WS_Y2 + LOFF(LAYER) * BIG_L + (NEWL ? (tid >> 3) * 32768 + ffn_slot(c) * 128 + (tid & 7) * 16 : ffn_slot(c) * 4096 + tid * 16), asu(y0), asu(y1), c.tag);
  }
  if (LAYER == 0) lp_step<1>(c);
  if (wave == 0) {
    int offs[4];
#pragma unroll
    for (int i = 0; i < 4; ++i) { const int pr = lane + 64 * i; offs[i] = WS_Y2 + LOFF(LAYER) * BIG_L + (NEWL ? (c.cp >> 3) * 32768 + (pr < 224 ? pr : lane) * 128 + (c.cp & 7) * 16 : (pr < 224 ? pr : lane) * 4096 + c.cp * 16); }
    v4u x[4];
    gather4<DELAY_A>(c.ws, offs, c.tag, x);
    const float e0 = lane < 32 ? asf(x[3][0]) : 0.f, e1 = lane < 32 ? asf(x[3][2]) : 0.f;
    float s0 = (asf(x[0][0]) + asf(x[1][0])) + (asf(x[2][0]) + e0);
    float s1 = (asf(x[0][2]) + asf(x[1][2])) + (asf(x[2][2]) + e1);
    wave_sum2(s0, s1, lane);
    xres = __builtin_amdgcn_readfirstlane(pk2(bflo(xres) + rbf(s0), bfhi(xres) + rbf(s1)));
    if (lane == 0) {
      if (LAYER == 0) stg(c.ws, WS_X2 + c.cp * 16, xres, xres, c.tag);
      else __builtin_amdgcn_raw_buffer_store_b32(xres, c.rout, c.cp * 4, 0, 0);
    }
    if (LAYER == 0) {
      gather_ln<14>(c, WS_X2);
      if (c.cu == 0 && lane == 0) __builtin_amdgcn_raw_buffer_store_b32(c.tag, c.ws, WS_EPOCH, 0, AUX_SC1);
    }
  }
  if (LAYER == 0) {
    BAR();
    const float r = gemv_block<1>(c);
    if (wave == 0) publish_q(c, r, 1);
    BAR();
  }
}

__global__ void __launch_bounds__(256, 1) __attribute__((amdgpu_num_vgpr(70))) fused_kernel(const u16* __restrict__ x, const u16* __restrict__ kc, const u16* __restrict__ vc,
    const u16* __restrict__ wq, const u16* __restrict__ wo, const u16* __restrict__ wg, const u16* __restrict__ wu, const u16* __restrict__ wd,
    u16* out, unsigned char* __restrict__ ws) {
  extern __shared__ __attribute__((aligned(16))) unsigned char smem[];
  Ctx c;
  c.lds = (LAS unsigned char*)smem;
  c.tid = threadIdx.x; c.lane = c.tid & 63; c.wave = __builtin_amdgcn_readfirstlane(c.tid >> 6); c.cu = blockIdx.x;
  c.cp = (c.cu & 7) * 32 + (c.cu >> 3);
  c.ws = __builtin_amdgcn_make_buffer_rsrc(ws, 0, WS_END, 0x00020000);
  c.rwq = __builtin_amdgcn_make_buffer_rsrc((void*)wq, 0, 512 * 4096 * 2, 0x00020000);
  c.rwo = __builtin_amdgcn_make_buffer_rsrc((void*)wo, 0, 4096 * 512 * 2, 0x00020000);
  c.rwg = __builtin_amdgcn_make_buffer_rsrc((void*)wg, 0, 512 * FF * 2, 0x00020000);
  c.rwu = __builtin_amdgcn_make_buffer_rsrc((void*)wu, 0, 512 * FF * 2, 0x00020000);
  c.rwd = __builtin_amdgcn_make_buffer_rsrc((void*)wd, 0, FF * 512 * 2, 0x00020000);
  asm volatile("" : "+s"(out));
  c.rout = __builtin_amdgcn_make_buffer_rsrc((void*)out, 0, 1024, 0x00020000);
  const int lane = c.lane, wave = c.wave;
  const int wid = (c.cu >> 3) * 4 + wave;
  const size_t kvbase = ((size_t)(c.cu & 7) * SK + (size_t)wid * 512) * 128;
  const rsrc_t rk = __builtin_amdgcn_make_buffer_rsrc((void*)(kc + kvbase), 0, 512 * 256, 0x00020000);
  const rsrc_t rv = __builtin_amdgcn_make_buffer_rsrc((void*)(vc + kvbase), 0, 512 * 256, 0x00020000);
  c.rkc = __builtin_amdgcn_make_buffer_rsrc((void*)(kc + ((size_t)(c.cu & 7) * SK + (size_t)(c.cu >> 3) * 2048) * 128), 0, 2048 * 256, 0x00020000);
  c.rvc = __builtin_amdgcn_make_buffer_rsrc((void*)(vc + ((size_t)(c.cu & 7) * SK + (size_t)(c.cu >> 3) * 2048) * 128), 0, 2048 * 256, 0x00020000);
  const unsigned rot = 0;
  const unsigned stg16 = wave * 32 + (c.cu >> 3);
  const unsigned tstart = (unsigned)__builtin_amdgcn_s_memrealtime();
  unsigned due = tstart + ((stg16 * DLW) >> 7) + T0OFF;
  const rsrc_t rt = (wave & 1) ? c.rwu : c.rwg;
  const int toff = c.cu * 7168 + (wave >> 1) * 3584 + (lane < 28 ? lane : 0) * 128;
  int koff, voff; unsigned ldsa;
#define PASS_ADDR() do { koff = ((c.lane >> 2) & 3) * 256 + (4 * (c.lane >> 4) + (c.lane & 3)) * 16; voff = c.lane * 16; ldsa = (unsigned)(size_t)(c.lds + c.wave * LDS_VW) + c.lane * 16; } while (0)
  PASS_ADDR();
  const unsigned long long m0 = 0x1111111111111111ull, m1 = m0 << 1, m2 = m0 << 2, m3 = m0 << 3;
  unsigned so0, so1;
#define LAUNDER() do { asm volatile("" : "+v"(c.tid), "+s"(c.cu), "+s"(c.wave)); c.lane = c.tid & 63; c.cp = (c.cu & 7) * 32 + (c.cu >> 3); } while (0)
#define PASS_IN [rk] "s"(rk), [rv] "s"(rv), [koff] "v"(koff), [voff] "v"(voff), [rot] "s"(rot)
#define PASS_CONST [m0] "s"(m0), [m1] "s"(m1), [m2] "s"(m2), [m3] "s"(m3), [c2] "s"(C2), [nc2] "s"(-C2), [slo] "s"(0x05040100u), [shi] "s"(0x07060302u), [lds] "v"(ldsa)

  unsigned long long pc0;
  asm volatile("s_getpc_b64 %0" : "=s"(pc0));
  pc0 &= ~255ull;
#define CODE_TOUCH(R0, R1, L0, L1) asm volatile("s_nop 4\n\tglobal_load_dword " R0 ", %0, %1\n\tglobal_load_dword " R1 ", %2, %1" :: "v"((L0) * 128), "s"(pc0), "v"(((L1) < CODE_LINES ? (L1) : 0) * 128) : "memory", R0, R1)
  if (((c.cu >> 3) & (CPF_MASK)) == 0) CODE_TOUCH("a254", "a255", c.tid < 192 ? c.tid : 0, c.tid < 192 ? c.tid : 0);
  unsigned epoch, xres, xp0[4];
  v2u xown;
  W16 wqs;
  {
    const int woff = (4 * (c.tid >> 1) * 4096 + (c.cu & 7) * 512 + (c.cu >> 3) * 16 + 8 * (c.tid & 1)) * 2;
    asm volatile(
      "s_nop 4\n\t"
      "buffer_load_dword %[ep], off, %[ws], 0 sc1\n\t"
      "global_load_dword %[x0], %[xoff], %[xp] offset:0\n\t"
      "global_load_dword %[x1], %[xoff], %[xp] offset:256\n\t"
      "global_load_dword %[x2], %[xoff], %[xp] offset:512\n\t"
      "global_load_dword %[x3], %[xoff], %[xp] offset:768\n\t"
      "global_load_dword %[xr], %[xroff], %[xp]\n\t"
      "global_load_dwordx2 %[xo], %[xooff], %[xp]\n\t"
      "s_mov_b32 %[so0], 16384\n\t"
      "s_mov_b32 %[so1], 24576\n\t"
      "buffer_load_dwordx4 %[w0], %[woff], %[rwq], 0 offen\n\t"
      "buffer_load_dwordx4 %[w2], %[woff], %[rwq], %[s8k] offen\n\t"
      "buffer_load_dwordx4 %[w1], %[woff], %[rwq], %[so0] offen\n\t"
      "buffer_load_dwordx4 %[w3], %[woff], %[rwq], %[so1] offen\n\t"
      "1:\n\t"
      "s_memrealtime s[96:97]\n\t"
      "s_waitcnt lgkmcnt(0)\n\t"
      "s_sub_u32 %[so0], s96, %[due]\n\t"
      "s_cmp_lt_i32 %[so0], 0\n\t"
      "s_cbranch_scc0 2f\n\t"
      "s_sleep 1\n\t"
      "s_branch 1b\n\t"
      "2:\n\t"
      "s_add_u32 %[due], %[due], %[dl]\n\t"
      "s_add_u32 %[so0], %[rot], 0\n\t"
      "s_and_b32 %[so0], %[so0], 0x1ffff\n\t"
      "s_add_u32 %[so1], %[so0], 4096\n\t"
      "buffer_load_dwordx4 a[0:3], %[koff], %[rk], %[so0] offen offset:0 nt\n\t"
      "buffer_load_dwordx4 a[4:7], %[koff], %[rk], %[so0] offen offset:1024 nt\n\t"
      "buffer_load_dwordx4 a[8:11], %[koff], %[rk], %[so0] offen offset:2048 nt\n\t"
      "buffer_load_dwordx4 a[12:15], %[koff], %[rk], %[so0] offen offset:3072 nt\n\t"
      "buffer_load_dwordx4 a[16:19], %[koff], %[rk], %[so1] offen offset:0 nt\n\t"
      "buffer_load_dwordx4 a[20:23], %[koff], %[rk], %[so1] offen offset:1024 nt\n\t"
      "buffer_load_dwordx4 a[24:27], %[koff], %[rk], %[so1] offen offset:2048 nt\n\t"
      "buffer_load_dwordx4 a[28:31], %[koff], %[rk], %[so1] offen offset:3072 nt\n\t"
      "s_add_u32 %[so0], %[rot], 8192\n\t"
      "s_and_b32 %[so0], %[so0], 0x1ffff\n\t"
      "s_add_u32 %[so1], %[so0], 4096\n\t"
      "buffer_load_dwordx4 a[32:35], %[koff], %[rk], %[so0] offen offset:0 nt\n\t"
      "buffer_load_dwordx4 a[36:39], %[koff], %[rk], %[so0] offen offset:1024 nt\n\t"
      "buffer_load_dwordx4 a[40:43], %[koff], %[rk], %[so0] offen offset:2048 nt\n\t"
      "buffer_load_dwordx4 a[44:47], %[koff], %[rk], %[so0] offen offset:3072 nt\n\t"
      "buffer_load_dwordx4 a[48:51], %[koff], %[rk], %[so1] offen offset:0 nt\n\t"
      "buffer_load_dwordx4 a[52:55], %[koff], %[rk], %[so1] offen offset:1024 nt\n\t"
      "buffer_load_dwordx4 a[56:59], %[koff], %[rk], %[so1] offen offset:2048 nt\n\t"
      "buffer_load_dwordx4 a[60:63], %[koff], %[rk], %[so1] offen offset:3072 nt\n\t"
      "s_waitcnt vmcnt(16)\n\t"
      : [ep] "=&v"(epoch), [x0] "=&v"(xp0[0]), [x1] "=&v"(xp0[1]), [x2] "=&v"(xp0[2]), [x3] "=&v"(xp0[3]), [xr] "=&v"(xres), [xo] "=&v"(xown),
        [w0] "=&v"(wqs.a0), [w1] "=&v"(wqs.a1), [w2] "=&v"(wqs.b0), [w3] "=&v"(wqs.b1), [so0] "=&s"(so0), [so1] "=&s"(so1), [due] "+s"(due)
      : PASS_IN, [ws] "s"(c.ws), [xp] "s"(x), [xoff] "v"(lane * 4), [xroff] "v"(c.cp * 4), [xooff] "v"((c.tid >> 1) * 8), [woff] "v"(woff), [rwq] "s"(c.rwq), [s8k] "s"(8192), [dl] "s"(DLS)
      : "memory", "scc", "vcc", "s96", "s97", "v72", "v73", "v74", "v75", "v76", "v77", "v78", "v79", "v80", "v81", "v82", "v83", "v84", "v85", "v86", "v87", "v88", "v89", "v90", "v91", "v92", "v93", "v94", "v95", "v96", "v97", "v98", "v99", "v100", "v101", "v102", "v103", "v104", "v105", "v106", "v107", "v108", "v109", "v110", "v111", "v112", "v113", "v114", "v115", "v116", "v117", "v118", "v119", "v120", "v121", "v122", "v123", "v124", "v125", "v126", "v127", "v128", "v129", "v130", "v131", "v132", "v133", "v134", "v135", "v136", "v137", "v138", "v139", "v140", "v141", "v142", "v143", "v144", "v145", "v146", "v147", "v148", "v149", "v150", "v151", "v152", "v153", "v154", "v155", "v156", "v157", "v158", "v159", "v160", "v161", "v162", "v163", "v164", "v165", "v166", "v167", "v168", "v169", "v170", "v171", "v172", "v173", "v174", "v175", "v176", "v177", "v178", "v179", "v180", "v181", "v182", "v183", "v184", "v185", "v186", "v187", "v188", "v189", "v190", "v191", "v192", "v193", "v194", "v195", "v196", "v197", "v198", "v199", "v200", "v201", "v202", "v203", "v204", "v205", "v206", "v207", "v208", "v209", "v210", "v211", "v212", "v213", "v214", "v215", "v216", "v217", "v218", "v219", "v220", "v221", "v222", "v223", "v224", "v225", "v226", "v227", "v228", "v229", "v230", "v231", "v232", "v233", "v234", "v235", "v236", "v237", "v238", "v239", "v240", "v241", "v242", "v243", "v244", "v245", "v246", "v247", "v248", "v249", "v250", "v251", "v252", "v253", "v254", "v255", "a0", "a1", "a2", "a3", "a4", "a5", "a6", "a7", "a8", "a9", "a10", "a11", "a12", "a13", "a14", "a15", "a16", "a17", "a18", "a19", "a20", "a21", "a22", "a23", "a24", "a25", "a26", "a27", "a28", "a29", "a30", "a31", "a32", "a33", "a34", "a35", "a36", "a37", "a38", "a39", "a40", "a41", "a42", "a43", "a44", "a45", "a46", "a47", "a48", "a49", "a50", "a51", "a52", "a53", "a54", "a55", "a56", "a57", "a58", "a59", "a60", "a61", "a62", "a63", "a64", "a65", "a66", "a67", "a68", "a69", "a70", "a71", "a72", "a73", "a74", "a75", "a76", "a77", "a78", "a79", "a80", "a81", "a82", "a83", "a84", "a85", "a86", "a87", "a88", "a89", "a90", "a91", "a92", "a93", "a94", "a95", "a96", "a97", "a98", "a99", "a100", "a101", "a102", "a103", "a104", "a105", "a106", "a107", "a108", "a109", "a110", "a111", "a112", "a113", "a114", "a115", "a116", "a117", "a118", "a119", "a120", "a121", "a122", "a123", "a124", "a125", "a126", "a127", "a128", "a129", "a130", "a131", "a132", "a133", "a134", "a135", "a136", "a137", "a138", "a139", "a140", "a141", "a142", "a143", "a144", "a145", "a146", "a147", "a148", "a149", "a150", "a151", "a152", "a153", "a154", "a155", "a156", "a157", "a158", "a159", "a160", "a161", "a162", "a163", "a164", "a165", "a166", "a167", "a168", "a169", "a170", "a171", "a172", "a173", "a174", "a175", "a176", "a177", "a178", "a179", "a180", "a181", "a182", "a183", "a184", "a185", "a186", "a187", "a188", "a189", "a190", "a191", "a192", "a193", "a194", "a195", "a196", "a197", "a198", "a199", "a200", "a201", "a202", "a203", "a204", "a205", "a206", "a207", "a208", "a209", "a210", "a211", "a212", "a213", "a214", "a215", "a216", "a217", "a218", "a219", "a220", "a221", "a222", "a223", "a224", "a225", "a226", "a227", "a228", "a229", "a230", "a231", "a232", "a233", "a234", "a235", "a236", "a237", "a238", "a239", "a240", "a241", "a242", "a243", "a244", "a245", "a246", "a247", "a248", "a249", "a250", "a251", "a252", "a253", "a254", "a255");
  }
  c.tag = __builtin_amdgcn_readfirstlane(epoch) + 0x9E3779B9u;
  xres = __builtin_amdgcn_readfirstlane(xres);
  {
    float mean, rden;
    ln_stats(xp0, lane, mean, rden);
    const unsigned x0 = ln_apply(xown[0], mean, rden), x1 = ln_apply(xown[1], mean, rden);
    const P16 wp = pair_up(wqs);
    float v[8];
#pragma unroll
    for (int k = 0; k < 8; ++k) v[k] = dot2(wp.p[8 + k], x1, dot2(wp.p[k], x0, 0.f));
    const float r = wave_reduce8(v, lane);
    volatile LAS v2u* red = (volatile LAS v2u*)(c.lds + LDS_SCR);
    if (lane < 16) red[wave * 16 + qcol(lane)] = v2u{asu(r), c.tag};
    if (wave == 3) {
      const int i = lane & 15;
      v2u a, b, d, e;
      for (int spins = 0;; ++spins) {
        a = red[i]; b = red[16 + i]; d = red[32 + i]; e = red[48 + i];
        if (__all(a[1] == c.tag && b[1] == c.tag && d[1] == c.tag && e[1] == c.tag) || spins > SPIN_CAP) break;
        __builtin_amdgcn_s_sleep(1);
      }
      publish_q(c, (asf(a[0]) + asf(b[0])) + (asf(d[0]) + asf(e[0])), 0);
    }
    asm volatile("v_mov_b32 v104, %0\n\tv_mov_b32 v105, %1\n\tv_mov_b32 v106, %2\n\tv_mov_b32 v107, %3\n\tv_mov_b32 v108, %4\n\tv_mov_b32 v109, %5\n\tv_mov_b32 v110, %6\n\tv_mov_b32 v111, %7\n\tv_mov_b32 v112, %8\n\tv_mov_b32 v113, %9\n\tv_mov_b32 v114, %10\n\tv_mov_b32 v115, %11\n\tv_mov_b32 v116, %12\n\tv_mov_b32 v117, %13\n\tv_mov_b32 v118, %14\n\tv_mov_b32 v119, %15\n\t" :: "v"(wp.p[0]), "v"(wp.p[1]), "v"(wp.p[2]), "v"(wp.p[3]), "v"(wp.p[4]), "v"(wp.p[5]), "v"(wp.p[6]), "v"(wp.p[7]),
                 "v"(wp.p[8]), "v"(wp.p[9]), "v"(wp.p[10]), "v"(wp.p[11]), "v"(wp.p[12]), "v"(wp.p[13]), "v"(wp.p[14]), "v"(wp.p[15]) : "v104", "v105", "v106", "v107", "v108", "v109", "v110", "v111", "v112", "v113", "v114", "v115", "v116", "v117", "v118", "v119");
  }
  v4u qf;
  {
    const int qoff = q_off(c, 0);
    v4u xq[2];
    asm volatile(
      "s_nop 4\n\t"
      "1:\n\t"
      "s_memrealtime s[96:97]\n\t"
      "s_waitcnt lgkmcnt(0)\n\t"
      "s_sub_u32 %[so0], s96, %[due]\n\t"
      "s_cmp_lt_i32 %[so0], 0\n\t"
      "s_cbranch_scc0 2f\n\t"
      "s_sleep 1\n\t"
      "s_branch 1b\n\t"
      "2:\n\t"
      "s_add_u32 %[due], %[due], %[dl]\n\t"
      "s_add_u32 %[so0], %[rot], 0\n\t"
      "s_and_b32 %[so0], %[so0], 0x1ffff\n\t"
      "s_add_u32 %[so1], %[so0], 4096\n\t"
      "buffer_load_dwordx4 v[128:131], %[voff], %[rv], %[so0] offen offset:0 nt\n\t"
      "buffer_load_dwordx4 v[132:135], %[voff], %[rv], %[so0] offen offset:1024 nt\n\t"
      "buffer_load_dwordx4 v[136:139], %[voff], %[rv], %[so0] offen offset:2048 nt\n\t"
      "buffer_load_dwordx4 v[140:143], %[voff], %[rv], %[so0] offen offset:3072 nt\n\t"
      "buffer_load_dwordx4 v[144:147], %[voff], %[rv], %[so1] offen offset:0 nt\n\t"
      "buffer_load_dwordx4 v[148:151], %[voff], %[rv], %[so1] offen offset:1024 nt\n\t"
      "buffer_load_dwordx4 v[152:155], %[voff], %[rv], %[so1] offen offset:2048 nt\n\t"
      "buffer_load_dwordx4 v[156:159], %[voff], %[rv], %[so1] offen offset:3072 nt\n\t"
      "s_add_u32 %[so0], %[rot], 8192\n\t"
      "s_and_b32 %[so0], %[so0], 0x1ffff\n\t"
      "s_add_u32 %[so1], %[so0], 4096\n\t"
      "buffer_load_dwordx4 v[160:163], %[voff], %[rv], %[so0] offen offset:0 nt\n\t"
      "buffer_load_dwordx4 v[164:167], %[voff], %[rv], %[so0] offen offset:1024 nt\n\t"
      "buffer_load_dwordx4 v[168:171], %[voff], %[rv], %[so0] offen offset:2048 nt\n\t"
      "buffer_load_dwordx4 v[172:175], %[voff], %[rv], %[so0] offen offset:3072 nt\n\t"
      "buffer_load_dwordx4 v[176:179], %[voff], %[rv], %[so1] offen offset:0 nt\n\t"
      "buffer_load_dwordx4 v[180:183], %[voff], %[rv], %[so1] offen offset:1024 nt\n\t"
      "buffer_load_dwordx4 v[184:187], %[voff], %[rv], %[so1] offen offset:2048 nt\n\t"
      "buffer_load_dwordx4 v[188:191], %[voff], %[rv], %[so1] offen offset:3072 nt\n\t"
      "1:\n\t"
      "s_memrealtime s[96:97]\n\t"
      "s_waitcnt lgkmcnt(0)\n\t"
      "s_sub_u32 %[so0], s96, %[due]\n\t"
      "s_cmp_lt_i32 %[so0], 0\n\t"
      "s_cbranch_scc0 2f\n\t"
      "s_sleep 1\n\t"
      "s_branch 1b\n\t"
      "2:\n\t"
      "s_add_u32 %[due], %[due], %[dl]\n\t"
      "buffer_load_dwordx4 %[qa0], %[qoff], %[ws], 0 offen sc1\n\t"
      "buffer_load_dwordx4 %[qa1], %[qoff], %[ws], 0 offen offset:16 sc1\n\t"
      "s_add_u32 %[so0], %[rot], 16384\n\t"
      "s_and_b32 %[so0], %[so0], 0x1ffff\n\t"
      "s_add_u32 %[so1], %[so0], 4096\n\t"
      "buffer_load_dwordx4 a[64:67], %[koff], %[rk], %[so0] offen offset:0 nt\n\t"
      "buffer_load_dwordx4 a[68:71], %[koff], %[rk], %[so0] offen offset:1024 nt\n\t"
      "buffer_load_dwordx4 a[72:75], %[koff], %[rk], %[so0] offen offset:2048 nt\n\t"
      "buffer_load_dwordx4 a[76:79], %[koff], %[rk], %[so0] offen offset:3072 nt\n\t"
      "buffer_load_dwordx4 a[80:83], %[koff], %[rk], %[so1] offen offset:0 nt\n\t"
      "buffer_load_dwordx4 a[84:87], %[koff], %[rk], %[so1] offen offset:1024 nt\n\t"
      "buffer_load_dwordx4 a[88:91], %[koff], %[rk], %[so1] offen offset:2048 nt\n\t"
      "buffer_load_dwordx4 a[92:95], %[koff], %[rk], %[so1] offen offset:3072 nt\n\t"
      "s_add_u32 %[so0], %[rot], 24576\n\t"
      "s_and_b32 %[so0], %[so0], 0x1ffff\n\t"
      "s_add_u32 %[so1], %[so0], 4096\n\t"
      "buffer_load_dwordx4 a[96:99], %[koff], %[rk], %[so0] offen offset:0 nt\n\t"
      "buffer_load_dwordx4 a[100:103], %[koff], %[rk], %[so0] offen offset:1024 nt\n\t"
      "buffer_load_dwordx4 a[104:107], %[koff], %[rk], %[so0] offen offset:2048 nt\n\t"
      "buffer_load_dwordx4 a[108:111], %[koff], %[rk], %[so0] offen offset:3072 nt\n\t"
      "buffer_load_dwordx4 a[112:115], %[koff], %[rk], %[so1] offen offset:0 nt\n\t"
      "buffer_load_dwordx4 a[116:119], %[koff], %[rk], %[so1] offen offset:1024 nt\n\t"
      "buffer_load_dwordx4 a[120:123], %[koff], %[rk], %[so1] offen offset:2048 nt\n\t"
      "buffer_load_dwordx4 a[124:127], %[koff], %[rk], %[so1] offen offset:3072 nt\n\t"
      "s_waitcnt vmcnt(16)\n\t"
      : [qa0] "=&v"(xq[0]), [qa1] "=&v"(xq[1]), [so0] "=&s"(so0), [so1] "=&s"(so1), [due] "+s"(due)
      : PASS_IN, [ws] "s"(c.ws), [qoff] "v"(qoff), [dl] "s"(DLS)
      : "memory", "scc", "vcc", "s96", "s97", "v72", "v73", "v74", "v75", "v76", "v77", "v78", "v79", "v80", "v81", "v82", "v83", "v84", "v85", "v86", "v87", "v88", "v89", "v90", "v91", "v92", "v93", "v94", "v95", "v96", "v97", "v98", "v99", "v100", "v101", "v102", "v103", "v104", "v105", "v106", "v107", "v108", "v109", "v110", "v111", "v112", "v113", "v114", "v115", "v116", "v117", "v118", "v119", "v120", "v121", "v122", "v123", "v124", "v125", "v126", "v127", "v128", "v129", "v130", "v131", "v132", "v133", "v134", "v135", "v136", "v137", "v138", "v139", "v140", "v141", "v142", "v143", "v144", "v145", "v146", "v147", "v148", "v149", "v150", "v151", "v152", "v153", "v154", "v155", "v156", "v157", "v158", "v159", "v160", "v161", "v162", "v163", "v164", "v165", "v166", "v167", "v168", "v169", "v170", "v171", "v172", "v173", "v174", "v175", "v176", "v177", "v178", "v179", "v180", "v181", "v182", "v183", "v184", "v185", "v186", "v187", "v188", "v189", "v190", "v191", "v192", "v193", "v194", "v195", "v196", "v197", "v198", "v199", "v200", "v201", "v202", "v203", "v204", "v205", "v206", "v207", "v208", "v209", "v210", "v211", "v212", "v213", "v214", "v215", "v216", "v217", "v218", "v219", "v220", "v221", "v222", "v223", "v224", "v225", "v226", "v227", "v228", "v229", "v230", "v231", "v232", "v233", "v234", "v235", "v236", "v237", "v238", "v239", "v240", "v241", "v242", "v243", "v244", "v245", "v246", "v247", "v248", "v249", "v250", "v251", "v252", "v253", "v254", "v255", "a0", "a1", "a2", "a3", "a4", "a5", "a6", "a7", "a8", "a9", "a10", "a11", "a12", "a13", "a14", "a15", "a16", "a17", "a18", "a19", "a20", "a21", "a22", "a23", "a24", "a25", "a26", "a27", "a28", "a29", "a30", "a31", "a32", "a33", "a34", "a35", "a36", "a37", "a38", "a39", "a40", "a41", "a42", "a43", "a44", "a45", "a46", "a47", "a48", "a49", "a50", "a51", "a52", "a53", "a54", "a55", "a56", "a57", "a58", "a59", "a60", "a61", "a62", "a63", "a64", "a65", "a66", "a67", "a68", "a69", "a70", "a71", "a72", "a73", "a74", "a75", "a76", "a77", "a78", "a79", "a80", "a81", "a82", "a83", "a84", "a85", "a86", "a87", "a88", "a89", "a90", "a91", "a92", "a93", "a94", "a95", "a96", "a97", "a98", "a99", "a100", "a101", "a102", "a103", "a104", "a105", "a106", "a107", "a108", "a109", "a110", "a111", "a112", "a113", "a114", "a115", "a116", "a117", "a118", "a119", "a120", "a121", "a122", "a123", "a124", "a125", "a126", "a127", "a128", "a129", "a130", "a131", "a132", "a133", "a134", "a135", "a136", "a137", "a138", "a139", "a140", "a141", "a142", "a143", "a144", "a145", "a146", "a147", "a148", "a149", "a150", "a151", "a152", "a153", "a154", "a155", "a156", "a157", "a158", "a159", "a160", "a161", "a162", "a163", "a164", "a165", "a166", "a167", "a168", "a169", "a170", "a171", "a172", "a173", "a174", "a175", "a176", "a177", "a178", "a179", "a180", "a181", "a182", "a183", "a184", "a185", "a186", "a187", "a188", "a189", "a190", "a191", "a192", "a193", "a194", "a195", "a196", "a197", "a198", "a199", "a200", "a201", "a202", "a203", "a204", "a205", "a206", "a207", "a208", "a209", "a210", "a211", "a212", "a213", "a214", "a215", "a216", "a217", "a218", "a219", "a220", "a221", "a222", "a223", "a224", "a225", "a226", "a227", "a228", "a229", "a230", "a231", "a232", "a233", "a234", "a235", "a236", "a237", "a238", "a239", "a240", "a241", "a242", "a243", "a244", "a245", "a246", "a247", "a248", "a249", "a250", "a251", "a252", "a253", "a254", "a255");
    if (!__all(tag_ok(xq[0], c.tag) && tag_ok(xq[1], c.tag))) {
      const int offs[2] = {qoff, qoff + 16};
      gather2<0>(c.ws, offs, c.tag, xq);
    }
    qf = v4u{xq[0][0], xq[0][2], xq[1][0], xq[1][2]};
  }
  unsigned slow = 0;
  const rsrc_t rsh = __builtin_amdgcn_make_buffer_rsrc(ws + WS_SH, 0, 4096, 0x00020000);
  const unsigned shslot = ((c.cu & 3) == ((c.cu >> 2) & 3) && wave == ((c.cu >> 4) & 3)) ? (c.cu >> 2) * 16 : 0x10000;
  {
    const int row = wave == 2 ? ffn_slot(c) : c.cu;
    int o = WS_PART + (wave * 2 + (lane >> 5)) * BIG_L + row * 4096 + (lane & 31) * 128;
    if (wave == 3) {
      const int k = lane & 7;
      o = k < 2 ? WS_ML + k * ML_L + (NEWL ? c.cp : c.cu) * 128 : k < 4 ? WS_X1 + (k - 2) * X_L + c.cp * 16 : WS_X2 + c.cp * 16;
    }
    if (WS_WARM == 2 || wave == 3) asm volatile("s_nop 4\n\tbuffer_load_dword a252, %0, %1, 0 offen sc1" :: "v"(o), "s"(c.ws) : "memory", "a252");
  }
  Att st;
  asm volatile(
      "v_mov_b32 v12, 0\n\t"
      "v_mov_b32 v13, 0\n\t"
      "v_mov_b32 v14, 0\n\t"
      "v_mov_b32 v15, 0\n\t"
      "v_mov_b32 v16, 0\n\t"
      "v_mov_b32 v17, 0\n\t"
      "v_mov_b32 v18, 0\n\t"
      "v_mov_b32 v19, 0\n\t"
      "v_mov_b32 v20, 0xff800000\n\t"
      "v_mov_b32 v21, 0\n\t"
      "v_mfma_f32_16x16x32_bf16 v[192:195], a[0:3], v[8:11], 0\n\t"
      "v_mfma_f32_16x16x32_bf16 v[196:199], a[4:7], v[8:11], 0\n\t"
      "v_mfma_f32_16x16x32_bf16 v[200:203], a[8:11], v[8:11], 0\n\t"
      "v_mfma_f32_16x16x32_bf16 v[204:207], a[12:15], v[8:11], 0\n\t"
      "v_mfma_f32_16x16x32_bf16 v[208:211], a[16:19], v[8:11], 0\n\t"
      "v_mfma_f32_16x16x32_bf16 v[212:215], a[20:23], v[8:11], 0\n\t"
      "v_mfma_f32_16x16x32_bf16 v[216:219], a[24:27], v[8:11], 0\n\t"
      "v_mfma_f32_16x16x32_bf16 v[220:223], a[28:31], v[8:11], 0\n\t"
      "s_nop 1\n\t"
      "v_mov_b32_dpp v26, v192 quad_perm:[0,0,0,0] row_mask:0xf bank_mask:0xf\n\t"
      "v_mov_b32_dpp v27, v196 quad_perm:[0,0,0,0] row_mask:0xf bank_mask:0xf\n\t"
      "v_mov_b32_dpp v28, v200 quad_perm:[0,0,0,0] row_mask:0xf bank_mask:0xf\n\t"
      "v_mov_b32_dpp v29, v204 quad_perm:[0,0,0,0] row_mask:0xf bank_mask:0xf\n\t"
      "v_add_f32_dpp v26, v193, v26 quad_perm:[1,1,1,1] row_mask:0xf bank_mask:0xf\n\t"
      "v_add_f32_dpp v27, v197, v27 quad_perm:[1,1,1,1] row_mask:0xf bank_mask:0xf\n\t"
      "v_add_f32_dpp v28, v201, v28 quad_perm:[1,1,1,1] row_mask:0xf bank_mask:0xf\n\t"
      "v_add_f32_dpp v29, v205, v29 quad_perm:[1,1,1,1] row_mask:0xf bank_mask:0xf\n\t"
      "v_add_f32_dpp v26, v194, v26 quad_perm:[2,2,2,2] row_mask:0xf bank_mask:0xf\n\t"
      "v_add_f32_dpp v27, v198, v27 quad_perm:[2,2,2,2] row_mask:0xf bank_mask:0xf\n\t"
      "v_add_f32_dpp v28, v202, v28 quad_perm:[2,2,2,2] row_mask:0xf bank_mask:0xf\n\t"
      "v_add_f32_dpp v29, v206, v29 quad_perm:[2,2,2,2] row_mask:0xf bank_mask:0xf\n\t"
      "v_add_f32_dpp v26, v195, v26 quad_perm:[3,3,3,3] row_mask:0xf bank_mask:0xf\n\t"
      "v_add_f32_dpp v27, v199, v27 quad_perm:[3,3,3,3] row_mask:0xf bank_mask:0xf\n\t"
      "v_add_f32_dpp v28, v203, v28 quad_perm:[3,3,3,3] row_mask:0xf bank_mask:0xf\n\t"
      "v_add_f32_dpp v29, v207, v29 quad_perm:[3,3,3,3] row_mask:0xf bank_mask:0xf\n\t"
      "v_mov_b32_dpp v30, v208 quad_perm:[0,0,0,0] row_mask:0xf bank_mask:0xf\n\t"
      "v_mov_b32_dpp v31, v212 quad_perm:[0,0,0,0] row_mask:0xf bank_mask:0xf\n\t"
      "v_mov_b32_dpp v32, v216 quad_perm:[0,0,0,0] row_mask:0xf bank_mask:0xf\n\t"
      "v_mov_b32_dpp v33, v220 quad_perm:[0,0,0,0] row_mask:0xf bank_mask:0xf\n\t"
      "v_add_f32_dpp v30, v209, v30 quad_perm:[1,1,1,1] row_mask:0xf bank_mask:0xf\n\t"
      "v_add_f32_dpp v31, v213, v31 quad_perm:[1,1,1,1] row_mask:0xf bank_mask:0xf\n\t"
      "v_add_f32_dpp v32, v217, v32 quad_perm:[1,1,1,1] row_mask:0xf bank_mask:0xf\n\t"
      "v_add_f32_dpp v33, v221, v33 quad_perm:[1,1,1,1] row_mask:0xf bank_mask:0xf\n\t"
      "v_add_f32_dpp v30, v210, v30 quad_perm:[2,2,2,2] row_mask:0xf bank_mask:0xf\n\t"
      "v_add_f32_dpp v31, v214, v31 quad_perm:[2,2,2,2] row_mask:0xf bank_mask:0xf\n\t"
      "v_add_f32_dpp v32, v218, v32 quad_perm:[2,2,2,2] row_mask:0xf bank_mask:0xf\n\t"
      "v_add_f32_dpp v33, v222, v33 quad_perm:[2,2,2,2] row_mask:0xf bank_mask:0xf\n\t"
      "v_add_f32_dpp v30, v211, v30 quad_perm:[3,3,3,3] row_mask:0xf bank_mask:0xf\n\t"
      "v_add_f32_dpp v31, v215, v31 quad_perm:[3,3,3,3] row_mask:0xf bank_mask:0xf\n\t"
      "v_add_f32_dpp v32, v219, v32 quad_perm:[3,3,3,3] row_mask:0xf bank_mask:0xf\n\t"
      "v_add_f32_dpp v33, v223, v33 quad_perm:[3,3,3,3] row_mask:0xf bank_mask:0xf\n\t"
      "v_mfma_f32_16x16x32_bf16 v[192:195], a[32:35], v[8:11], 0\n\t"
      "v_mfma_f32_16x16x32_bf16 v[196:199], a[36:39], v[8:11], 0\n\t"
      "v_mfma_f32_16x16x32_bf16 v[200:203], a[40:43], v[8:11], 0\n\t"
      "v_mfma_f32_16x16x32_bf16 v[204:207], a[44:47], v[8:11], 0\n\t"
      "v_mfma_f32_16x16x32_bf16 v[208:211], a[48:51], v[8:11], 0\n\t"
      "v_mfma_f32_16x16x32_bf16 v[212:215], a[52:55], v[8:11], 0\n\t"
      "v_mfma_f32_16x16x32_bf16 v[216:219], a[56:59], v[8:11], 0\n\t"
      "v_mfma_f32_16x16x32_bf16 v[220:223], a[60:63], v[8:11], 0\n\t"
      "s_nop 1\n\t"
      "v_mov_b32_dpp v192, v192 quad_perm:[0,0,0,0] row_mask:0xf bank_mask:0xf\n\t"
      "v_mov_b32_dpp v196, v196 quad_perm:[0,0,0,0] row_mask:0xf bank_mask:0xf\n\t"
      "v_mov_b32_dpp v200, v200 quad_perm:[0,0,0,0] row_mask:0xf bank_mask:0xf\n\t"
      "v_mov_b32_dpp v204, v204 quad_perm:[0,0,0,0] row_mask:0xf bank_mask:0xf\n\t"
      "v_add_f32_dpp v192, v193, v192 quad_perm:[1,1,1,1] row_mask:0xf bank_mask:0xf\n\t"
      "v_add_f32_dpp v196, v197, v196 quad_perm:[1,1,1,1] row_mask:0xf bank_mask:0xf\n\t"
      "v_add_f32_dpp v200, v201, v200 quad_perm:[1,1,1,1] row_mask:0xf bank_mask:0xf\n\t"
      "v_add_f32_dpp v204, v205, v204 quad_perm:[1,1,1,1] row_mask:0xf bank_mask:0xf\n\t"
      "v_add_f32_dpp v192, v194, v192 quad_perm:[2,2,2,2] row_mask:0xf bank_mask:0xf\n\t"
      "v_add_f32_dpp v196, v198, v196 quad_perm:[2,2,2,2] row_mask:0xf bank_mask:0xf\n\t"
      "v_add_f32_dpp v200, v202, v200 quad_perm:[2,2,2,2] row_mask:0xf bank_mask:0xf\n\t"
      "v_add_f32_dpp v204, v206, v204 quad_perm:[2,2,2,2] row_mask:0xf bank_mask:0xf\n\t"
      "v_add_f32_dpp v192, v195, v192 quad_perm:[3,3,3,3] row_mask:0xf bank_mask:0xf\n\t"
      "v_add_f32_dpp v196, v199, v196 quad_perm:[3,3,3,3] row_mask:0xf bank_mask:0xf\n\t"
      "v_add_f32_dpp v200, v203, v200 quad_perm:[3,3,3,3] row_mask:0xf bank_mask:0xf\n\t"
      "v_add_f32_dpp v204, v207, v204 quad_perm:[3,3,3,3] row_mask:0xf bank_mask:0xf\n\t"
      "v_mov_b32_dpp v208, v208 quad_perm:[0,0,0,0] row_mask:0xf bank_mask:0xf\n\t"
      "v_mov_b32_dpp v212, v212 quad_perm:[0,0,0,0] row_mask:0xf bank_mask:0xf\n\t"
      "v_mov_b32_dpp v216, v216 quad_perm:[0,0,0,0] row_mask:0xf bank_mask:0xf\n\t"
      "v_mov_b32_dpp v220, v220 quad_perm:[0,0,0,0] row_mask:0xf bank_mask:0xf\n\t"
      "v_add_f32_dpp v208, v209, v208 quad_perm:[1,1,1,1] row_mask:0xf bank_mask:0xf\n\t"
      "v_add_f32_dpp v212, v213, v212 quad_perm:[1,1,1,1] row_mask:0xf bank_mask:0xf\n\t"
      "v_add_f32_dpp v216, v217, v216 quad_perm:[1,1,1,1] row_mask:0xf bank_mask:0xf\n\t"
      "v_add_f32_dpp v220, v221, v220 quad_perm:[1,1,1,1] row_mask:0xf bank_mask:0xf\n\t"
      "v_add_f32_dpp v208, v210, v208 quad_perm:[2,2,2,2] row_mask:0xf bank_mask:0xf\n\t"
      "v_add_f32_dpp v212, v214, v212 quad_perm:[2,2,2,2] row_mask:0xf bank_mask:0xf\n\t"
      "v_add_f32_dpp v216, v218, v216 quad_perm:[2,2,2,2] row_mask:0xf bank_mask:0xf\n\t"
      "v_add_f32_dpp v220, v222, v220 quad_perm:[2,2,2,2] row_mask:0xf bank_mask:0xf\n\t"
      "v_add_f32_dpp v208, v211, v208 quad_perm:[3,3,3,3] row_mask:0xf bank_mask:0xf\n\t"
      "v_add_f32_dpp v212, v215, v212 quad_perm:[3,3,3,3] row_mask:0xf bank_mask:0xf\n\t"
      "v_add_f32_dpp v216, v219, v216 quad_perm:[3,3,3,3] row_mask:0xf bank_mask:0xf\n\t"
      "v_add_f32_dpp v220, v223, v220 quad_perm:[3,3,3,3] row_mask:0xf bank_mask:0xf\n\t"
      "v_max3_f32 v22, v26, v27, v20\n\t"
      "v_max3_f32 v23, v28, v29, v30\n\t"
      "v_max3_f32 v22, v22, v31, v32\n\t"
      "v_max3_f32 v23, v23, v33, v192\n\t"
      "v_max3_f32 v22, v22, v196, v200\n\t"
      "v_max3_f32 v23, v23, v204, v208\n\t"
      "v_max3_f32 v22, v22, v212, v216\n\t"
      "v_max3_f32 v22, v22, v23, v220\n\t"
      "v_mov_b32 v23, v22\n\t"
      "s_nop 1\n\t"
      "v_permlane16_swap_b32 v22, v23\n\t"
      "s_nop 0\n\t"
      "v_max_f32 v22, v22, v23\n\t"
      "v_mov_b32 v23, v22\n\t"
      "s_nop 1\n\t"
      "v_permlane32_swap_b32 v22, v23\n\t"
      "s_nop 0\n\t"
      "v_max_f32 v22, v22, v23\n\t"
      "v_sub_f32 v23, v20, v22\n\t"
      "v_mul_f32 v24, %[nc2], v22\n\t"
      "v_mul_f32 v23, %[c2], v23\n\t"
      "v_mov_b32 v20, v22\n\t"
      "v_exp_f32 v23, v23\n\t"
      "v_fma_f32 v26, v26, %[c2], v24\n\t"
      "v_fma_f32 v27, v27, %[c2], v24\n\t"
      "v_fma_f32 v28, v28, %[c2], v24\n\t"
      "v_fma_f32 v29, v29, %[c2], v24\n\t"
      "v_fma_f32 v30, v30, %[c2], v24\n\t"
      "v_fma_f32 v31, v31, %[c2], v24\n\t"
      "v_fma_f32 v32, v32, %[c2], v24\n\t"
      "v_fma_f32 v33, v33, %[c2], v24\n\t"
      "v_fma_f32 v192, v192, %[c2], v24\n\t"
      "v_fma_f32 v196, v196, %[c2], v24\n\t"
      "v_fma_f32 v200, v200, %[c2], v24\n\t"
      "v_fma_f32 v204, v204, %[c2], v24\n\t"
      "v_fma_f32 v208, v208, %[c2], v24\n\t"
      "v_fma_f32 v212, v212, %[c2], v24\n\t"
      "v_fma_f32 v216, v216, %[c2], v24\n\t"
      "v_fma_f32 v220, v220, %[c2], v24\n\t"
      "v_exp_f32 v26, v26\n\t"
      "v_mul_f32 v12, v23, v12\n\t"
      "v_exp_f32 v27, v27\n\t"
      "v_mul_f32 v13, v23, v13\n\t"
      "v_exp_f32 v28, v28\n\t"
      "v_mul_f32 v14, v23, v14\n\t"
      "v_exp_f32 v29, v29\n\t"
      "v_mul_f32 v15, v23, v15\n\t"
      "v_exp_f32 v30, v30\n\t"
      "v_mul_f32 v16, v23, v16\n\t"
      "v_exp_f32 v31, v31\n\t"
      "v_mul_f32 v17, v23, v17\n\t"
      "v_exp_f32 v32, v32\n\t"
      "v_mul_f32 v18, v23, v18\n\t"
      "v_exp_f32 v33, v33\n\t"
      "v_mul_f32 v19, v23, v19\n\t"
      "v_exp_f32 v192, v192\n\t"
      "v_exp_f32 v196, v196\n\t"
      "v_exp_f32 v200, v200\n\t"
      "v_exp_f32 v204, v204\n\t"
      "v_exp_f32 v208, v208\n\t"
      "v_exp_f32 v212, v212\n\t"
      "v_exp_f32 v216, v216\n\t"
      "v_exp_f32 v220, v220\n\t"
      "v_mul_f32 v21, v21, v23\n\t"
      "v_add_f32 v22, v26, v27\n\t"
      "v_add_f32 v23, v28, v29\n\t"
      "v_add_f32 v22, v22, v30\n\t"
      "v_add_f32 v23, v23, v31\n\t"
      "v_add_f32 v22, v22, v32\n\t"
      "v_add_f32 v23, v23, v33\n\t"
      "v_add_f32 v22, v22, v192\n\t"
      "v_add_f32 v23, v23, v196\n\t"
      "v_add_f32 v22, v22, v200\n\t"
      "v_add_f32 v23, v23, v204\n\t"
      "v_add_f32 v22, v22, v208\n\t"
      "v_add_f32 v23, v23, v212\n\t"
      "v_add_f32 v22, v22, v216\n\t"
      "v_add_f32 v23, v23, v220\n\t"
      "v_add_f32 v21, v21, v22\n\t"
      "v_cvt_pk_bf16_f32 v34, v26, v27\n\t"
      "v_cvt_pk_bf16_f32 v35, v28, v29\n\t"
      "v_cvt_pk_bf16_f32 v36, v30, v31\n\t"
      "v_cvt_pk_bf16_f32 v37, v32, v33\n\t"
      "v_add_f32 v21, v21, v23\n\t"
      "v_cvt_pk_bf16_f32 v38, v192, v196\n\t"
      "v_cvt_pk_bf16_f32 v39, v200, v204\n\t"
      "v_cvt_pk_bf16_f32 v24, v208, v212\n\t"
      "v_cvt_pk_bf16_f32 v25, v216, v220\n\t"
      "ds_write_b128 %[lds], a[0:3] offset:32768\n\t"
      "ds_write_b128 %[lds], a[4:7] offset:33792\n\t"
      "ds_write_b128 %[lds], a[8:11] offset:34816\n\t"
      "ds_write_b128 %[lds], a[12:15] offset:35840\n\t"
      "ds_write_b128 %[lds], a[16:19] offset:36864\n\t"
      "ds_write_b128 %[lds], a[20:23] offset:37888\n\t"
      "v_perm_b32 v224, v132, v128, %[slo]\n\t"
      "v_perm_b32 v228, v132, v128, %[shi]\n\t"
      "v_perm_b32 v225, v140, v136, %[slo]\n\t"
      "v_perm_b32 v229, v140, v136, %[shi]\n\t"
      "v_perm_b32 v226, v148, v144, %[slo]\n\t"
      "v_perm_b32 v230, v148, v144, %[shi]\n\t"
      "v_perm_b32 v227, v156, v152, %[slo]\n\t"
      "v_perm_b32 v231, v156, v152, %[shi]\n\t"
      "v_perm_b32 v232, v133, v129, %[slo]\n\t"
      "v_perm_b32 v236, v133, v129, %[shi]\n\t"
      "v_perm_b32 v233, v141, v137, %[slo]\n\t"
      "v_perm_b32 v237, v141, v137, %[shi]\n\t"
      "v_perm_b32 v234, v149, v145, %[slo]\n\t"
      "v_perm_b32 v238, v149, v145, %[shi]\n\t"
      "v_perm_b32 v235, v157, v153, %[slo]\n\t"
      "v_perm_b32 v239, v157, v153, %[shi]\n\t"
      "v_perm_b32 v240, v134, v130, %[slo]\n\t"
      "v_perm_b32 v244, v134, v130, %[shi]\n\t"
      "v_perm_b32 v241, v142, v138, %[slo]\n\t"
      "v_perm_b32 v245, v142, v138, %[shi]\n\t"
      "v_perm_b32 v242, v150, v146, %[slo]\n\t"
      "v_perm_b32 v246, v150, v146, %[shi]\n\t"
      "v_perm_b32 v243, v158, v154, %[slo]\n\t"
      "v_perm_b32 v247, v158, v154, %[shi]\n\t"
      "v_perm_b32 v248, v135, v131, %[slo]\n\t"
      "v_perm_b32 v252, v135, v131, %[shi]\n\t"
      "v_perm_b32 v249, v143, v139, %[slo]\n\t"
      "v_perm_b32 v253, v143, v139, %[shi]\n\t"
      "v_perm_b32 v250, v151, v147, %[slo]\n\t"
      "v_perm_b32 v254, v151, v147, %[shi]\n\t"
      "v_perm_b32 v251, v159, v155, %[slo]\n\t"
      "v_perm_b32 v255, v159, v155, %[shi]\n\t"
      "v_cndmask_b32 v26, 0, v34, %[m0]\n\t"
      "v_cndmask_b32 v27, 0, v35, %[m0]\n\t"
      "v_cndmask_b32 v28, 0, v36, %[m0]\n\t"
      "v_cndmask_b32 v29, 0, v37, %[m0]\n\t"
      "v_cndmask_b32 v30, 0, v34, %[m1]\n\t"
      "v_cndmask_b32 v31, 0, v35, %[m1]\n\t"
      "v_cndmask_b32 v32, 0, v36, %[m1]\n\t"
      "v_cndmask_b32 v33, 0, v37, %[m1]\n\t"
      "v_mfma_f32_16x16x32_bf16 v[12:15], v[224:227], v[26:29], v[12:15]\n\t"
      "v_mfma_f32_16x16x32_bf16 v[16:19], v[240:243], v[26:29], v[16:19]\n\t"
      "v_cndmask_b32 v26, 0, v34, %[m2]\n\t"
      "v_cndmask_b32 v27, 0, v35, %[m2]\n\t"
      "v_cndmask_b32 v28, 0, v36, %[m2]\n\t"
      "v_cndmask_b32 v29, 0, v37, %[m2]\n\t"
      "v_mfma_f32_16x16x32_bf16 v[12:15], v[228:231], v[30:33], v[12:15]\n\t"
      "v_mfma_f32_16x16x32_bf16 v[16:19], v[244:247], v[30:33], v[16:19]\n\t"
      "v_cndmask_b32 v30, 0, v34, %[m3]\n\t"
      "v_cndmask_b32 v31, 0, v35, %[m3]\n\t"
      "v_cndmask_b32 v32, 0, v36, %[m3]\n\t"
      "v_cndmask_b32 v33, 0, v37, %[m3]\n\t"
      "v_mfma_f32_16x16x32_bf16 v[12:15], v[232:235], v[26:29], v[12:15]\n\t"
      "v_mfma_f32_16x16x32_bf16 v[16:19], v[248:251], v[26:29], v[16:19]\n\t"
      "v_mfma_f32_16x16x32_bf16 v[12:15], v[236:239], v[30:33], v[12:15]\n\t"
      "v_mfma_f32_16x16x32_bf16 v[16:19], v[252:255], v[30:33], v[16:19]\n\t"
      "1:\n\t"
      "s_memrealtime s[96:97]\n\t"
      "s_waitcnt lgkmcnt(0)\n\t"
      "s_sub_u32 %[so0], s96, %[due]\n\t"
      "s_cmp_lt_i32 %[so0], 0\n\t"
      "s_cbranch_scc0 2f\n\t"
      "s_sleep 1\n\t"
      "s_branch 1b\n\t"
      "2:\n\t"
      "s_add_u32 %[due], %[due], %[dlh]\n\t"
      "s_add_u32 %[so0], %[rot], 16384\n\t"
      "s_and_b32 %[so0], %[so0], 0x1ffff\n\t"
      "s_add_u32 %[so1], %[so0], 4096\n\t"
      "buffer_load_dwordx4 v[192:195], %[voff], %[rv], %[so0] offen offset:0 nt\n\t"
      "buffer_load_dwordx4 v[196:199], %[voff], %[rv], %[so0] offen offset:1024 nt\n\t"
      "buffer_load_dwordx4 v[200:203], %[voff], %[rv], %[so0] offen offset:2048 nt\n\t"
      "buffer_load_dwordx4 v[204:207], %[voff], %[rv], %[so0] offen offset:3072 nt\n\t"
      "buffer_load_dwordx4 v[208:211], %[voff], %[rv], %[so1] offen offset:0 nt\n\t"
      "buffer_load_dwordx4 v[212:215], %[voff], %[rv], %[so1] offen offset:1024 nt\n\t"
      "buffer_load_dwordx4 v[216:219], %[voff], %[rv], %[so1] offen offset:2048 nt\n\t"
      "buffer_load_dwordx4 v[220:223], %[voff], %[rv], %[so1] offen offset:3072 nt\n\t"
      "s_add_u32 %[so0], %[rot], 24576\n\t"
      "s_and_b32 %[so0], %[so0], 0x1ffff\n\t"
      "s_add_u32 %[so1], %[so0], 4096\n\t"
      "buffer_load_dwordx4 v[224:227], %[voff], %[rv], %[so0] offen offset:0 nt\n\t"
      "buffer_load_dwordx4 v[228:231], %[voff], %[rv], %[so0] offen offset:1024 nt\n\t"
      "buffer_load_dwordx4 v[232:235], %[voff], %[rv], %[so0] offen offset:2048 nt\n\t"
      "buffer_load_dwordx4 v[236:239], %[voff], %[rv], %[so0] offen offset:3072 nt\n\t"
      "buffer_load_dwordx4 v[240:243], %[voff], %[rv], %[so1] offen offset:0 nt\n\t"
      "buffer_load_dwordx4 v[244:247], %[voff], %[rv], %[so1] offen offset:1024 nt\n\t"
      "buffer_load_dwordx4 v[248:251], %[voff], %[rv], %[so1] offen offset:2048 nt\n\t"
      "buffer_load_dwordx4 v[252:255], %[voff], %[rv], %[so1] offen offset:3072 nt\n\t"
      "s_mov_b32 %[so0], 0\n\t"
      "buffer_load_dword v72, %[wooff], %[rwo], %[so0] offen offset:0 nt\n\t"
      "buffer_load_dword v74, %[wooff], %[rwo], %[so0] offen offset:1024 nt\n\t"
      "buffer_load_dword v76, %[wooff], %[rwo], %[so0] offen offset:2048 nt\n\t"
      "buffer_load_dword v78, %[wooff], %[rwo], %[so0] offen offset:3072 nt\n\t"
      "s_waitcnt vmcnt(20)\n\t"
      "v_perm_b32 v128, v164, v160, %[slo]\n\t"
      "v_perm_b32 v132, v164, v160, %[shi]\n\t"
      "v_perm_b32 v129, v172, v168, %[slo]\n\t"
      "v_perm_b32 v133, v172, v168, %[shi]\n\t"
      "v_perm_b32 v130, v180, v176, %[slo]\n\t"
      "v_perm_b32 v134, v180, v176, %[shi]\n\t"
      "v_perm_b32 v131, v188, v184, %[slo]\n\t"
      "v_perm_b32 v135, v188, v184, %[shi]\n\t"
      "v_perm_b32 v136, v165, v161, %[slo]\n\t"
      "v_perm_b32 v140, v165, v161, %[shi]\n\t"
      "v_perm_b32 v137, v173, v169, %[slo]\n\t"
      "v_perm_b32 v141, v173, v169, %[shi]\n\t"
      "v_perm_b32 v138, v181, v177, %[slo]\n\t"
      "v_perm_b32 v142, v181, v177, %[shi]\n\t"
      "v_perm_b32 v139, v189, v185, %[slo]\n\t"
      "v_perm_b32 v143, v189, v185, %[shi]\n\t"
      "v_perm_b32 v144, v166, v162, %[slo]\n\t"
      "v_perm_b32 v148, v166, v162, %[shi]\n\t"
      "v_perm_b32 v145, v174, v170, %[slo]\n\t"
      "v_perm_b32 v149, v174, v170, %[shi]\n\t"
      "v_perm_b32 v146, v182, v178, %[slo]\n\t"
      "v_perm_b32 v150, v182, v178, %[shi]\n\t"
      "v_perm_b32 v147, v190, v186, %[slo]\n\t"
      "v_perm_b32 v151, v190, v186, %[shi]\n\t"
      "v_perm_b32 v152, v167, v163, %[slo]\n\t"
      "v_perm_b32 v156, v167, v163, %[shi]\n\t"
      "v_perm_b32 v153, v175, v171, %[slo]\n\t"
      "v_perm_b32 v157, v175, v171, %[shi]\n\t"
      "v_perm_b32 v154, v183, v179, %[slo]\n\t"
      "v_perm_b32 v158, v183, v179, %[shi]\n\t"
      "v_perm_b32 v155, v191, v187, %[slo]\n\t"
      "v_perm_b32 v159, v191, v187, %[shi]\n\t"
      "v_cndmask_b32 v26, 0, v38, %[m0]\n\t"
      "v_cndmask_b32 v27, 0, v39, %[m0]\n\t"
      "v_cndmask_b32 v28, 0, v24, %[m0]\n\t"
      "v_cndmask_b32 v29, 0, v25, %[m0]\n\t"
      "v_cndmask_b32 v30, 0, v38, %[m1]\n\t"
      "v_cndmask_b32 v31, 0, v39, %[m1]\n\t"
      "v_cndmask_b32 v32, 0, v24, %[m1]\n\t"
      "v_cndmask_b32 v33, 0, v25, %[m1]\n\t"
      "v_mfma_f32_16x16x32_bf16 v[12:15], v[128:131], v[26:29], v[12:15]\n\t"
      "v_mfma_f32_16x16x32_bf16 v[16:19], v[144:147], v[26:29], v[16:19]\n\t"
      "v_cndmask_b32 v26, 0, v38, %[m2]\n\t"
      "v_cndmask_b32 v27, 0, v39, %[m2]\n\t"
      "v_cndmask_b32 v28, 0, v24, %[m2]\n\t"
      "v_cndmask_b32 v29, 0, v25, %[m2]\n\t"
      "v_mfma_f32_16x16x32_bf16 v[12:15], v[132:135], v[30:33], v[12:15]\n\t"
      "v_mfma_f32_16x16x32_bf16 v[16:19], v[148:151], v[30:33], v[16:19]\n\t"
      "v_cndmask_b32 v30, 0, v38, %[m3]\n\t"
      "v_cndmask_b32 v31, 0, v39, %[m3]\n\t"
      "v_cndmask_b32 v32, 0, v24, %[m3]\n\t"
      "v_cndmask_b32 v33, 0, v25, %[m3]\n\t"
      "v_mfma_f32_16x16x32_bf16 v[12:15], v[136:139], v[26:29], v[12:15]\n\t"
      "v_mfma_f32_16x16x32_bf16 v[16:19], v[152:155], v[26:29], v[16:19]\n\t"
      "v_mfma_f32_16x16x32_bf16 v[12:15], v[140:143], v[30:33], v[12:15]\n\t"
      "v_mfma_f32_16x16x32_bf16 v[16:19], v[156:159], v[30:33], v[16:19]\n\t"
      "v_mfma_f32_16x16x32_bf16 v[160:163], a[64:67], v[8:11], 0\n\t"
      "v_mfma_f32_16x16x32_bf16 v[164:167], a[68:71], v[8:11], 0\n\t"
      "v_mfma_f32_16x16x32_bf16 v[168:171], a[72:75], v[8:11], 0\n\t"
      "v_mfma_f32_16x16x32_bf16 v[172:175], a[76:79], v[8:11], 0\n\t"
      "v_mfma_f32_16x16x32_bf16 v[176:179], a[80:83], v[8:11], 0\n\t"
      "v_mfma_f32_16x16x32_bf16 v[180:183], a[84:87], v[8:11], 0\n\t"
      "v_mfma_f32_16x16x32_bf16 v[184:187], a[88:91], v[8:11], 0\n\t"
      "v_mfma_f32_16x16x32_bf16 v[188:191], a[92:95], v[8:11], 0\n\t"
      "s_nop 1\n\t"
      "v_mov_b32_dpp v26, v160 quad_perm:[0,0,0,0] row_mask:0xf bank_mask:0xf\n\t"
      "v_mov_b32_dpp v27, v164 quad_perm:[0,0,0,0] row_mask:0xf bank_mask:0xf\n\t"
      "v_mov_b32_dpp v28, v168 quad_perm:[0,0,0,0] row_mask:0xf bank_mask:0xf\n\t"
      "v_mov_b32_dpp v29, v172 quad_perm:[0,0,0,0] row_mask:0xf bank_mask:0xf\n\t"
      "v_add_f32_dpp v26, v161, v26 quad_perm:[1,1,1,1] row_mask:0xf bank_mask:0xf\n\t"
      "v_add_f32_dpp v27, v165, v27 quad_perm:[1,1,1,1] row_mask:0xf bank_mask:0xf\n\t"
      "v_add_f32_dpp v28, v169, v28 quad_perm:[1,1,1,1] row_mask:0xf bank_mask:0xf\n\t"
      "v_add_f32_dpp v29, v173, v29 quad_perm:[1,1,1,1] row_mask:0xf bank_mask:0xf\n\t"
      "v_add_f32_dpp v26, v162, v26 quad_perm:[2,2,2,2] row_mask:0xf bank_mask:0xf\n\t"
      "v_add_f32_dpp v27, v166, v27 quad_perm:[2,2,2,2] row_mask:0xf bank_mask:0xf\n\t"
      "v_add_f32_dpp v28, v170, v28 quad_perm:[2,2,2,2] row_mask:0xf bank_mask:0xf\n\t"
      "v_add_f32_dpp v29, v174, v29 quad_perm:[2,2,2,2] row_mask:0xf bank_mask:0xf\n\t"
      "v_add_f32_dpp v26, v163, v26 quad_perm:[3,3,3,3] row_mask:0xf bank_mask:0xf\n\t"
      "v_add_f32_dpp v27, v167, v27 quad_perm:[3,3,3,3] row_mask:0xf bank_mask:0xf\n\t"
      "v_add_f32_dpp v28, v171, v28 quad_perm:[3,3,3,3] row_mask:0xf bank_mask:0xf\n\t"
      "v_add_f32_dpp v29, v175, v29 quad_perm:[3,3,3,3] row_mask:0xf bank_mask:0xf\n\t"
      "v_mov_b32_dpp v30, v176 quad_perm:[0,0,0,0] row_mask:0xf bank_mask:0xf\n\t"
      "v_mov_b32_dpp v31, v180 quad_perm:[0,0,0,0] row_mask:0xf bank_mask:0xf\n\t"
      "v_mov_b32_dpp v32, v184 quad_perm:[0,0,0,0] row_mask:0xf bank_mask:0xf\n\t"
      "v_mov_b32_dpp v33, v188 quad_perm:[0,0,0,0] row_mask:0xf bank_mask:0xf\n\t"
      "v_add_f32_dpp v30, v177, v30 quad_perm:[1,1,1,1] row_mask:0xf bank_mask:0xf\n\t"
      "v_add_f32_dpp v31, v181, v31 quad_perm:[1,1,1,1] row_mask:0xf bank_mask:0xf\n\t"
      "v_add_f32_dpp v32, v185, v32 quad_perm:[1,1,1,1] row_mask:0xf bank_mask:0xf\n\t"
      "v_add_f32_dpp v33, v189, v33 quad_perm:[1,1,1,1] row_mask:0xf bank_mask:0xf\n\t"
      "v_add_f32_dpp v30, v178, v30 quad_perm:[2,2,2,2] row_mask:0xf bank_mask:0xf\n\t"
      "v_add_f32_dpp v31, v182, v31 quad_perm:[2,2,2,2] row_mask:0xf bank_mask:0xf\n\t"
      "v_add_f32_dpp v32, v186, v32 quad_perm:[2,2,2,2] row_mask:0xf bank_mask:0xf\n\t"
      "v_add_f32_dpp v33, v190, v33 quad_perm:[2,2,2,2] row_mask:0xf bank_mask:0xf\n\t"
      "v_add_f32_dpp v30, v179, v30 quad_perm:[3,3,3,3] row_mask:0xf bank_mask:0xf\n\t"
      "v_add_f32_dpp v31, v183, v31 quad_perm:[3,3,3,3] row_mask:0xf bank_mask:0xf\n\t"
      "v_add_f32_dpp v32, v187, v32 quad_perm:[3,3,3,3] row_mask:0xf bank_mask:0xf\n\t"
      "v_add_f32_dpp v33, v191, v33 quad_perm:[3,3,3,3] row_mask:0xf bank_mask:0xf\n\t"
      "v_mfma_f32_16x16x32_bf16 v[160:163], a[96:99], v[8:11], 0\n\t"
      "v_mfma_f32_16x16x32_bf16 v[164:167], a[100:103], v[8:11], 0\n\t"
      "v_mfma_f32_16x16x32_bf16 v[168:171], a[104:107], v[8:11], 0\n\t"
      "v_mfma_f32_16x16x32_bf16 v[172:175], a[108:111], v[8:11], 0\n\t"
      "v_mfma_f32_16x16x32_bf16 v[176:179], a[112:115], v[8:11], 0\n\t"
      "v_mfma_f32_16x16x32_bf16 v[180:183], a[116:119], v[8:11], 0\n\t"
      "v_mfma_f32_16x16x32_bf16 v[184:187], a[120:123], v[8:11], 0\n\t"
      "v_mfma_f32_16x16x32_bf16 v[188:191], a[124:127], v[8:11], 0\n\t"
      "s_nop 1\n\t"
      "v_mov_b32_dpp v160, v160 quad_perm:[0,0,0,0] row_mask:0xf bank_mask:0xf\n\t"
      "v_mov_b32_dpp v164, v164 quad_perm:[0,0,0,0] row_mask:0xf bank_mask:0xf\n\t"
      "v_mov_b32_dpp v168, v168 quad_perm:[0,0,0,0] row_mask:0xf bank_mask:0xf\n\t"
      "v_mov_b32_dpp v172, v172 quad_perm:[0,0,0,0] row_mask:0xf bank_mask:0xf\n\t"
      "v_add_f32_dpp v160, v161, v160 quad_perm:[1,1,1,1] row_mask:0xf bank_mask:0xf\n\t"
      "v_add_f32_dpp v164, v165, v164 quad_perm:[1,1,1,1] row_mask:0xf bank_mask:0xf\n\t"
      "v_add_f32_dpp v168, v169, v168 quad_perm:[1,1,1,1] row_mask:0xf bank_mask:0xf\n\t"
      "v_add_f32_dpp v172, v173, v172 quad_perm:[1,1,1,1] row_mask:0xf bank_mask:0xf\n\t"
      "v_add_f32_dpp v160, v162, v160 quad_perm:[2,2,2,2] row_mask:0xf bank_mask:0xf\n\t"
      "v_add_f32_dpp v164, v166, v164 quad_perm:[2,2,2,2] row_mask:0xf bank_mask:0xf\n\t"
      "v_add_f32_dpp v168, v170, v168 quad_perm:[2,2,2,2] row_mask:0xf bank_mask:0xf\n\t"
      "v_add_f32_dpp v172, v174, v172 quad_perm:[2,2,2,2] row_mask:0xf bank_mask:0xf\n\t"
      "v_add_f32_dpp v160, v163, v160 quad_perm:[3,3,3,3] row_mask:0xf bank_mask:0xf\n\t"
      "v_add_f32_dpp v164, v167, v164 quad_perm:[3,3,3,3] row_mask:0xf bank_mask:0xf\n\t"
      "v_add_f32_dpp v168, v171, v168 quad_perm:[3,3,3,3] row_mask:0xf bank_mask:0xf\n\t"
      "v_add_f32_dpp v172, v175, v172 quad_perm:[3,3,3,3] row_mask:0xf bank_mask:0xf\n\t"
      "v_mov_b32_dpp v176, v176 quad_perm:[0,0,0,0] row_mask:0xf bank_mask:0xf\n\t"
      "v_mov_b32_dpp v180, v180 quad_perm:[0,0,0,0] row_mask:0xf bank_mask:0xf\n\t"
      "v_mov_b32_dpp v184, v184 quad_perm:[0,0,0,0] row_mask:0xf bank_mask:0xf\n\t"
      "v_mov_b32_dpp v188, v188 quad_perm:[0,0,0,0] row_mask:0xf bank_mask:0xf\n\t"
      "v_add_f32_dpp v176, v177, v176 quad_perm:[1,1,1,1] row_mask:0xf bank_mask:0xf\n\t"
      "v_add_f32_dpp v180, v181, v180 quad_perm:[1,1,1,1] row_mask:0xf bank_mask:0xf\n\t"
      "v_add_f32_dpp v184, v185, v184 quad_perm:[1,1,1,1] row_mask:0xf bank_mask:0xf\n\t"
      "v_add_f32_dpp v188, v189, v188 quad_perm:[1,1,1,1] row_mask:0xf bank_mask:0xf\n\t"
      "v_add_f32_dpp v176, v178, v176 quad_perm:[2,2,2,2] row_mask:0xf bank_mask:0xf\n\t"
      "v_add_f32_dpp v180, v182, v180 quad_perm:[2,2,2,2] row_mask:0xf bank_mask:0xf\n\t"
      "v_add_f32_dpp v184, v186, v184 quad_perm:[2,2,2,2] row_mask:0xf bank_mask:0xf\n\t"
      "v_add_f32_dpp v188, v190, v188 quad_perm:[2,2,2,2] row_mask:0xf bank_mask:0xf\n\t"
      "v_add_f32_dpp v176, v179, v176 quad_perm:[3,3,3,3] row_mask:0xf bank_mask:0xf\n\t"
      "v_add_f32_dpp v180, v183, v180 quad_perm:[3,3,3,3] row_mask:0xf bank_mask:0xf\n\t"
      "v_add_f32_dpp v184, v187, v184 quad_perm:[3,3,3,3] row_mask:0xf bank_mask:0xf\n\t"
      "v_add_f32_dpp v188, v191, v188 quad_perm:[3,3,3,3] row_mask:0xf bank_mask:0xf\n\t"
      "v_max3_f32 v22, v26, v27, v20\n\t"
      "v_max3_f32 v23, v28, v29, v30\n\t"
      "v_max3_f32 v22, v22, v31, v32\n\t"
      "v_max3_f32 v23, v23, v33, v160\n\t"
      "v_max3_f32 v22, v22, v164, v168\n\t"
      "v_max3_f32 v23, v23, v172, v176\n\t"
      "v_max3_f32 v22, v22, v180, v184\n\t"
      "v_max3_f32 v22, v22, v23, v188\n\t"
      "v_mov_b32 v23, v22\n\t"
      "s_nop 1\n\t"
      "v_permlane16_swap_b32 v22, v23\n\t"
      "s_nop 0\n\t"
      "v_max_f32 v22, v22, v23\n\t"
      "v_mov_b32 v23, v22\n\t"
      "s_nop 1\n\t"
      "v_permlane32_swap_b32 v22, v23\n\t"
      "s_nop 0\n\t"
      "v_max_f32 v22, v22, v23\n\t"
      "v_sub_f32 v23, v20, v22\n\t"
      "v_mul_f32 v24, %[nc2], v22\n\t"
      "v_mul_f32 v23, %[c2], v23\n\t"
      "v_mov_b32 v20, v22\n\t"
      "v_exp_f32 v23, v23\n\t"
      "v_fma_f32 v26, v26, %[c2], v24\n\t"
      "v_fma_f32 v27, v27, %[c2], v24\n\t"
      "v_fma_f32 v28, v28, %[c2], v24\n\t"
      "v_fma_f32 v29, v29, %[c2], v24\n\t"
      "v_fma_f32 v30, v30, %[c2], v24\n\t"
      "v_fma_f32 v31, v31, %[c2], v24\n\t"
      "v_fma_f32 v32, v32, %[c2], v24\n\t"
      "v_fma_f32 v33, v33, %[c2], v24\n\t"
      "v_fma_f32 v160, v160, %[c2], v24\n\t"
      "v_fma_f32 v164, v164, %[c2], v24\n\t"
      "v_fma_f32 v168, v168, %[c2], v24\n\t"
      "v_fma_f32 v172, v172, %[c2], v24\n\t"
      "v_fma_f32 v176, v176, %[c2], v24\n\t"
      "v_fma_f32 v180, v180, %[c2], v24\n\t"
      "v_fma_f32 v184, v184, %[c2], v24\n\t"
      "v_fma_f32 v188, v188, %[c2], v24\n\t"
      "v_exp_f32 v26, v26\n\t"
      "v_mul_f32 v12, v23, v12\n\t"
      "v_exp_f32 v27, v27\n\t"
      "v_mul_f32 v13, v23, v13\n\t"
      "v_exp_f32 v28, v28\n\t"
      "v_mul_f32 v14, v23, v14\n\t"
      "v_exp_f32 v29, v29\n\t"
      "v_mul_f32 v15, v23, v15\n\t"
      "v_exp_f32 v30, v30\n\t"
      "v_mul_f32 v16, v23, v16\n\t"
      "v_exp_f32 v31, v31\n\t"
      "v_mul_f32 v17, v23, v17\n\t"
      "v_exp_f32 v32, v32\n\t"
      "v_mul_f32 v18, v23, v18\n\t"
      "v_exp_f32 v33, v33\n\t"
      "v_mul_f32 v19, v23, v19\n\t"
      "v_exp_f32 v160, v160\n\t"
      "v_exp_f32 v164, v164\n\t"
      "v_exp_f32 v168, v168\n\t"
      "v_exp_f32 v172, v172\n\t"
      "v_exp_f32 v176, v176\n\t"
      "v_exp_f32 v180, v180\n\t"
      "v_exp_f32 v184, v184\n\t"
      "v_exp_f32 v188, v188\n\t"
      "v_mul_f32 v21, v21, v23\n\t"
      "v_add_f32 v22, v26, v27\n\t"
      "v_add_f32 v23, v28, v29\n\t"
      "v_add_f32 v22, v22, v30\n\t"
      "v_add_f32 v23, v23, v31\n\t"
      "v_add_f32 v22, v22, v32\n\t"
      "v_add_f32 v23, v23, v33\n\t"
      "v_add_f32 v22, v22, v160\n\t"
      "v_add_f32 v23, v23, v164\n\t"
      "v_add_f32 v22, v22, v168\n\t"
      "v_add_f32 v23, v23, v172\n\t"
      "v_add_f32 v22, v22, v176\n\t"
      "v_add_f32 v23, v23, v180\n\t"
      "v_add_f32 v22, v22, v184\n\t"
      "v_add_f32 v23, v23, v188\n\t"
      "v_add_f32 v21, v21, v22\n\t"
      "v_cvt_pk_bf16_f32 v34, v26, v27\n\t"
      "v_cvt_pk_bf16_f32 v35, v28, v29\n\t"
      "v_cvt_pk_bf16_f32 v36, v30, v31\n\t"
      "v_cvt_pk_bf16_f32 v37, v32, v33\n\t"
      "v_add_f32 v21, v21, v23\n\t"
      "v_cvt_pk_bf16_f32 v38, v160, v164\n\t"
      "v_cvt_pk_bf16_f32 v39, v168, v172\n\t"
      "v_cvt_pk_bf16_f32 v24, v176, v180\n\t"
      "v_cvt_pk_bf16_f32 v25, v184, v188\n\t"
      "1:\n\t"
      "s_memrealtime s[96:97]\n\t"
      "s_waitcnt lgkmcnt(0)\n\t"
      "s_sub_u32 %[so0], s96, %[due]\n\t"
      "s_cmp_lt_i32 %[so0], 0\n\t"
      "s_cbranch_scc0 2f\n\t"
      "s_sleep 1\n\t"
      "s_branch 1b\n\t"
      "2:\n\t"
      "s_add_u32 %[due], %[due], %[dlh]\n\t"
      "s_add_u32 %[so0], %[rot], 32768\n\t"
      "s_and_b32 %[so0], %[so0], 0x1ffff\n\t"
      "s_add_u32 %[so1], %[so0], 4096\n\t"
      "buffer_load_dwordx4 a[128:131], %[koff], %[rk], %[so0] offen offset:0 nt\n\t"
      "buffer_load_dwordx4 a[132:135], %[koff], %[rk], %[so0] offen offset:1024 nt\n\t"
      "buffer_load_dwordx4 a[136:139], %[koff], %[rk], %[so0] offen offset:2048 nt\n\t"
      "buffer_load_dwordx4 a[140:143], %[koff], %[rk], %[so0] offen offset:3072 nt\n\t"
      "buffer_load_dwordx4 a[144:147], %[koff], %[rk], %[so1] offen offset:0 nt\n\t"
      "buffer_load_dwordx4 a[148:151], %[koff], %[rk], %[so1] offen offset:1024 nt\n\t"
      "buffer_load_dwordx4 a[152:155], %[koff], %[rk], %[so1] offen offset:2048 nt\n\t"
      "buffer_load_dwordx4 a[156:159], %[koff], %[rk], %[so1] offen offset:3072 nt\n\t"
      "s_add_u32 %[so0], %[rot], 40960\n\t"
      "s_and_b32 %[so0], %[so0], 0x1ffff\n\t"
      "s_add_u32 %[so1], %[so0], 4096\n\t"
      "buffer_load_dwordx4 a[160:163], %[koff], %[rk], %[so0] offen offset:0 nt\n\t"
      "buffer_load_dwordx4 a[164:167], %[koff], %[rk], %[so0] offen offset:1024 nt\n\t"
      "buffer_load_dwordx4 a[168:171], %[koff], %[rk], %[so0] offen offset:2048 nt\n\t"
      "buffer_load_dwordx4 a[172:175], %[koff], %[rk], %[so0] offen offset:3072 nt\n\t"
      "buffer_load_dwordx4 a[176:179], %[koff], %[rk], %[so1] offen offset:0 nt\n\t"
      "buffer_load_dwordx4 a[180:183], %[koff], %[rk], %[so1] offen offset:1024 nt\n\t"
      "buffer_load_dwordx4 a[184:187], %[koff], %[rk], %[so1] offen offset:2048 nt\n\t"
      "buffer_load_dwordx4 a[188:191], %[koff], %[rk], %[so1] offen offset:3072 nt\n\t"
      "buffer_load_dwordx4 v[88:91], %[wgoff], %[rwg], 0 offen\n\t"
      "s_waitcnt vmcnt(21)\n\t"
      "v_perm_b32 v160, v196, v192, %[slo]\n\t"
      "v_perm_b32 v164, v196, v192, %[shi]\n\t"
      "v_perm_b32 v161, v204, v200, %[slo]\n\t"
      "v_perm_b32 v165, v204, v200, %[shi]\n\t"
      "v_perm_b32 v162, v212, v208, %[slo]\n\t"
      "v_perm_b32 v166, v212, v208, %[shi]\n\t"
      "v_perm_b32 v163, v220, v216, %[slo]\n\t"
      "v_perm_b32 v167, v220, v216, %[shi]\n\t"
      "v_perm_b32 v168, v197, v193, %[slo]\n\t"
      "v_perm_b32 v172, v197, v193, %[shi]\n\t"
      "v_perm_b32 v169, v205, v201, %[slo]\n\t"
      "v_perm_b32 v173, v205, v201, %[shi]\n\t"
      "v_perm_b32 v170, v213, v209, %[slo]\n\t"
      "v_perm_b32 v174, v213, v209, %[shi]\n\t"
      "v_perm_b32 v171, v221, v217, %[slo]\n\t"
      "v_perm_b32 v175, v221, v217, %[shi]\n\t"
      "v_perm_b32 v176, v198, v194, %[slo]\n\t"
      "v_perm_b32 v180, v198, v194, %[shi]\n\t"
      "v_perm_b32 v177, v206, v202, %[slo]\n\t"
      "v_perm_b32 v181, v206, v202, %[shi]\n\t"
      "v_perm_b32 v178, v214, v210, %[slo]\n\t"
      "v_perm_b32 v182, v214, v210, %[shi]\n\t"
      "v_perm_b32 v179, v222, v218, %[slo]\n\t"
      "v_perm_b32 v183, v222, v218, %[shi]\n\t"
      "v_perm_b32 v184, v199, v195, %[slo]\n\t"
      "v_perm_b32 v188, v199, v195, %[shi]\n\t"
      "v_perm_b32 v185, v207, v203, %[slo]\n\t"
      "v_perm_b32 v189, v207, v203, %[shi]\n\t"
      "v_perm_b32 v186, v215, v211, %[slo]\n\t"
      "v_perm_b32 v190, v215, v211, %[shi]\n\t"
      "v_perm_b32 v187, v223, v219, %[slo]\n\t"
      "v_perm_b32 v191, v223, v219, %[shi]\n\t"
      "v_cndmask_b32 v26, 0, v34, %[m0]\n\t"
      "v_cndmask_b32 v27, 0, v35, %[m0]\n\t"
      "v_cndmask_b32 v28, 0, v36, %[m0]\n\t"
      "v_cndmask_b32 v29, 0, v37, %[m0]\n\t"
      "v_cndmask_b32 v30, 0, v34, %[m1]\n\t"
      "v_cndmask_b32 v31, 0, v35, %[m1]\n\t"
      "v_cndmask_b32 v32, 0, v36, %[m1]\n\t"
      "v_cndmask_b32 v33, 0, v37, %[m1]\n\t"
      "v_mfma_f32_16x16x32_bf16 v[12:15], v[160:163], v[26:29], v[12:15]\n\t"
      "v_mfma_f32_16x16x32_bf16 v[16:19], v[176:179], v[26:29], v[16:19]\n\t"
      "v_cndmask_b32 v26, 0, v34, %[m2]\n\t"
      "v_cndmask_b32 v27, 0, v35, %[m2]\n\t"
      "v_cndmask_b32 v28, 0, v36, %[m2]\n\t"
      "v_cndmask_b32 v29, 0, v37, %[m2]\n\t"
      "v_mfma_f32_16x16x32_bf16 v[12:15], v[164:167], v[30:33], v[12:15]\n\t"
      "v_mfma_f32_16x16x32_bf16 v[16:19], v[180:183], v[30:33], v[16:19]\n\t"
      "v_cndmask_b32 v30, 0, v34, %[m3]\n\t"
      "v_cndmask_b32 v31, 0, v35, %[m3]\n\t"
      "v_cndmask_b32 v32, 0, v36, %[m3]\n\t"
      "v_cndmask_b32 v33, 0, v37, %[m3]\n\t"
      "v_mfma_f32_16x16x32_bf16 v[12:15], v[168:171], v[26:29], v[12:15]\n\t"
      "v_mfma_f32_16x16x32_bf16 v[16:19], v[184:187], v[26:29], v[16:19]\n\t"
      "v_mfma_f32_16x16x32_bf16 v[12:15], v[172:175], v[30:33], v[12:15]\n\t"
      "v_mfma_f32_16x16x32_bf16 v[16:19], v[188:191], v[30:33], v[16:19]\n\t"
      "v_perm_b32 v128, v228, v224, %[slo]\n\t"
      "v_perm_b32 v132, v228, v224, %[shi]\n\t"
      "v_perm_b32 v129, v236, v232, %[slo]\n\t"
      "v_perm_b32 v133, v236, v232, %[shi]\n\t"
      "v_perm_b32 v130, v244, v240, %[slo]\n\t"
      "v_perm_b32 v134, v244, v240, %[shi]\n\t"
      "v_perm_b32 v131, v252, v248, %[slo]\n\t"
      "v_perm_b32 v135, v252, v248, %[shi]\n\t"
      "v_perm_b32 v136, v229, v225, %[slo]\n\t"
      "v_perm_b32 v140, v229, v225, %[shi]\n\t"
      "v_perm_b32 v137, v237, v233, %[slo]\n\t"
      "v_perm_b32 v141, v237, v233, %[shi]\n\t"
      "v_perm_b32 v138, v245, v241, %[slo]\n\t"
      "v_perm_b32 v142, v245, v241, %[shi]\n\t"
      "v_perm_b32 v139, v253, v249, %[slo]\n\t"
      "v_perm_b32 v143, v253, v249, %[shi]\n\t"
      "v_perm_b32 v144, v230, v226, %[slo]\n\t"
      "v_perm_b32 v148, v230, v226, %[shi]\n\t"
      "v_perm_b32 v145, v238, v234, %[slo]\n\t"
      "v_perm_b32 v149, v238, v234, %[shi]\n\t"
      "v_perm_b32 v146, v246, v242, %[slo]\n\t"
      "v_perm_b32 v150, v246, v242, %[shi]\n\t"
      "v_perm_b32 v147, v254, v250, %[slo]\n\t"
      "v_perm_b32 v151, v254, v250, %[shi]\n\t"
      "v_perm_b32 v152, v231, v227, %[slo]\n\t"
      "v_perm_b32 v156, v231, v227, %[shi]\n\t"
      "v_perm_b32 v153, v239, v235, %[slo]\n\t"
      "v_perm_b32 v157, v239, v235, %[shi]\n\t"
      "v_perm_b32 v154, v247, v243, %[slo]\n\t"
      "v_perm_b32 v158, v247, v243, %[shi]\n\t"
      "v_perm_b32 v155, v255, v251, %[slo]\n\t"
      "v_perm_b32 v159, v255, v251, %[shi]\n\t"
      "v_cndmask_b32 v26, 0, v38, %[m0]\n\t"
      "v_cndmask_b32 v27, 0, v39, %[m0]\n\t"
      "v_cndmask_b32 v28, 0, v24, %[m0]\n\t"
      "v_cndmask_b32 v29, 0, v25, %[m0]\n\t"
      "v_cndmask_b32 v30, 0, v38, %[m1]\n\t"
      "v_cndmask_b32 v31, 0, v39, %[m1]\n\t"
      "v_cndmask_b32 v32, 0, v24, %[m1]\n\t"
      "v_cndmask_b32 v33, 0, v25, %[m1]\n\t"
      "v_mfma_f32_16x16x32_bf16 v[12:15], v[128:131], v[26:29], v[12:15]\n\t"
      "v_mfma_f32_16x16x32_bf16 v[16:19], v[144:147], v[26:29], v[16:19]\n\t"
      "v_cndmask_b32 v26, 0, v38, %[m2]\n\t"
      "v_cndmask_b32 v27, 0, v39, %[m2]\n\t"
      "v_cndmask_b32 v28, 0, v24, %[m2]\n\t"
      "v_cndmask_b32 v29, 0, v25, %[m2]\n\t"
      "v_mfma_f32_16x16x32_bf16 v[12:15], v[132:135], v[30:33], v[12:15]\n\t"
      "v_mfma_f32_16x16x32_bf16 v[16:19], v[148:151], v[30:33], v[16:19]\n\t"
      "v_cndmask_b32 v30, 0, v38, %[m3]\n\t"
      "v_cndmask_b32 v31, 0, v39, %[m3]\n\t"
      "v_cndmask_b32 v32, 0, v24, %[m3]\n\t"
      "v_cndmask_b32 v33, 0, v25, %[m3]\n\t"
      "v_mfma_f32_16x16x32_bf16 v[12:15], v[136:139], v[26:29], v[12:15]\n\t"
      "v_mfma_f32_16x16x32_bf16 v[16:19], v[152:155], v[26:29], v[16:19]\n\t"
      "v_mfma_f32_16x16x32_bf16 v[12:15], v[140:143], v[30:33], v[12:15]\n\t"
      "v_mfma_f32_16x16x32_bf16 v[16:19], v[156:159], v[30:33], v[16:19]\n\t"
      "1:\n\t"
      "s_memrealtime s[96:97]\n\t"
      "s_waitcnt lgkmcnt(0)\n\t"
      "s_sub_u32 %[so0], s96, %[due]\n\t"
      "s_cmp_lt_i32 %[so0], 0\n\t"
      "s_cbranch_scc0 2f\n\t"
      "s_sleep 1\n\t"
      "s_branch 1b\n\t"
      "2:\n\t"
      "s_add_u32 %[due], %[due], %[dlh]\n\t"
      "s_add_u32 %[so0], %[rot], 32768\n\t"
      "s_and_b32 %[so0], %[so0], 0x1ffff\n\t"
      "s_add_u32 %[so1], %[so0], 4096\n\t"
      "buffer_load_dwordx4 v[128:131], %[voff], %[rv], %[so0] offen offset:0 nt\n\t"
      "buffer_load_dwordx4 v[132:135], %[voff], %[rv], %[so0] offen offset:1024 nt\n\t"
      "buffer_load_dwordx4 v[136:139], %[voff], %[rv], %[so0] offen offset:2048 nt\n\t"
      "buffer_load_dwordx4 v[140:143], %[voff], %[rv], %[so0] offen offset:3072 nt\n\t"
      "buffer_load_dwordx4 v[144:147], %[voff], %[rv], %[so1] offen offset:0 nt\n\t"
      "buffer_load_dwordx4 v[148:151], %[voff], %[rv], %[so1] offen offset:1024 nt\n\t"
      "buffer_load_dwordx4 v[152:155], %[voff], %[rv], %[so1] offen offset:2048 nt\n\t"
      "buffer_load_dwordx4 v[156:159], %[voff], %[rv], %[so1] offen offset:3072 nt\n\t"
      "s_add_u32 %[so0], %[rot], 40960\n\t"
      "s_and_b32 %[so0], %[so0], 0x1ffff\n\t"
      "s_add_u32 %[so1], %[so0], 4096\n\t"
      "buffer_load_dwordx4 v[40:43], %[voff], %[rv], %[so0] offen offset:0 nt\n\t"
      "buffer_load_dwordx4 v[44:47], %[voff], %[rv], %[so0] offen offset:1024 nt\n\t"
      "buffer_load_dwordx4 v[48:51], %[voff], %[rv], %[so0] offen offset:2048 nt\n\t"
      "buffer_load_dwordx4 v[52:55], %[voff], %[rv], %[so0] offen offset:3072 nt\n\t"
      "buffer_load_dwordx4 v[56:59], %[voff], %[rv], %[so1] offen offset:0 nt\n\t"
      "buffer_load_dwordx4 v[60:63], %[voff], %[rv], %[so1] offen offset:1024 nt\n\t"
      "buffer_load_dwordx4 v[64:67], %[voff], %[rv], %[so1] offen offset:2048 nt\n\t"
      "buffer_load_dwordx4 v[68:71], %[voff], %[rv], %[so1] offen offset:3072 nt\n\t"
      "s_mov_b32 %[so0], 8192\n\t"
      "buffer_load_dword v73, %[wooff], %[rwo], %[so0] offen offset:0 nt\n\t"
      "buffer_load_dword v75, %[wooff], %[rwo], %[so0] offen offset:1024 nt\n\t"
      "buffer_load_dword v77, %[wooff], %[rwo], %[so0] offen offset:2048 nt\n\t"
      "buffer_load_dword v79, %[wooff], %[rwo], %[so0] offen offset:3072 nt\n\t"
      "s_waitcnt vmcnt(21)\n\t"
      "v_mfma_f32_16x16x32_bf16 v[224:227], a[128:131], v[8:11], 0\n\t"
      "v_mfma_f32_16x16x32_bf16 v[228:231], a[132:135], v[8:11], 0\n\t"
      "v_mfma_f32_16x16x32_bf16 v[232:235], a[136:139], v[8:11], 0\n\t"
      "v_mfma_f32_16x16x32_bf16 v[236:239], a[140:143], v[8:11], 0\n\t"
      "v_mfma_f32_16x16x32_bf16 v[240:243], a[144:147], v[8:11], 0\n\t"
      "v_mfma_f32_16x16x32_bf16 v[244:247], a[148:151], v[8:11], 0\n\t"
      "v_mfma_f32_16x16x32_bf16 v[248:251], a[152:155], v[8:11], 0\n\t"
      "v_mfma_f32_16x16x32_bf16 v[252:255], a[156:159], v[8:11], 0\n\t"
      "s_nop 1\n\t"
      "v_mov_b32_dpp v26, v224 quad_perm:[0,0,0,0] row_mask:0xf bank_mask:0xf\n\t"
      "v_mov_b32_dpp v27, v228 quad_perm:[0,0,0,0] row_mask:0xf bank_mask:0xf\n\t"
      "v_mov_b32_dpp v28, v232 quad_perm:[0,0,0,0] row_mask:0xf bank_mask:0xf\n\t"
      "v_mov_b32_dpp v29, v236 quad_perm:[0,0,0,0] row_mask:0xf bank_mask:0xf\n\t"
      "v_add_f32_dpp v26, v225, v26 quad_perm:[1,1,1,1] row_mask:0xf bank_mask:0xf\n\t"
      "v_add_f32_dpp v27, v229, v27 quad_perm:[1,1,1,1] row_mask:0xf bank_mask:0xf\n\t"
      "v_add_f32_dpp v28, v233, v28 quad_perm:[1,1,1,1] row_mask:0xf bank_mask:0xf\n\t"
      "v_add_f32_dpp v29, v237, v29 quad_perm:[1,1,1,1] row_mask:0xf bank_mask:0xf\n\t"
      "v_add_f32_dpp v26, v226, v26 quad_perm:[2,2,2,2] row_mask:0xf bank_mask:0xf\n\t"
      "v_add_f32_dpp v27, v230, v27 quad_perm:[2,2,2,2] row_mask:0xf bank_mask:0xf\n\t"
      "v_add_f32_dpp v28, v234, v28 quad_perm:[2,2,2,2] row_mask:0xf bank_mask:0xf\n\t"
      "v_add_f32_dpp v29, v238, v29 quad_perm:[2,2,2,2] row_mask:0xf bank_mask:0xf\n\t"
      "v_add_f32_dpp v26, v227, v26 quad_perm:[3,3,3,3] row_mask:0xf bank_mask:0xf\n\t"
      "v_add_f32_dpp v27, v231, v27 quad_perm:[3,3,3,3] row_mask:0xf bank_mask:0xf\n\t"
      "v_add_f32_dpp v28, v235, v28 quad_perm:[3,3,3,3] row_mask:0xf bank_mask:0xf\n\t"
      "v_add_f32_dpp v29, v239, v29 quad_perm:[3,3,3,3] row_mask:0xf bank_mask:0xf\n\t"
      "v_mov_b32_dpp v30, v240 quad_perm:[0,0,0,0] row_mask:0xf bank_mask:0xf\n\t"
      "v_mov_b32_dpp v31, v244 quad_perm:[0,0,0,0] row_mask:0xf bank_mask:0xf\n\t"
      "v_mov_b32_dpp v32, v248 quad_perm:[0,0,0,0] row_mask:0xf bank_mask:0xf\n\t"
      "v_mov_b32_dpp v33, v252 quad_perm:[0,0,0,0] row_mask:0xf bank_mask:0xf\n\t"
      "v_add_f32_dpp v30, v241, v30 quad_perm:[1,1,1,1] row_mask:0xf bank_mask:0xf\n\t"
      "v_add_f32_dpp v31, v245, v31 quad_perm:[1,1,1,1] row_mask:0xf bank_mask:0xf\n\t"
      "v_add_f32_dpp v32, v249, v32 quad_perm:[1,1,1,1] row_mask:0xf bank_mask:0xf\n\t"
      "v_add_f32_dpp v33, v253, v33 quad_perm:[1,1,1,1] row_mask:0xf bank_mask:0xf\n\t"
      "v_add_f32_dpp v30, v242, v30 quad_perm:[2,2,2,2] row_mask:0xf bank_mask:0xf\n\t"
      "v_add_f32_dpp v31, v246, v31 quad_perm:[2,2,2,2] row_mask:0xf bank_mask:0xf\n\t"
      "v_add_f32_dpp v32, v250, v32 quad_perm:[2,2,2,2] row_mask:0xf bank_mask:0xf\n\t"
      "v_add_f32_dpp v33, v254, v33 quad_perm:[2,2,2,2] row_mask:0xf bank_mask:0xf\n\t"
      "v_add_f32_dpp v30, v243, v30 quad_perm:[3,3,3,3] row_mask:0xf bank_mask:0xf\n\t"
      "v_add_f32_dpp v31, v247, v31 quad_perm:[3,3,3,3] row_mask:0xf bank_mask:0xf\n\t"
      "v_add_f32_dpp v32, v251, v32 quad_perm:[3,3,3,3] row_mask:0xf bank_mask:0xf\n\t"
      "v_add_f32_dpp v33, v255, v33 quad_perm:[3,3,3,3] row_mask:0xf bank_mask:0xf\n\t"
      "v_mfma_f32_16x16x32_bf16 v[224:227], a[160:163], v[8:11], 0\n\t"
      "v_mfma_f32_16x16x32_bf16 v[228:231], a[164:167], v[8:11], 0\n\t"
      "v_mfma_f32_16x16x32_bf16 v[232:235], a[168:171], v[8:11], 0\n\t"
      "v_mfma_f32_16x16x32_bf16 v[236:239], a[172:175], v[8:11], 0\n\t"
      "v_mfma_f32_16x16x32_bf16 v[240:243], a[176:179], v[8:11], 0\n\t"
      "v_mfma_f32_16x16x32_bf16 v[244:247], a[180:183], v[8:11], 0\n\t"
      "v_mfma_f32_16x16x32_bf16 v[248:251], a[184:187], v[8:11], 0\n\t"
      "v_mfma_f32_16x16x32_bf16 v[252:255], a[188:191], v[8:11], 0\n\t"
      "s_nop 1\n\t"
      "v_mov_b32_dpp v224, v224 quad_perm:[0,0,0,0] row_mask:0xf bank_mask:0xf\n\t"
      "v_mov_b32_dpp v228, v228 quad_perm:[0,0,0,0] row_mask:0xf bank_mask:0xf\n\t"
      "v_mov_b32_dpp v232, v232 quad_perm:[0,0,0,0] row_mask:0xf bank_mask:0xf\n\t"
      "v_mov_b32_dpp v236, v236 quad_perm:[0,0,0,0] row_mask:0xf bank_mask:0xf\n\t"
      "v_add_f32_dpp v224, v225, v224 quad_perm:[1,1,1,1] row_mask:0xf bank_mask:0xf\n\t"
      "v_add_f32_dpp v228, v229, v228 quad_perm:[1,1,1,1] row_mask:0xf bank_mask:0xf\n\t"
      "v_add_f32_dpp v232, v233, v232 quad_perm:[1,1,1,1] row_mask:0xf bank_mask:0xf\n\t"
      "v_add_f32_dpp v236, v237, v236 quad_perm:[1,1,1,1] row_mask:0xf bank_mask:0xf\n\t"
      "v_add_f32_dpp v224, v226, v224 quad_perm:[2,2,2,2] row_mask:0xf bank_mask:0xf\n\t"
      "v_add_f32_dpp v228, v230, v228 quad_perm:[2,2,2,2] row_mask:0xf bank_mask:0xf\n\t"
      "v_add_f32_dpp v232, v234, v232 quad_perm:[2,2,2,2] row_mask:0xf bank_mask:0xf\n\t"
      "v_add_f32_dpp v236, v238, v236 quad_perm:[2,2,2,2] row_mask:0xf bank_mask:0xf\n\t"
      "v_add_f32_dpp v224, v227, v224 quad_perm:[3,3,3,3] row_mask:0xf bank_mask:0xf\n\t"
      "v_add_f32_dpp v228, v231, v228 quad_perm:[3,3,3,3] row_mask:0xf bank_mask:0xf\n\t"
      "v_add_f32_dpp v232, v235, v232 quad_perm:[3,3,3,3] row_mask:0xf bank_mask:0xf\n\t"
      "v_add_f32_dpp v236, v239, v236 quad_perm:[3,3,3,3] row_mask:0xf bank_mask:0xf\n\t"
      "v_mov_b32_dpp v240, v240 quad_perm:[0,0,0,0] row_mask:0xf bank_mask:0xf\n\t"
      "v_mov_b32_dpp v244, v244 quad_perm:[0,0,0,0] row_mask:0xf bank_mask:0xf\n\t"
      "v_mov_b32_dpp v248, v248 quad_perm:[0,0,0,0] row_mask:0xf bank_mask:0xf\n\t"
      "v_mov_b32_dpp v252, v252 quad_perm:[0,0,0,0] row_mask:0xf bank_mask:0xf\n\t"
      "v_add_f32_dpp v240, v241, v240 quad_perm:[1,1,1,1] row_mask:0xf bank_mask:0xf\n\t"
      "v_add_f32_dpp v244, v245, v244 quad_perm:[1,1,1,1] row_mask:0xf bank_mask:0xf\n\t"
      "v_add_f32_dpp v248, v249, v248 quad_perm:[1,1,1,1] row_mask:0xf bank_mask:0xf\n\t"
      "v_add_f32_dpp v252, v253, v252 quad_perm:[1,1,1,1] row_mask:0xf bank_mask:0xf\n\t"
      "v_add_f32_dpp v240, v242, v240 quad_perm:[2,2,2,2] row_mask:0xf bank_mask:0xf\n\t"
      "v_add_f32_dpp v244, v246, v244 quad_perm:[2,2,2,2] row_mask:0xf bank_mask:0xf\n\t"
      "v_add_f32_dpp v248, v250, v248 quad_perm:[2,2,2,2] row_mask:0xf bank_mask:0xf\n\t"
      "v_add_f32_dpp v252, v254, v252 quad_perm:[2,2,2,2] row_mask:0xf bank_mask:0xf\n\t"
      "v_add_f32_dpp v240, v243, v240 quad_perm:[3,3,3,3] row_mask:0xf bank_mask:0xf\n\t"
      "v_add_f32_dpp v244, v247, v244 quad_perm:[3,3,3,3] row_mask:0xf bank_mask:0xf\n\t"
      "v_add_f32_dpp v248, v251, v248 quad_perm:[3,3,3,3] row_mask:0xf bank_mask:0xf\n\t"
      "v_add_f32_dpp v252, v255, v252 quad_perm:[3,3,3,3] row_mask:0xf bank_mask:0xf\n\t"
      "v_max3_f32 v22, v26, v27, v20\n\t"
      "v_max3_f32 v23, v28, v29, v30\n\t"
      "v_max3_f32 v22, v22, v31, v32\n\t"
      "v_max3_f32 v23, v23, v33, v224\n\t"
      "v_max3_f32 v22, v22, v228, v232\n\t"
      "v_max3_f32 v23, v23, v236, v240\n\t"
      "v_max3_f32 v22, v22, v244, v248\n\t"
      "v_max3_f32 v22, v22, v23, v252\n\t"
      "v_mov_b32 v23, v22\n\t"
      "s_nop 1\n\t"
      "v_permlane16_swap_b32 v22, v23\n\t"
      "s_nop 0\n\t"
      "v_max_f32 v22, v22, v23\n\t"
      "v_mov_b32 v23, v22\n\t"
      "s_nop 1\n\t"
      "v_permlane32_swap_b32 v22, v23\n\t"
      "s_nop 0\n\t"
      "v_max_f32 v22, v22, v23\n\t"
      "v_sub_f32 v23, v20, v22\n\t"
      "v_mul_f32 v24, %[nc2], v22\n\t"
      "v_mul_f32 v23, %[c2], v23\n\t"
      "v_mov_b32 v20, v22\n\t"
      "v_exp_f32 v23, v23\n\t"
      "v_fma_f32 v26, v26, %[c2], v24\n\t"
      "v_fma_f32 v27, v27, %[c2], v24\n\t"
      "v_fma_f32 v28, v28, %[c2], v24\n\t"
      "v_fma_f32 v29, v29, %[c2], v24\n\t"
      "v_fma_f32 v30, v30, %[c2], v24\n\t"
      "v_fma_f32 v31, v31, %[c2], v24\n\t"
      "v_fma_f32 v32, v32, %[c2], v24\n\t"
      "v_fma_f32 v33, v33, %[c2], v24\n\t"
      "v_fma_f32 v224, v224, %[c2], v24\n\t"
      "v_fma_f32 v228, v228, %[c2], v24\n\t"
      "v_fma_f32 v232, v232, %[c2], v24\n\t"
      "v_fma_f32 v236, v236, %[c2], v24\n\t"
      "v_fma_f32 v240, v240, %[c2], v24\n\t"
      "v_fma_f32 v244, v244, %[c2], v24\n\t"
      "v_fma_f32 v248, v248, %[c2], v24\n\t"
      "v_fma_f32 v252, v252, %[c2], v24\n\t"
      "v_exp_f32 v26, v26\n\t"
      "v_mul_f32 v12, v23, v12\n\t"
      "v_exp_f32 v27, v27\n\t"
      "v_mul_f32 v13, v23, v13\n\t"
      "v_exp_f32 v28, v28\n\t"
      "v_mul_f32 v14, v23, v14\n\t"
      "v_exp_f32 v29, v29\n\t"
      "v_mul_f32 v15, v23, v15\n\t"
      "v_exp_f32 v30, v30\n\t"
      "v_mul_f32 v16, v23, v16\n\t"
      "v_exp_f32 v31, v31\n\t"
      "v_mul_f32 v17, v23, v17\n\t"
      "v_exp_f32 v32, v32\n\t"
      "v_mul_f32 v18, v23, v18\n\t"
      "v_exp_f32 v33, v33\n\t"
      "v_mul_f32 v19, v23, v19\n\t"
      "v_exp_f32 v224, v224\n\t"
      "v_exp_f32 v228, v228\n\t"
      "v_exp_f32 v232, v232\n\t"
      "v_exp_f32 v236, v236\n\t"
      "v_exp_f32 v240, v240\n\t"
      "v_exp_f32 v244, v244\n\t"
      "v_exp_f32 v248, v248\n\t"
      "v_exp_f32 v252, v252\n\t"
      "v_mul_f32 v21, v21, v23\n\t"
      "v_add_f32 v22, v26, v27\n\t"
      "v_add_f32 v23, v28, v29\n\t"
      "v_add_f32 v22, v22, v30\n\t"
      "v_add_f32 v23, v23, v31\n\t"
      "v_add_f32 v22, v22, v32\n\t"
      "v_add_f32 v23, v23, v33\n\t"
      "v_add_f32 v22, v22, v224\n\t"
      "v_add_f32 v23, v23, v228\n\t"
      "v_add_f32 v22, v22, v232\n\t"
      "v_add_f32 v23, v23, v236\n\t"
      "v_add_f32 v22, v22, v240\n\t"
      "v_add_f32 v23, v23, v244\n\t"
      "v_add_f32 v22, v22, v248\n\t"
      "v_add_f32 v23, v23, v252\n\t"
      "v_add_f32 v21, v21, v22\n\t"
      "v_cvt_pk_bf16_f32 v34, v26, v27\n\t"
      "v_cvt_pk_bf16_f32 v35, v28, v29\n\t"
      "v_cvt_pk_bf16_f32 v36, v30, v31\n\t"
      "v_cvt_pk_bf16_f32 v37, v32, v33\n\t"
      "v_add_f32 v21, v21, v23\n\t"
      "v_cvt_pk_bf16_f32 v38, v224, v228\n\t"
      "v_cvt_pk_bf16_f32 v39, v232, v236\n\t"
      "v_cvt_pk_bf16_f32 v24, v240, v244\n\t"
      "v_cvt_pk_bf16_f32 v25, v248, v252\n\t"
      "1:\n\t"
      "s_memrealtime s[96:97]\n\t"
      "s_waitcnt lgkmcnt(0)\n\t"
      "s_sub_u32 %[so0], s96, %[due]\n\t"
      "s_cmp_lt_i32 %[so0], 0\n\t"
      "s_cbranch_scc0 2f\n\t"
      "s_sleep 1\n\t"
      "s_branch 1b\n\t"
      "2:\n\t"
      "s_add_u32 %[due], %[due], %[dlh]\n\t"
      "s_add_u32 %[so0], %[rot], 49152\n\t"
      "s_and_b32 %[so0], %[so0], 0x1ffff\n\t"
      "s_add_u32 %[so1], %[so0], 4096\n\t"
      "buffer_load_dwordx4 a[192:195], %[koff], %[rk], %[so0] offen offset:0 nt\n\t"
      "buffer_load_dwordx4 a[196:199], %[koff], %[rk], %[so0] offen offset:1024 nt\n\t"
      "buffer_load_dwordx4 a[200:203], %[koff], %[rk], %[so0] offen offset:2048 nt\n\t"
      "buffer_load_dwordx4 a[204:207], %[koff], %[rk], %[so0] offen offset:3072 nt\n\t"
      "buffer_load_dwordx4 a[208:211], %[koff], %[rk], %[so1] offen offset:0 nt\n\t"
      "buffer_load_dwordx4 a[212:215], %[koff], %[rk], %[so1] offen offset:1024 nt\n\t"
      "buffer_load_dwordx4 a[216:219], %[koff], %[rk], %[so1] offen offset:2048 nt\n\t"
      "buffer_load_dwordx4 a[220:223], %[koff], %[rk], %[so1] offen offset:3072 nt\n\t"
      "s_add_u32 %[so0], %[rot], 57344\n\t"
      "s_and_b32 %[so0], %[so0], 0x1ffff\n\t"
      "s_add_u32 %[so1], %[so0], 4096\n\t"
      "buffer_load_dwordx4 a[224:227], %[koff], %[rk], %[so0] offen offset:0 nt\n\t"
      "buffer_load_dwordx4 a[228:231], %[koff], %[rk], %[so0] offen offset:1024 nt\n\t"
      "buffer_load_dwordx4 a[232:235], %[koff], %[rk], %[so0] offen offset:2048 nt\n\t"
      "buffer_load_dwordx4 a[236:239], %[koff], %[rk], %[so0] offen offset:3072 nt\n\t"
      "buffer_load_dwordx4 a[240:243], %[koff], %[rk], %[so1] offen offset:0 nt\n\t"
      "buffer_load_dwordx4 a[244:247], %[koff], %[rk], %[so1] offen offset:1024 nt\n\t"
      "buffer_load_dwordx4 a[248:251], %[koff], %[rk], %[so1] offen offset:2048 nt\n\t"
      "buffer_load_dwordx4 a[252:255], %[koff], %[rk], %[so1] offen offset:3072 nt\n\t"
      "buffer_load_dwordx4 v[92:95], %[wgoff], %[rwg], 0 offen offset:3584\n\t"
      "s_waitcnt vmcnt(21)\n\t"
      "v_perm_b32 v224, v132, v128, %[slo]\n\t"
      "v_perm_b32 v228, v132, v128, %[shi]\n\t"
      "v_perm_b32 v225, v140, v136, %[slo]\n\t"
      "v_perm_b32 v229, v140, v136, %[shi]\n\t"
      "v_perm_b32 v226, v148, v144, %[slo]\n\t"
      "v_perm_b32 v230, v148, v144, %[shi]\n\t"
      "v_perm_b32 v227, v156, v152, %[slo]\n\t"
      "v_perm_b32 v231, v156, v152, %[shi]\n\t"
      "v_perm_b32 v232, v133, v129, %[slo]\n\t"
      "v_perm_b32 v236, v133, v129, %[shi]\n\t"
      "v_perm_b32 v233, v141, v137, %[slo]\n\t"
      "v_perm_b32 v237, v141, v137, %[shi]\n\t"
      "v_perm_b32 v234, v149, v145, %[slo]\n\t"
      "v_perm_b32 v238, v149, v145, %[shi]\n\t"
      "v_perm_b32 v235, v157, v153, %[slo]\n\t"
      "v_perm_b32 v239, v157, v153, %[shi]\n\t"
      "v_perm_b32 v240, v134, v130, %[slo]\n\t"
      "v_perm_b32 v244, v134, v130, %[shi]\n\t"
      "v_perm_b32 v241, v142, v138, %[slo]\n\t"
      "v_perm_b32 v245, v142, v138, %[shi]\n\t"
      "v_perm_b32 v242, v150, v146, %[slo]\n\t"
      "v_perm_b32 v246, v150, v146, %[shi]\n\t"
      "v_perm_b32 v243, v158, v154, %[slo]\n\t"
      "v_perm_b32 v247, v158, v154, %[shi]\n\t"
      "v_perm_b32 v248, v135, v131, %[slo]\n\t"
      "v_perm_b32 v252, v135, v131, %[shi]\n\t"
      "v_perm_b32 v249, v143, v139, %[slo]\n\t"
      "v_perm_b32 v253, v143, v139, %[shi]\n\t"
      "v_perm_b32 v250, v151, v147, %[slo]\n\t"
      "v_perm_b32 v254, v151, v147, %[shi]\n\t"
      "v_perm_b32 v251, v159, v155, %[slo]\n\t"
      "v_perm_b32 v255, v159, v155, %[shi]\n\t"
      "v_cndmask_b32 v26, 0, v34, %[m0]\n\t"
      "v_cndmask_b32 v27, 0, v35, %[m0]\n\t"
      "v_cndmask_b32 v28, 0, v36, %[m0]\n\t"
      "v_cndmask_b32 v29, 0, v37, %[m0]\n\t"
      "v_cndmask_b32 v30, 0, v34, %[m1]\n\t"
      "v_cndmask_b32 v31, 0, v35, %[m1]\n\t"
      "v_cndmask_b32 v32, 0, v36, %[m1]\n\t"
      "v_cndmask_b32 v33, 0, v37, %[m1]\n\t"
      "v_mfma_f32_16x16x32_bf16 v[12:15], v[224:227], v[26:29], v[12:15]\n\t"
      "v_mfma_f32_16x16x32_bf16 v[16:19], v[240:243], v[26:29], v[16:19]\n\t"
      "v_cndmask_b32 v26, 0, v34, %[m2]\n\t"
      "v_cndmask_b32 v27, 0, v35, %[m2]\n\t"
      "v_cndmask_b32 v28, 0, v36, %[m2]\n\t"
      "v_cndmask_b32 v29, 0, v37, %[m2]\n\t"
      "v_mfma_f32_16x16x32_bf16 v[12:15], v[228:231], v[30:33], v[12:15]\n\t"
      "v_mfma_f32_16x16x32_bf16 v[16:19], v[244:247], v[30:33], v[16:19]\n\t"
      "v_cndmask_b32 v30, 0, v34, %[m3]\n\t"
      "v_cndmask_b32 v31, 0, v35, %[m3]\n\t"
      "v_cndmask_b32 v32, 0, v36, %[m3]\n\t"
      "v_cndmask_b32 v33, 0, v37, %[m3]\n\t"
      "v_mfma_f32_16x16x32_bf16 v[12:15], v[232:235], v[26:29], v[12:15]\n\t"
      "v_mfma_f32_16x16x32_bf16 v[16:19], v[248:251], v[26:29], v[16:19]\n\t"
      "v_mfma_f32_16x16x32_bf16 v[12:15], v[236:239], v[30:33], v[12:15]\n\t"
      "v_mfma_f32_16x16x32_bf16 v[16:19], v[252:255], v[30:33], v[16:19]\n\t"
      "v_perm_b32 v160, v44, v40, %[slo]\n\t"
      "v_perm_b32 v164, v44, v40, %[shi]\n\t"
      "v_perm_b32 v161, v52, v48, %[slo]\n\t"
      "v_perm_b32 v165, v52, v48, %[shi]\n\t"
      "v_perm_b32 v162, v60, v56, %[slo]\n\t"
      "v_perm_b32 v166, v60, v56, %[shi]\n\t"
      "v_perm_b32 v163, v68, v64, %[slo]\n\t"
      "v_perm_b32 v167, v68, v64, %[shi]\n\t"
      "v_perm_b32 v168, v45, v41, %[slo]\n\t"
      "v_perm_b32 v172, v45, v41, %[shi]\n\t"
      "v_perm_b32 v169, v53, v49, %[slo]\n\t"
      "v_perm_b32 v173, v53, v49, %[shi]\n\t"
      "v_perm_b32 v170, v61, v57, %[slo]\n\t"
      "v_perm_b32 v174, v61, v57, %[shi]\n\t"
      "v_perm_b32 v171, v69, v65, %[slo]\n\t"
      "v_perm_b32 v175, v69, v65, %[shi]\n\t"
      "v_perm_b32 v176, v46, v42, %[slo]\n\t"
      "v_perm_b32 v180, v46, v42, %[shi]\n\t"
      "v_perm_b32 v177, v54, v50, %[slo]\n\t"
      "v_perm_b32 v181, v54, v50, %[shi]\n\t"
      "v_perm_b32 v178, v62, v58, %[slo]\n\t"
      "v_perm_b32 v182, v62, v58, %[shi]\n\t"
      "v_perm_b32 v179, v70, v66, %[slo]\n\t"
      "v_perm_b32 v183, v70, v66, %[shi]\n\t"
      "v_perm_b32 v184, v47, v43, %[slo]\n\t"
      "v_perm_b32 v188, v47, v43, %[shi]\n\t"
      "v_perm_b32 v185, v55, v51, %[slo]\n\t"
      "v_perm_b32 v189, v55, v51, %[shi]\n\t"
      "v_perm_b32 v186, v63, v59, %[slo]\n\t"
      "v_perm_b32 v190, v63, v59, %[shi]\n\t"
      "v_perm_b32 v187, v71, v67, %[slo]\n\t"
      "v_perm_b32 v191, v71, v67, %[shi]\n\t"
      "v_cndmask_b32 v26, 0, v38, %[m0]\n\t"
      "v_cndmask_b32 v27, 0, v39, %[m0]\n\t"
      "v_cndmask_b32 v28, 0, v24, %[m0]\n\t"
      "v_cndmask_b32 v29, 0, v25, %[m0]\n\t"
      "v_cndmask_b32 v30, 0, v38, %[m1]\n\t"
      "v_cndmask_b32 v31, 0, v39, %[m1]\n\t"
      "v_cndmask_b32 v32, 0, v24, %[m1]\n\t"
      "v_cndmask_b32 v33, 0, v25, %[m1]\n\t"
      "v_mfma_f32_16x16x32_bf16 v[12:15], v[160:163], v[26:29], v[12:15]\n\t"
      "v_mfma_f32_16x16x32_bf16 v[16:19], v[176:179], v[26:29], v[16:19]\n\t"
      "v_cndmask_b32 v26, 0, v38, %[m2]\n\t"
      "v_cndmask_b32 v27, 0, v39, %[m2]\n\t"
      "v_cndmask_b32 v28, 0, v24, %[m2]\n\t"
      "v_cndmask_b32 v29, 0, v25, %[m2]\n\t"
      "v_mfma_f32_16x16x32_bf16 v[12:15], v[164:167], v[30:33], v[12:15]\n\t"
      "v_mfma_f32_16x16x32_bf16 v[16:19], v[180:183], v[30:33], v[16:19]\n\t"
      "v_cndmask_b32 v30, 0, v38, %[m3]\n\t"
      "v_cndmask_b32 v31, 0, v39, %[m3]\n\t"
      "v_cndmask_b32 v32, 0, v24, %[m3]\n\t"
      "v_cndmask_b32 v33, 0, v25, %[m3]\n\t"
      "v_mfma_f32_16x16x32_bf16 v[12:15], v[168:171], v[26:29], v[12:15]\n\t"
      "v_mfma_f32_16x16x32_bf16 v[16:19], v[184:187], v[26:29], v[16:19]\n\t"
      "v_mfma_f32_16x16x32_bf16 v[12:15], v[172:175], v[30:33], v[12:15]\n\t"
      "v_mfma_f32_16x16x32_bf16 v[16:19], v[188:191], v[30:33], v[16:19]\n\t"
      "1:\n\t"
      "s_memrealtime s[96:97]\n\t"
      "s_waitcnt lgkmcnt(0)\n\t"
      "s_sub_u32 %[so0], s96, %[due]\n\t"
      "s_cmp_lt_i32 %[so0], 0\n\t"
      "s_cbranch_scc0 2f\n\t"
      "s_sleep 1\n\t"
      "s_branch 1b\n\t"
      "2:\n\t"
      "s_add_u32 %[due], %[due], %[dlh]\n\t"
      "s_add_u32 %[so0], %[rot], 49152\n\t"
      "s_and_b32 %[so0], %[so0], 0x1ffff\n\t"
      "s_add_u32 %[so1], %[so0], 4096\n\t"
      "buffer_load_dwordx4 v[40:43], %[voff], %[rv], %[so0] offen offset:0 nt\n\t"
      "buffer_load_dwordx4 v[44:47], %[voff], %[rv], %[so0] offen offset:1024 nt\n\t"
      "buffer_load_dwordx4 v[48:51], %[voff], %[rv], %[so0] offen offset:2048 nt\n\t"
      "buffer_load_dwordx4 v[52:55], %[voff], %[rv], %[so0] offen offset:3072 nt\n\t"
      "buffer_load_dwordx4 v[56:59], %[voff], %[rv], %[so1] offen offset:0 nt\n\t"
      "buffer_load_dwordx4 v[60:63], %[voff], %[rv], %[so1] offen offset:1024 nt\n\t"
      "buffer_load_dwordx4 v[64:67], %[voff], %[rv], %[so1] offen offset:2048 nt\n\t"
      "buffer_load_dwordx4 v[68:71], %[voff], %[rv], %[so1] offen offset:3072 nt\n\t"
      "s_add_u32 %[so0], %[rot], 57344\n\t"
      "s_and_b32 %[so0], %[so0], 0x1ffff\n\t"
      "s_add_u32 %[so1], %[so0], 4096\n\t"
      "buffer_load_dwordx4 v[160:163], %[voff], %[rv], %[so0] offen offset:0 nt\n\t"
      "buffer_load_dwordx4 v[164:167], %[voff], %[rv], %[so0] offen offset:1024 nt\n\t"
      "buffer_load_dwordx4 v[168:171], %[voff], %[rv], %[so0] offen offset:2048 nt\n\t"
      "buffer_load_dwordx4 v[172:175], %[voff], %[rv], %[so0] offen offset:3072 nt\n\t"
      "buffer_load_dwordx4 v[176:179], %[voff], %[rv], %[so1] offen offset:0 nt\n\t"
      "buffer_load_dwordx4 v[180:183], %[voff], %[rv], %[so1] offen offset:1024 nt\n\t"
      "buffer_load_dwordx4 v[184:187], %[voff], %[rv], %[so1] offen offset:2048 nt\n\t"
      "buffer_load_dwordx4 v[188:191], %[voff], %[rv], %[so1] offen offset:3072 nt\n\t"
      "s_mov_b32 %[so0], 32768\n\t"
      "buffer_load_dword v80, %[wooff], %[rwo], %[so0] offen offset:0 nt\n\t"
      "buffer_load_dword v82, %[wooff], %[rwo], %[so0] offen offset:1024 nt\n\t"
      "buffer_load_dword v84, %[wooff], %[rwo], %[so0] offen offset:2048 nt\n\t"
      "buffer_load_dword v86, %[wooff], %[rwo], %[so0] offen offset:3072 nt\n\t"
      "s_waitcnt vmcnt(21)\n\t"
      "v_mfma_f32_16x16x32_bf16 v[224:227], a[192:195], v[8:11], 0\n\t"
      "v_mfma_f32_16x16x32_bf16 v[228:231], a[196:199], v[8:11], 0\n\t"
      "v_mfma_f32_16x16x32_bf16 v[232:235], a[200:203], v[8:11], 0\n\t"
      "v_mfma_f32_16x16x32_bf16 v[236:239], a[204:207], v[8:11], 0\n\t"
      "v_mfma_f32_16x16x32_bf16 v[240:243], a[208:211], v[8:11], 0\n\t"
      "v_mfma_f32_16x16x32_bf16 v[244:247], a[212:215], v[8:11], 0\n\t"
      "v_mfma_f32_16x16x32_bf16 v[248:251], a[216:219], v[8:11], 0\n\t"
      "v_mfma_f32_16x16x32_bf16 v[252:255], a[220:223], v[8:11], 0\n\t"
      "s_nop 1\n\t"
      "v_mov_b32_dpp v26, v224 quad_perm:[0,0,0,0] row_mask:0xf bank_mask:0xf\n\t"
      "v_mov_b32_dpp v27, v228 quad_perm:[0,0,0,0] row_mask:0xf bank_mask:0xf\n\t"
      "v_mov_b32_dpp v28, v232 quad_perm:[0,0,0,0] row_mask:0xf bank_mask:0xf\n\t"
      "v_mov_b32_dpp v29, v236 quad_perm:[0,0,0,0] row_mask:0xf bank_mask:0xf\n\t"
      "v_add_f32_dpp v26, v225, v26 quad_perm:[1,1,1,1] row_mask:0xf bank_mask:0xf\n\t"
      "v_add_f32_dpp v27, v229, v27 quad_perm:[1,1,1,1] row_mask:0xf bank_mask:0xf\n\t"
      "v_add_f32_dpp v28, v233, v28 quad_perm:[1,1,1,1] row_mask:0xf bank_mask:0xf\n\t"
      "v_add_f32_dpp v29, v237, v29 quad_perm:[1,1,1,1] row_mask:0xf bank_mask:0xf\n\t"
      "v_add_f32_dpp v26, v226, v26 quad_perm:[2,2,2,2] row_mask:0xf bank_mask:0xf\n\t"
      "v_add_f32_dpp v27, v230, v27 quad_perm:[2,2,2,2] row_mask:0xf bank_mask:0xf\n\t"
      "v_add_f32_dpp v28, v234, v28 quad_perm:[2,2,2,2] row_mask:0xf bank_mask:0xf\n\t"
      "v_add_f32_dpp v29, v238, v29 quad_perm:[2,2,2,2] row_mask:0xf bank_mask:0xf\n\t"
      "v_add_f32_dpp v26, v227, v26 quad_perm:[3,3,3,3] row_mask:0xf bank_mask:0xf\n\t"
      "v_add_f32_dpp v27, v231, v27 quad_perm:[3,3,3,3] row_mask:0xf bank_mask:0xf\n\t"
      "v_add_f32_dpp v28, v235, v28 quad_perm:[3,3,3,3] row_mask:0xf bank_mask:0xf\n\t"
      "v_add_f32_dpp v29, v239, v29 quad_perm:[3,3,3,3] row_mask:0xf bank_mask:0xf\n\t"
      "v_mov_b32_dpp v30, v240 quad_perm:[0,0,0,0] row_mask:0xf bank_mask:0xf\n\t"
      "v_mov_b32_dpp v31, v244 quad_perm:[0,0,0,0] row_mask:0xf bank_mask:0xf\n\t"
      "v_mov_b32_dpp v32, v248 quad_perm:[0,0,0,0] row_mask:0xf bank_mask:0xf\n\t"
      "v_mov_b32_dpp v33, v252 quad_perm:[0,0,0,0] row_mask:0xf bank_mask:0xf\n\t"
      "v_add_f32_dpp v30, v241, v30 quad_perm:[1,1,1,1] row_mask:0xf bank_mask:0xf\n\t"
      "v_add_f32_dpp v31, v245, v31 quad_perm:[1,1,1,1] row_mask:0xf bank_mask:0xf\n\t"
      "v_add_f32_dpp v32, v249, v32 quad_perm:[1,1,1,1] row_mask:0xf bank_mask:0xf\n\t"
      "v_add_f32_dpp v33, v253, v33 quad_perm:[1,1,1,1] row_mask:0xf bank_mask:0xf\n\t"
      "v_add_f32_dpp v30, v242, v30 quad_perm:[2,2,2,2] row_mask:0xf bank_mask:0xf\n\t"
      "v_add_f32_dpp v31, v246, v31 quad_perm:[2,2,2,2] row_mask:0xf bank_mask:0xf\n\t"
      "v_add_f32_dpp v32, v250, v32 quad_perm:[2,2,2,2] row_mask:0xf bank_mask:0xf\n\t"
      "v_add_f32_dpp v33, v254, v33 quad_perm:[2,2,2,2] row_mask:0xf bank_mask:0xf\n\t"
      "v_add_f32_dpp v30, v243, v30 quad_perm:[3,3,3,3] row_mask:0xf bank_mask:0xf\n\t"
      "v_add_f32_dpp v31, v247, v31 quad_perm:[3,3,3,3] row_mask:0xf bank_mask:0xf\n\t"
      "v_add_f32_dpp v32, v251, v32 quad_perm:[3,3,3,3] row_mask:0xf bank_mask:0xf\n\t"
      "v_add_f32_dpp v33, v255, v33 quad_perm:[3,3,3,3] row_mask:0xf bank_mask:0xf\n\t"
      "v_mfma_f32_16x16x32_bf16 v[224:227], a[224:227], v[8:11], 0\n\t"
      "v_mfma_f32_16x16x32_bf16 v[228:231], a[228:231], v[8:11], 0\n\t"
      "v_mfma_f32_16x16x32_bf16 v[232:235], a[232:235], v[8:11], 0\n\t"
      "v_mfma_f32_16x16x32_bf16 v[236:239], a[236:239], v[8:11], 0\n\t"
      "v_mfma_f32_16x16x32_bf16 v[240:243], a[240:243], v[8:11], 0\n\t"
      "v_mfma_f32_16x16x32_bf16 v[244:247], a[244:247], v[8:11], 0\n\t"
      "v_mfma_f32_16x16x32_bf16 v[248:251], a[248:251], v[8:11], 0\n\t"
      "v_mfma_f32_16x16x32_bf16 v[252:255], a[252:255], v[8:11], 0\n\t"
      "s_nop 1\n\t"
      "v_mov_b32_dpp v224, v224 quad_perm:[0,0,0,0] row_mask:0xf bank_mask:0xf\n\t"
      "v_mov_b32_dpp v228, v228 quad_perm:[0,0,0,0] row_mask:0xf bank_mask:0xf\n\t"
      "v_mov_b32_dpp v232, v232 quad_perm:[0,0,0,0] row_mask:0xf bank_mask:0xf\n\t"
      "v_mov_b32_dpp v236, v236 quad_perm:[0,0,0,0] row_mask:0xf bank_mask:0xf\n\t"
      "v_add_f32_dpp v224, v225, v224 quad_perm:[1,1,1,1] row_mask:0xf bank_mask:0xf\n\t"
      "v_add_f32_dpp v228, v229, v228 quad_perm:[1,1,1,1] row_mask:0xf bank_mask:0xf\n\t"
      "v_add_f32_dpp v232, v233, v232 quad_perm:[1,1,1,1] row_mask:0xf bank_mask:0xf\n\t"
      "v_add_f32_dpp v236, v237, v236 quad_perm:[1,1,1,1] row_mask:0xf bank_mask:0xf\n\t"
      "v_add_f32_dpp v224, v226, v224 quad_perm:[2,2,2,2] row_mask:0xf bank_mask:0xf\n\t"
      "v_add_f32_dpp v228, v230, v228 quad_perm:[2,2,2,2] row_mask:0xf bank_mask:0xf\n\t"
      "v_add_f32_dpp v232, v234, v232 quad_perm:[2,2,2,2] row_mask:0xf bank_mask:0xf\n\t"
      "v_add_f32_dpp v236, v238, v236 quad_perm:[2,2,2,2] row_mask:0xf bank_mask:0xf\n\t"
      "v_add_f32_dpp v224, v227, v224 quad_perm:[3,3,3,3] row_mask:0xf bank_mask:0xf\n\t"
      "v_add_f32_dpp v228, v231, v228 quad_perm:[3,3,3,3] row_mask:0xf bank_mask:0xf\n\t"
      "v_add_f32_dpp v232, v235, v232 quad_perm:[3,3,3,3] row_mask:0xf bank_mask:0xf\n\t"
      "v_add_f32_dpp v236, v239, v236 quad_perm:[3,3,3,3] row_mask:0xf bank_mask:0xf\n\t"
      "v_mov_b32_dpp v240, v240 quad_perm:[0,0,0,0] row_mask:0xf bank_mask:0xf\n\t"
      "v_mov_b32_dpp v244, v244 quad_perm:[0,0,0,0] row_mask:0xf bank_mask:0xf\n\t"
      "v_mov_b32_dpp v248, v248 quad_perm:[0,0,0,0] row_mask:0xf bank_mask:0xf\n\t"
      "v_mov_b32_dpp v252, v252 quad_perm:[0,0,0,0] row_mask:0xf bank_mask:0xf\n\t"
      "v_add_f32_dpp v240, v241, v240 quad_perm:[1,1,1,1] row_mask:0xf bank_mask:0xf\n\t"
      "v_add_f32_dpp v244, v245, v244 quad_perm:[1,1,1,1] row_mask:0xf bank_mask:0xf\n\t"
      "v_add_f32_dpp v248, v249, v248 quad_perm:[1,1,1,1] row_mask:0xf bank_mask:0xf\n\t"
      "v_add_f32_dpp v252, v253, v252 quad_perm:[1,1,1,1] row_mask:0xf bank_mask:0xf\n\t"
      "v_add_f32_dpp v240, v242, v240 quad_perm:[2,2,2,2] row_mask:0xf bank_mask:0xf\n\t"
      "v_add_f32_dpp v244, v246, v244 quad_perm:[2,2,2,2] row_mask:0xf bank_mask:0xf\n\t"
      "v_add_f32_dpp v248, v250, v248 quad_perm:[2,2,2,2] row_mask:0xf bank_mask:0xf\n\t"
      "v_add_f32_dpp v252, v254, v252 quad_perm:[2,2,2,2] row_mask:0xf bank_mask:0xf\n\t"
      "v_add_f32_dpp v240, v243, v240 quad_perm:[3,3,3,3] row_mask:0xf bank_mask:0xf\n\t"
      "v_add_f32_dpp v244, v247, v244 quad_perm:[3,3,3,3] row_mask:0xf bank_mask:0xf\n\t"
      "v_add_f32_dpp v248, v251, v248 quad_perm:[3,3,3,3] row_mask:0xf bank_mask:0xf\n\t"
      "v_add_f32_dpp v252, v255, v252 quad_perm:[3,3,3,3] row_mask:0xf bank_mask:0xf\n\t"
      "v_max3_f32 v22, v26, v27, v20\n\t"
      "v_max3_f32 v23, v28, v29, v30\n\t"
      "v_max3_f32 v22, v22, v31, v32\n\t"
      "v_max3_f32 v23, v23, v33, v224\n\t"
      "v_max3_f32 v22, v22, v228, v232\n\t"
      "v_max3_f32 v23, v23, v236, v240\n\t"
      "v_max3_f32 v22, v22, v244, v248\n\t"
      "v_max3_f32 v22, v22, v23, v252\n\t"
      "v_mov_b32 v23, v22\n\t"
      "s_nop 1\n\t"
      "v_permlane16_swap_b32 v22, v23\n\t"
      "s_nop 0\n\t"
      "v_max_f32 v22, v22, v23\n\t"
      "v_mov_b32 v23, v22\n\t"
      "s_nop 1\n\t"
      "v_permlane32_swap_b32 v22, v23\n\t"
      "s_nop 0\n\t"
      "v_max_f32 v22, v22, v23\n\t"
      "v_sub_f32 v23, v20, v22\n\t"
      "v_mul_f32 v24, %[nc2], v22\n\t"
      "v_mul_f32 v23, %[c2], v23\n\t"
      "v_mov_b32 v20, v22\n\t"
      "v_exp_f32 v23, v23\n\t"
      "v_fma_f32 v26, v26, %[c2], v24\n\t"
      "v_fma_f32 v27, v27, %[c2], v24\n\t"
      "v_fma_f32 v28, v28, %[c2], v24\n\t"
      "v_fma_f32 v29, v29, %[c2], v24\n\t"
      "v_fma_f32 v30, v30, %[c2], v24\n\t"
      "v_fma_f32 v31, v31, %[c2], v24\n\t"
      "v_fma_f32 v32, v32, %[c2], v24\n\t"
      "v_fma_f32 v33, v33, %[c2], v24\n\t"
      "v_fma_f32 v224, v224, %[c2], v24\n\t"
      "v_fma_f32 v228, v228, %[c2], v24\n\t"
      "v_fma_f32 v232, v232, %[c2], v24\n\t"
      "v_fma_f32 v236, v236, %[c2], v24\n\t"
      "v_fma_f32 v240, v240, %[c2], v24\n\t"
      "v_fma_f32 v244, v244, %[c2], v24\n\t"
      "v_fma_f32 v248, v248, %[c2], v24\n\t"
      "v_fma_f32 v252, v252, %[c2], v24\n\t"
      "v_exp_f32 v26, v26\n\t"
      "v_mul_f32 v12, v23, v12\n\t"
      "v_exp_f32 v27, v27\n\t"
      "v_mul_f32 v13, v23, v13\n\t"
      "v_exp_f32 v28, v28\n\t"
      "v_mul_f32 v14, v23, v14\n\t"
      "v_exp_f32 v29, v29\n\t"
      "v_mul_f32 v15, v23, v15\n\t"
      "v_exp_f32 v30, v30\n\t"
      "v_mul_f32 v16, v23, v16\n\t"
      "v_exp_f32 v31, v31\n\t"
      "v_mul_f32 v17, v23, v17\n\t"
      "v_exp_f32 v32, v32\n\t"
      "v_mul_f32 v18, v23, v18\n\t"
      "v_exp_f32 v33, v33\n\t"
      "v_mul_f32 v19, v23, v19\n\t"
      "v_exp_f32 v224, v224\n\t"
      "v_exp_f32 v228, v228\n\t"
      "v_exp_f32 v232, v232\n\t"
      "v_exp_f32 v236, v236\n\t"
      "v_exp_f32 v240, v240\n\t"
      "v_exp_f32 v244, v244\n\t"
      "v_exp_f32 v248, v248\n\t"
      "v_exp_f32 v252, v252\n\t"
      "v_mul_f32 v21, v21, v23\n\t"
      "v_add_f32 v22, v26, v27\n\t"
      "v_add_f32 v23, v28, v29\n\t"
      "v_add_f32 v22, v22, v30\n\t"
      "v_add_f32 v23, v23, v31\n\t"
      "v_add_f32 v22, v22, v32\n\t"
      "v_add_f32 v23, v23, v33\n\t"
      "v_add_f32 v22, v22, v224\n\t"
      "v_add_f32 v23, v23, v228\n\t"
      "v_add_f32 v22, v22, v232\n\t"
      "v_add_f32 v23, v23, v236\n\t"
      "v_add_f32 v22, v22, v240\n\t"
      "v_add_f32 v23, v23, v244\n\t"
      "v_add_f32 v22, v22, v248\n\t"
      "v_add_f32 v23, v23, v252\n\t"
      "v_add_f32 v21, v21, v22\n\t"
      "v_cvt_pk_bf16_f32 v34, v26, v27\n\t"
      "v_cvt_pk_bf16_f32 v35, v28, v29\n\t"
      "v_cvt_pk_bf16_f32 v36, v30, v31\n\t"
      "v_cvt_pk_bf16_f32 v37, v32, v33\n\t"
      "v_add_f32 v21, v21, v23\n\t"
      "v_cvt_pk_bf16_f32 v38, v224, v228\n\t"
      "v_cvt_pk_bf16_f32 v39, v232, v236\n\t"
      "v_cvt_pk_bf16_f32 v24, v240, v244\n\t"
      "v_cvt_pk_bf16_f32 v25, v248, v252\n\t"
      "1:\n\t"
      "s_memrealtime s[96:97]\n\t"
      "s_waitcnt lgkmcnt(0)\n\t"
      "s_sub_u32 %[so0], s96, %[due]\n\t"
      "s_cmp_lt_i32 %[so0], 0\n\t"
      "s_cbranch_scc0 2f\n\t"
      "s_sleep 1\n\t"
      "s_branch 1b\n\t"
      "2:\n\t"
      "s_add_u32 %[due], %[due], %[dlh]\n\t"
      "s_add_u32 %[so0], %[rot], 65536\n\t"
      "s_and_b32 %[so0], %[so0], 0x1ffff\n\t"
      "s_add_u32 %[so1], %[so0], 4096\n\t"
      "buffer_load_dwordx4 a[0:3], %[koff], %[rk], %[so0] offen offset:0 nt\n\t"
      "buffer_load_dwordx4 a[4:7], %[koff], %[rk], %[so0] offen offset:1024 nt\n\t"
      "buffer_load_dwordx4 a[8:11], %[koff], %[rk], %[so0] offen offset:2048 nt\n\t"
      "buffer_load_dwordx4 a[12:15], %[koff], %[rk], %[so0] offen offset:3072 nt\n\t"
      "buffer_load_dwordx4 a[16:19], %[koff], %[rk], %[so1] offen offset:0 nt\n\t"
      "buffer_load_dwordx4 a[20:23], %[koff], %[rk], %[so1] offen offset:1024 nt\n\t"
      "buffer_load_dwordx4 a[24:27], %[koff], %[rk], %[so1] offen offset:2048 nt\n\t"
      "buffer_load_dwordx4 a[28:31], %[koff], %[rk], %[so1] offen offset:3072 nt\n\t"
      "s_add_u32 %[so0], %[rot], 73728\n\t"
      "s_and_b32 %[so0], %[so0], 0x1ffff\n\t"
      "s_add_u32 %[so1], %[so0], 4096\n\t"
      "buffer_load_dwordx4 a[32:35], %[koff], %[rk], %[so0] offen offset:0 nt\n\t"
      "buffer_load_dwordx4 a[36:39], %[koff], %[rk], %[so0] offen offset:1024 nt\n\t"
      "buffer_load_dwordx4 a[40:43], %[koff], %[rk], %[so0] offen offset:2048 nt\n\t"
      "buffer_load_dwordx4 a[44:47], %[koff], %[rk], %[so0] offen offset:3072 nt\n\t"
      "buffer_load_dwordx4 a[48:51], %[koff], %[rk], %[so1] offen offset:0 nt\n\t"
      "buffer_load_dwordx4 a[52:55], %[koff], %[rk], %[so1] offen offset:1024 nt\n\t"
      "buffer_load_dwordx4 a[56:59], %[koff], %[rk], %[so1] offen offset:2048 nt\n\t"
      "buffer_load_dwordx4 a[60:63], %[koff], %[rk], %[so1] offen offset:3072 nt\n\t"
      "buffer_load_dwordx4 v[96:99], %[wgoff], %[rwu], 0 offen\n\t"
      "s_waitcnt vmcnt(21)\n\t"
      "v_perm_b32 v224, v44, v40, %[slo]\n\t"
      "v_perm_b32 v228, v44, v40, %[shi]\n\t"
      "v_perm_b32 v225, v52, v48, %[slo]\n\t"
      "v_perm_b32 v229, v52, v48, %[shi]\n\t"
      "v_perm_b32 v226, v60, v56, %[slo]\n\t"
      "v_perm_b32 v230, v60, v56, %[shi]\n\t"
      "v_perm_b32 v227, v68, v64, %[slo]\n\t"
      "v_perm_b32 v231, v68, v64, %[shi]\n\t"
      "v_perm_b32 v232, v45, v41, %[slo]\n\t"
      "v_perm_b32 v236, v45, v41, %[shi]\n\t"
      "v_perm_b32 v233, v53, v49, %[slo]\n\t"
      "v_perm_b32 v237, v53, v49, %[shi]\n\t"
      "v_perm_b32 v234, v61, v57, %[slo]\n\t"
      "v_perm_b32 v238, v61, v57, %[shi]\n\t"
      "v_perm_b32 v235, v69, v65, %[slo]\n\t"
      "v_perm_b32 v239, v69, v65, %[shi]\n\t"
      "v_perm_b32 v240, v46, v42, %[slo]\n\t"
      "v_perm_b32 v244, v46, v42, %[shi]\n\t"
      "v_perm_b32 v241, v54, v50, %[slo]\n\t"
      "v_perm_b32 v245, v54, v50, %[shi]\n\t"
      "v_perm_b32 v242, v62, v58, %[slo]\n\t"
      "v_perm_b32 v246, v62, v58, %[shi]\n\t"
      "v_perm_b32 v243, v70, v66, %[slo]\n\t"
      "v_perm_b32 v247, v70, v66, %[shi]\n\t"
      "v_perm_b32 v248, v47, v43, %[slo]\n\t"
      "v_perm_b32 v252, v47, v43, %[shi]\n\t"
      "v_perm_b32 v249, v55, v51, %[slo]\n\t"
      "v_perm_b32 v253, v55, v51, %[shi]\n\t"
      "v_perm_b32 v250, v63, v59, %[slo]\n\t"
      "v_perm_b32 v254, v63, v59, %[shi]\n\t"
      "v_perm_b32 v251, v71, v67, %[slo]\n\t"
      "v_perm_b32 v255, v71, v67, %[shi]\n\t"
      "v_cndmask_b32 v26, 0, v34, %[m0]\n\t"
      "v_cndmask_b32 v27, 0, v35, %[m0]\n\t"
      "v_cndmask_b32 v28, 0, v36, %[m0]\n\t"
      "v_cndmask_b32 v29, 0, v37, %[m0]\n\t"
      "v_cndmask_b32 v30, 0, v34, %[m1]\n\t"
      "v_cndmask_b32 v31, 0, v35, %[m1]\n\t"
      "v_cndmask_b32 v32, 0, v36, %[m1]\n\t"
      "v_cndmask_b32 v33, 0, v37, %[m1]\n\t"
      "v_mfma_f32_16x16x32_bf16 v[12:15], v[224:227], v[26:29], v[12:15]\n\t"
      "v_mfma_f32_16x16x32_bf16 v[16:19], v[240:243], v[26:29], v[16:19]\n\t"
      "v_cndmask_b32 v26, 0, v34, %[m2]\n\t"
      "v_cndmask_b32 v27, 0, v35, %[m2]\n\t"
      "v_cndmask_b32 v28, 0, v36, %[m2]\n\t"
      "v_cndmask_b32 v29, 0, v37, %[m2]\n\t"
      "v_mfma_f32_16x16x32_bf16 v[12:15], v[228:231], v[30:33], v[12:15]\n\t"
      "v_mfma_f32_16x16x32_bf16 v[16:19], v[244:247], v[30:33], v[16:19]\n\t"
      "v_cndmask_b32 v30, 0, v34, %[m3]\n\t"
      "v_cndmask_b32 v31, 0, v35, %[m3]\n\t"
      "v_cndmask_b32 v32, 0, v36, %[m3]\n\t"
      "v_cndmask_b32 v33, 0, v37, %[m3]\n\t"
      "v_mfma_f32_16x16x32_bf16 v[12:15], v[232:235], v[26:29], v[12:15]\n\t"
      "v_mfma_f32_16x16x32_bf16 v[16:19], v[248:251], v[26:29], v[16:19]\n\t"
      "v_mfma_f32_16x16x32_bf16 v[12:15], v[236:239], v[30:33], v[12:15]\n\t"
      "v_mfma_f32_16x16x32_bf16 v[16:19], v[252:255], v[30:33], v[16:19]\n\t"
      "ds_write_b128 %[lds], v[224:227] offset:0\n\t"
      "ds_write_b128 %[lds], v[228:231] offset:1024\n\t"
      "ds_write_b128 %[lds], v[232:235] offset:2048\n\t"
      "ds_write_b128 %[lds], v[236:239] offset:3072\n\t"
      "ds_write_b128 %[lds], v[240:243] offset:4096\n\t"
      "ds_write_b128 %[lds], v[244:247] offset:5120\n\t"
      "ds_write_b128 %[lds], v[248:251] offset:6144\n\t"
      "ds_write_b128 %[lds], v[252:255] offset:7168\n\t"
      "v_perm_b32 v40, v164, v160, %[slo]\n\t"
      "v_perm_b32 v44, v164, v160, %[shi]\n\t"
      "v_perm_b32 v41, v172, v168, %[slo]\n\t"
      "v_perm_b32 v45, v172, v168, %[shi]\n\t"
      "v_perm_b32 v42, v180, v176, %[slo]\n\t"
      "v_perm_b32 v46, v180, v176, %[shi]\n\t"
      "v_perm_b32 v43, v188, v184, %[slo]\n\t"
      "v_perm_b32 v47, v188, v184, %[shi]\n\t"
      "v_perm_b32 v48, v165, v161, %[slo]\n\t"
      "v_perm_b32 v52, v165, v161, %[shi]\n\t"
      "v_perm_b32 v49, v173, v169, %[slo]\n\t"
      "v_perm_b32 v53, v173, v169, %[shi]\n\t"
      "v_perm_b32 v50, v181, v177, %[slo]\n\t"
      "v_perm_b32 v54, v181, v177, %[shi]\n\t"
      "v_perm_b32 v51, v189, v185, %[slo]\n\t"
      "v_perm_b32 v55, v189, v185, %[shi]\n\t"
      "v_perm_b32 v56, v166, v162, %[slo]\n\t"
      "v_perm_b32 v60, v166, v162, %[shi]\n\t"
      "v_perm_b32 v57, v174, v170, %[slo]\n\t"
      "v_perm_b32 v61, v174, v170, %[shi]\n\t"
      "v_perm_b32 v58, v182, v178, %[slo]\n\t"
      "v_perm_b32 v62, v182, v178, %[shi]\n\t"
      "v_perm_b32 v59, v190, v186, %[slo]\n\t"
      "v_perm_b32 v63, v190, v186, %[shi]\n\t"
      "v_perm_b32 v64, v167, v163, %[slo]\n\t"
      "v_perm_b32 v68, v167, v163, %[shi]\n\t"
      "v_perm_b32 v65, v175, v171, %[slo]\n\t"
      "v_perm_b32 v69, v175, v171, %[shi]\n\t"
      "v_perm_b32 v66, v183, v179, %[slo]\n\t"
      "v_perm_b32 v70, v183, v179, %[shi]\n\t"
      "v_perm_b32 v67, v191, v187, %[slo]\n\t"
      "v_perm_b32 v71, v191, v187, %[shi]\n\t"
      "v_cndmask_b32 v26, 0, v38, %[m0]\n\t"
      "v_cndmask_b32 v27, 0, v39, %[m0]\n\t"
      "v_cndmask_b32 v28, 0, v24, %[m0]\n\t"
      "v_cndmask_b32 v29, 0, v25, %[m0]\n\t"
      "v_cndmask_b32 v30, 0, v38, %[m1]\n\t"
      "v_cndmask_b32 v31, 0, v39, %[m1]\n\t"
      "v_cndmask_b32 v32, 0, v24, %[m1]\n\t"
      "v_cndmask_b32 v33, 0, v25, %[m1]\n\t"
      "v_mfma_f32_16x16x32_bf16 v[12:15], v[40:43], v[26:29], v[12:15]\n\t"
      "v_mfma_f32_16x16x32_bf16 v[16:19], v[56:59], v[26:29], v[16:19]\n\t"
      "v_cndmask_b32 v26, 0, v38, %[m2]\n\t"
      "v_cndmask_b32 v27, 0, v39, %[m2]\n\t"
      "v_cndmask_b32 v28, 0, v24, %[m2]\n\t"
      "v_cndmask_b32 v29, 0, v25, %[m2]\n\t"
      "v_mfma_f32_16x16x32_bf16 v[12:15], v[44:47], v[30:33], v[12:15]\n\t"
      "v_mfma_f32_16x16x32_bf16 v[16:19], v[60:63], v[30:33], v[16:19]\n\t"
      "v_cndmask_b32 v30, 0, v38, %[m3]\n\t"
      "v_cndmask_b32 v31, 0, v39, %[m3]\n\t"
      "v_cndmask_b32 v32, 0, v24, %[m3]\n\t"
      "v_cndmask_b32 v33, 0, v25, %[m3]\n\t"
      "v_mfma_f32_16x16x32_bf16 v[12:15], v[48:51], v[26:29], v[12:15]\n\t"
      "v_mfma_f32_16x16x32_bf16 v[16:19], v[64:67], v[26:29], v[16:19]\n\t"
      "v_mfma_f32_16x16x32_bf16 v[12:15], v[52:55], v[30:33], v[12:15]\n\t"
      "v_mfma_f32_16x16x32_bf16 v[16:19], v[68:71], v[30:33], v[16:19]\n\t"
      "ds_write_b128 %[lds], v[40:43] offset:8192\n\t"
      "ds_write_b128 %[lds], v[44:47] offset:9216\n\t"
      "ds_write_b128 %[lds], v[48:51] offset:10240\n\t"
      "ds_write_b128 %[lds], v[52:55] offset:11264\n\t"
      "ds_write_b128 %[lds], v[56:59] offset:12288\n\t"
      "ds_write_b128 %[lds], v[60:63] offset:13312\n\t"
      "ds_write_b128 %[lds], v[64:67] offset:14336\n\t"
      "ds_write_b128 %[lds], v[68:71] offset:15360\n\t"
      "1:\n\t"
      "s_memrealtime s[96:97]\n\t"
      "s_waitcnt lgkmcnt(0)\n\t"
      "s_sub_u32 %[so0], s96, %[due]\n\t"
      "s_cmp_lt_i32 %[so0], 0\n\t"
      "s_cbranch_scc0 2f\n\t"
      "s_sleep 1\n\t"
      "s_branch 1b\n\t"
      "2:\n\t"
      "s_add_u32 %[due], %[due], %[dlh]\n\t"
      "s_add_u32 %[so0], %[rot], 65536\n\t"
      "s_and_b32 %[so0], %[so0], 0x1ffff\n\t"
      "s_add_u32 %[so1], %[so0], 4096\n\t"
      "buffer_load_dwordx4 v[40:43], %[voff], %[rv], %[so0] offen offset:0 nt\n\t"
      "buffer_load_dwordx4 v[44:47], %[voff], %[rv], %[so0] offen offset:1024 nt\n\t"
      "buffer_load_dwordx4 v[48:51], %[voff], %[rv], %[so0] offen offset:2048 nt\n\t"
      "buffer_load_dwordx4 v[52:55], %[voff], %[rv], %[so0] offen offset:3072 nt\n\t"
      "buffer_load_dwordx4 v[56:59], %[voff], %[rv], %[so1] offen offset:0 nt\n\t"
      "buffer_load_dwordx4 v[60:63], %[voff], %[rv], %[so1] offen offset:1024 nt\n\t"
      "buffer_load_dwordx4 v[64:67], %[voff], %[rv], %[so1] offen offset:2048 nt\n\t"
      "buffer_load_dwordx4 v[68:71], %[voff], %[rv], %[so1] offen offset:3072 nt\n\t"
      "s_add_u32 %[so0], %[rot], 73728\n\t"
      "s_and_b32 %[so0], %[so0], 0x1ffff\n\t"
      "s_add_u32 %[so1], %[so0], 4096\n\t"
      "buffer_load_dwordx4 v[160:163], %[voff], %[rv], %[so0] offen offset:0 nt\n\t"
      "buffer_load_dwordx4 v[164:167], %[voff], %[rv], %[so0] offen offset:1024 nt\n\t"
      "buffer_load_dwordx4 v[168:171], %[voff], %[rv], %[so0] offen offset:2048 nt\n\t"
      "buffer_load_dwordx4 v[172:175], %[voff], %[rv], %[so0] offen offset:3072 nt\n\t"
      "buffer_load_dwordx4 v[176:179], %[voff], %[rv], %[so1] offen offset:0 nt\n\t"
      "buffer_load_dwordx4 v[180:183], %[voff], %[rv], %[so1] offen offset:1024 nt\n\t"
      "buffer_load_dwordx4 v[184:187], %[voff], %[rv], %[so1] offen offset:2048 nt\n\t"
      "buffer_load_dwordx4 v[188:191], %[voff], %[rv], %[so1] offen offset:3072 nt\n\t"
      "s_mov_b32 %[so0], 40960\n\t"
      "buffer_load_dword v81, %[wooff], %[rwo], %[so0] offen offset:0 nt\n\t"
      "buffer_load_dword v83, %[wooff], %[rwo], %[so0] offen offset:1024 nt\n\t"
      "buffer_load_dword v85, %[wooff], %[rwo], %[so0] offen offset:2048 nt\n\t"
      "buffer_load_dword v87, %[wooff], %[rwo], %[so0] offen offset:3072 nt\n\t"
      "s_waitcnt vmcnt(21)\n\t"
      "v_mfma_f32_16x16x32_bf16 v[224:227], a[0:3], v[8:11], 0\n\t"
      "v_mfma_f32_16x16x32_bf16 v[228:231], a[4:7], v[8:11], 0\n\t"
      "v_mfma_f32_16x16x32_bf16 v[232:235], a[8:11], v[8:11], 0\n\t"
      "v_mfma_f32_16x16x32_bf16 v[236:239], a[12:15], v[8:11], 0\n\t"
      "v_mfma_f32_16x16x32_bf16 v[240:243], a[16:19], v[8:11], 0\n\t"
      "v_mfma_f32_16x16x32_bf16 v[244:247], a[20:23], v[8:11], 0\n\t"
      "v_mfma_f32_16x16x32_bf16 v[248:251], a[24:27], v[8:11], 0\n\t"
      "v_mfma_f32_16x16x32_bf16 v[252:255], a[28:31], v[8:11], 0\n\t"
      "s_nop 1\n\t"
      "v_mov_b32_dpp v26, v224 quad_perm:[0,0,0,0] row_mask:0xf bank_mask:0xf\n\t"
      "v_mov_b32_dpp v27, v228 quad_perm:[0,0,0,0] row_mask:0xf bank_mask:0xf\n\t"
      "v_mov_b32_dpp v28, v232 quad_perm:[0,0,0,0] row_mask:0xf bank_mask:0xf\n\t"
      "v_mov_b32_dpp v29, v236 quad_perm:[0,0,0,0] row_mask:0xf bank_mask:0xf\n\t"
      "v_add_f32_dpp v26, v225, v26 quad_perm:[1,1,1,1] row_mask:0xf bank_mask:0xf\n\t"
      "v_add_f32_dpp v27, v229, v27 quad_perm:[1,1,1,1] row_mask:0xf bank_mask:0xf\n\t"
      "v_add_f32_dpp v28, v233, v28 quad_perm:[1,1,1,1] row_mask:0xf bank_mask:0xf\n\t"
      "v_add_f32_dpp v29, v237, v29 quad_perm:[1,1,1,1] row_mask:0xf bank_mask:0xf\n\t"
      "v_add_f32_dpp v26, v226, v26 quad_perm:[2,2,2,2] row_mask:0xf bank_mask:0xf\n\t"
      "v_add_f32_dpp v27, v230, v27 quad_perm:[2,2,2,2] row_mask:0xf bank_mask:0xf\n\t"
      "v_add_f32_dpp v28, v234, v28 quad_perm:[2,2,2,2] row_mask:0xf bank_mask:0xf\n\t"
      "v_add_f32_dpp v29, v238, v29 quad_perm:[2,2,2,2] row_mask:0xf bank_mask:0xf\n\t"
      "v_add_f32_dpp v26, v227, v26 quad_perm:[3,3,3,3] row_mask:0xf bank_mask:0xf\n\t"
      "v_add_f32_dpp v27, v231, v27 quad_perm:[3,3,3,3] row_mask:0xf bank_mask:0xf\n\t"
      "v_add_f32_dpp v28, v235, v28 quad_perm:[3,3,3,3] row_mask:0xf bank_mask:0xf\n\t"
      "v_add_f32_dpp v29, v239, v29 quad_perm:[3,3,3,3] row_mask:0xf bank_mask:0xf\n\t"
      "v_mov_b32_dpp v30, v240 quad_perm:[0,0,0,0] row_mask:0xf bank_mask:0xf\n\t"
      "v_mov_b32_dpp v31, v244 quad_perm:[0,0,0,0] row_mask:0xf bank_mask:0xf\n\t"
      "v_mov_b32_dpp v32, v248 quad_perm:[0,0,0,0] row_mask:0xf bank_mask:0xf\n\t"
      "v_mov_b32_dpp v33, v252 quad_perm:[0,0,0,0] row_mask:0xf bank_mask:0xf\n\t"
      "v_add_f32_dpp v30, v241, v30 quad_perm:[1,1,1,1] row_mask:0xf bank_mask:0xf\n\t"
      "v_add_f32_dpp v31, v245, v31 quad_perm:[1,1,1,1] row_mask:0xf bank_mask:0xf\n\t"
      "v_add_f32_dpp v32, v249, v32 quad_perm:[1,1,1,1] row_mask:0xf bank_mask:0xf\n\t"
      "v_add_f32_dpp v33, v253, v33 quad_perm:[1,1,1,1] row_mask:0xf bank_mask:0xf\n\t"
      "v_add_f32_dpp v30, v242, v30 quad_perm:[2,2,2,2] row_mask:0xf bank_mask:0xf\n\t"
      "v_add_f32_dpp v31, v246, v31 quad_perm:[2,2,2,2] row_mask:0xf bank_mask:0xf\n\t"
      "v_add_f32_dpp v32, v250, v32 quad_perm:[2,2,2,2] row_mask:0xf bank_mask:0xf\n\t"
      "v_add_f32_dpp v33, v254, v33 quad_perm:[2,2,2,2] row_mask:0xf bank_mask:0xf\n\t"
      "v_add_f32_dpp v30, v243, v30 quad_perm:[3,3,3,3] row_mask:0xf bank_mask:0xf\n\t"
      "v_add_f32_dpp v31, v247, v31 quad_perm:[3,3,3,3] row_mask:0xf bank_mask:0xf\n\t"
      "v_add_f32_dpp v32, v251, v32 quad_perm:[3,3,3,3] row_mask:0xf bank_mask:0xf\n\t"
      "v_add_f32_dpp v33, v255, v33 quad_perm:[3,3,3,3] row_mask:0xf bank_mask:0xf\n\t"
      "v_mfma_f32_16x16x32_bf16 v[224:227], a[32:35], v[8:11], 0\n\t"
      "v_mfma_f32_16x16x32_bf16 v[228:231], a[36:39], v[8:11], 0\n\t"
      "v_mfma_f32_16x16x32_bf16 v[232:235], a[40:43], v[8:11], 0\n\t"
      "v_mfma_f32_16x16x32_bf16 v[236:239], a[44:47], v[8:11], 0\n\t"
      "v_mfma_f32_16x16x32_bf16 v[240:243], a[48:51], v[8:11], 0\n\t"
      "v_mfma_f32_16x16x32_bf16 v[244:247], a[52:55], v[8:11], 0\n\t"
      "v_mfma_f32_16x16x32_bf16 v[248:251], a[56:59], v[8:11], 0\n\t"
      "v_mfma_f32_16x16x32_bf16 v[252:255], a[60:63], v[8:11], 0\n\t"
      "s_nop 1\n\t"
      "v_mov_b32_dpp v224, v224 quad_perm:[0,0,0,0] row_mask:0xf bank_mask:0xf\n\t"
      "v_mov_b32_dpp v228, v228 quad_perm:[0,0,0,0] row_mask:0xf bank_mask:0xf\n\t"
      "v_mov_b32_dpp v232, v232 quad_perm:[0,0,0,0] row_mask:0xf bank_mask:0xf\n\t"
      "v_mov_b32_dpp v236, v236 quad_perm:[0,0,0,0] row_mask:0xf bank_mask:0xf\n\t"
      "v_add_f32_dpp v224, v225, v224 quad_perm:[1,1,1,1] row_mask:0xf bank_mask:0xf\n\t"
      "v_add_f32_dpp v228, v229, v228 quad_perm:[1,1,1,1] row_mask:0xf bank_mask:0xf\n\t"
      "v_add_f32_dpp v232, v233, v232 quad_perm:[1,1,1,1] row_mask:0xf bank_mask:0xf\n\t"
      "v_add_f32_dpp v236, v237, v236 quad_perm:[1,1,1,1] row_mask:0xf bank_mask:0xf\n\t"
      "v_add_f32_dpp v224, v226, v224 quad_perm:[2,2,2,2] row_mask:0xf bank_mask:0xf\n\t"
      "v_add_f32_dpp v228, v230, v228 quad_perm:[2,2,2,2] row_mask:0xf bank_mask:0xf\n\t"
      "v_add_f32_dpp v232, v234, v232 quad_perm:[2,2,2,2] row_mask:0xf bank_mask:0xf\n\t"
      "v_add_f32_dpp v236, v238, v236 quad_perm:[2,2,2,2] row_mask:0xf bank_mask:0xf\n\t"
      "v_add_f32_dpp v224, v227, v224 quad_perm:[3,3,3,3] row_mask:0xf bank_mask:0xf\n\t"
      "v_add_f32_dpp v228, v231, v228 quad_perm:[3,3,3,3] row_mask:0xf bank_mask:0xf\n\t"
      "v_add_f32_dpp v232, v235, v232 quad_perm:[3,3,3,3] row_mask:0xf bank_mask:0xf\n\t"
      "v_add_f32_dpp v236, v239, v236 quad_perm:[3,3,3,3] row_mask:0xf bank_mask:0xf\n\t"
      "v_mov_b32_dpp v240, v240 quad_perm:[0,0,0,0] row_mask:0xf bank_mask:0xf\n\t"
      "v_mov_b32_dpp v244, v244 quad_perm:[0,0,0,0] row_mask:0xf bank_mask:0xf\n\t"
      "v_mov_b32_dpp v248, v248 quad_perm:[0,0,0,0] row_mask:0xf bank_mask:0xf\n\t"
      "v_mov_b32_dpp v252, v252 quad_perm:[0,0,0,0] row_mask:0xf bank_mask:0xf\n\t"
      "v_add_f32_dpp v240, v241, v240 quad_perm:[1,1,1,1] row_mask:0xf bank_mask:0xf\n\t"
      "v_add_f32_dpp v244, v245, v244 quad_perm:[1,1,1,1] row_mask:0xf bank_mask:0xf\n\t"
      "v_add_f32_dpp v248, v249, v248 quad_perm:[1,1,1,1] row_mask:0xf bank_mask:0xf\n\t"
      "v_add_f32_dpp v252, v253, v252 quad_perm:[1,1,1,1] row_mask:0xf bank_mask:0xf\n\t"
      "v_add_f32_dpp v240, v242, v240 quad_perm:[2,2,2,2] row_mask:0xf bank_mask:0xf\n\t"
      "v_add_f32_dpp v244, v246, v244 quad_perm:[2,2,2,2] row_mask:0xf bank_mask:0xf\n\t"
      "v_add_f32_dpp v248, v250, v248 quad_perm:[2,2,2,2] row_mask:0xf bank_mask:0xf\n\t"
      "v_add_f32_dpp v252, v254, v252 quad_perm:[2,2,2,2] row_mask:0xf bank_mask:0xf\n\t"
      "v_add_f32_dpp v240, v243, v240 quad_perm:[3,3,3,3] row_mask:0xf bank_mask:0xf\n\t"
      "v_add_f32_dpp v244, v247, v244 quad_perm:[3,3,3,3] row_mask:0xf bank_mask:0xf\n\t"
      "v_add_f32_dpp v248, v251, v248 quad_perm:[3,3,3,3] row_mask:0xf bank_mask:0xf\n\t"
      "v_add_f32_dpp v252, v255, v252 quad_perm:[3,3,3,3] row_mask:0xf bank_mask:0xf\n\t"
      "v_max3_f32 v22, v26, v27, v20\n\t"
      "v_max3_f32 v23, v28, v29, v30\n\t"
      "v_max3_f32 v22, v22, v31, v32\n\t"
      "v_max3_f32 v23, v23, v33, v224\n\t"
      "v_max3_f32 v22, v22, v228, v232\n\t"
      "v_max3_f32 v23, v23, v236, v240\n\t"
      "v_max3_f32 v22, v22, v244, v248\n\t"
      "v_max3_f32 v22, v22, v23, v252\n\t"
      "v_mov_b32 v23, v22\n\t"
      "s_nop 1\n\t"
      "v_permlane16_swap_b32 v22, v23\n\t"
      "s_nop 0\n\t"
      "v_max_f32 v22, v22, v23\n\t"
      "v_mov_b32 v23, v22\n\t"
      "s_nop 1\n\t"
      "v_permlane32_swap_b32 v22, v23\n\t"
      "s_nop 0\n\t"
      "v_max_f32 v22, v22, v23\n\t"
      "v_sub_f32 v23, v20, v22\n\t"
      "v_mul_f32 v24, %[nc2], v22\n\t"
      "v_mul_f32 v23, %[c2], v23\n\t"
      "v_mov_b32 v20, v22\n\t"
      "v_exp_f32 v23, v23\n\t"
      "v_fma_f32 v26, v26, %[c2], v24\n\t"
      "v_fma_f32 v27, v27, %[c2], v24\n\t"
      "v_fma_f32 v28, v28, %[c2], v24\n\t"
      "v_fma_f32 v29, v29, %[c2], v24\n\t"
      "v_fma_f32 v30, v30, %[c2], v24\n\t"
      "v_fma_f32 v31, v31, %[c2], v24\n\t"
      "v_fma_f32 v32, v32, %[c2], v24\n\t"
      "v_fma_f32 v33, v33, %[c2], v24\n\t"
      "v_fma_f32 v224, v224, %[c2], v24\n\t"
      "v_fma_f32 v228, v228, %[c2], v24\n\t"
      "v_fma_f32 v232, v232, %[c2], v24\n\t"
      "v_fma_f32 v236, v236, %[c2], v24\n\t"
      "v_fma_f32 v240, v240, %[c2], v24\n\t"
      "v_fma_f32 v244, v244, %[c2], v24\n\t"
      "v_fma_f32 v248, v248, %[c2], v24\n\t"
      "v_fma_f32 v252, v252, %[c2], v24\n\t"
      "v_exp_f32 v26, v26\n\t"
      "v_mul_f32 v12, v23, v12\n\t"
      "v_exp_f32 v27, v27\n\t"
      "v_mul_f32 v13, v23, v13\n\t"
      "v_exp_f32 v28, v28\n\t"
      "v_mul_f32 v14, v23, v14\n\t"
      "v_exp_f32 v29, v29\n\t"
      "v_mul_f32 v15, v23, v15\n\t"
      "v_exp_f32 v30, v30\n\t"
      "v_mul_f32 v16, v23, v16\n\t"
      "v_exp_f32 v31, v31\n\t"
      "v_mul_f32 v17, v23, v17\n\t"
      "v_exp_f32 v32, v32\n\t"
      "v_mul_f32 v18, v23, v18\n\t"
      "v_exp_f32 v33, v33\n\t"
      "v_mul_f32 v19, v23, v19\n\t"
      "v_exp_f32 v224, v224\n\t"
      "v_exp_f32 v228, v228\n\t"
      "v_exp_f32 v232, v232\n\t"
      "v_exp_f32 v236, v236\n\t"
      "v_exp_f32 v240, v240\n\t"
      "v_exp_f32 v244, v244\n\t"
      "v_exp_f32 v248, v248\n\t"
      "v_exp_f32 v252, v252\n\t"
      "v_mul_f32 v21, v21, v23\n\t"
      "v_add_f32 v22, v26, v27\n\t"
      "v_add_f32 v23, v28, v29\n\t"
      "v_add_f32 v22, v22, v30\n\t"
      "v_add_f32 v23, v23, v31\n\t"
      "v_add_f32 v22, v22, v32\n\t"
      "v_add_f32 v23, v23, v33\n\t"
      "v_add_f32 v22, v22, v224\n\t"
      "v_add_f32 v23, v23, v228\n\t"
      "v_add_f32 v22, v22, v232\n\t"
      "v_add_f32 v23, v23, v236\n\t"
      "v_add_f32 v22, v22, v240\n\t"
      "v_add_f32 v23, v23, v244\n\t"
      "v_add_f32 v22, v22, v248\n\t"
      "v_add_f32 v23, v23, v252\n\t"
      "v_add_f32 v21, v21, v22\n\t"
      "v_cvt_pk_bf16_f32 v34, v26, v27\n\t"
      "v_cvt_pk_bf16_f32 v35, v28, v29\n\t"
      "v_cvt_pk_bf16_f32 v36, v30, v31\n\t"
      "v_cvt_pk_bf16_f32 v37, v32, v33\n\t"
      "v_add_f32 v21, v21, v23\n\t"
      "v_cvt_pk_bf16_f32 v38, v224, v228\n\t"
      "v_cvt_pk_bf16_f32 v39, v232, v236\n\t"
      "v_cvt_pk_bf16_f32 v24, v240, v244\n\t"
      "v_cvt_pk_bf16_f32 v25, v248, v252\n\t"
      "1:\n\t"
      "s_memrealtime s[96:97]\n\t"
      "s_waitcnt lgkmcnt(0)\n\t"
      "s_sub_u32 %[so0], s96, %[due]\n\t"
      "s_cmp_lt_i32 %[so0], 0\n\t"
      "s_cbranch_scc0 2f\n\t"
      "s_sleep 1\n\t"
      "s_branch 1b\n\t"
      "2:\n\t"
      "s_add_u32 %[due], %[due], %[dlh]\n\t"
      "s_add_u32 %[so0], %[rot], 81920\n\t"
      "s_and_b32 %[so0], %[so0], 0x1ffff\n\t"
      "s_add_u32 %[so1], %[so0], 4096\n\t"
      "buffer_load_dwordx4 a[64:67], %[koff], %[rk], %[so0] offen offset:0 nt\n\t"
      "buffer_load_dwordx4 a[68:71], %[koff], %[rk], %[so0] offen offset:1024 nt\n\t"
      "buffer_load_dwordx4 a[72:75], %[koff], %[rk], %[so0] offen offset:2048 nt\n\t"
      "buffer_load_dwordx4 a[76:79], %[koff], %[rk], %[so0] offen offset:3072 nt\n\t"
      "buffer_load_dwordx4 a[80:83], %[koff], %[rk], %[so1] offen offset:0 nt\n\t"
      "buffer_load_dwordx4 a[84:87], %[koff], %[rk], %[so1] offen offset:1024 nt\n\t"
      "buffer_load_dwordx4 a[88:91], %[koff], %[rk], %[so1] offen offset:2048 nt\n\t"
      "buffer_load_dwordx4 a[92:95], %[koff], %[rk], %[so1] offen offset:3072 nt\n\t"
      "s_add_u32 %[so0], %[rot], 90112\n\t"
      "s_and_b32 %[so0], %[so0], 0x1ffff\n\t"
      "s_add_u32 %[so1], %[so0], 4096\n\t"
      "buffer_load_dwordx4 a[96:99], %[koff], %[rk], %[so0] offen offset:0 nt\n\t"
      "buffer_load_dwordx4 a[100:103], %[koff], %[rk], %[so0] offen offset:1024 nt\n\t"
      "buffer_load_dwordx4 a[104:107], %[koff], %[rk], %[so0] offen offset:2048 nt\n\t"
      "buffer_load_dwordx4 a[108:111], %[koff], %[rk], %[so0] offen offset:3072 nt\n\t"
      "buffer_load_dwordx4 a[112:115], %[koff], %[rk], %[so1] offen offset:0 nt\n\t"
      "buffer_load_dwordx4 a[116:119], %[koff], %[rk], %[so1] offen offset:1024 nt\n\t"
      "buffer_load_dwordx4 a[120:123], %[koff], %[rk], %[so1] offen offset:2048 nt\n\t"
      "buffer_load_dwordx4 a[124:127], %[koff], %[rk], %[so1] offen offset:3072 nt\n\t"
      "buffer_load_dwordx4 v[100:103], %[wgoff], %[rwu], 0 offen offset:3584\n\t"
      "s_waitcnt vmcnt(21)\n\t"
      "v_perm_b32 v128, v44, v40, %[slo]\n\t"
      "v_perm_b32 v132, v44, v40, %[shi]\n\t"
      "v_perm_b32 v129, v52, v48, %[slo]\n\t"
      "v_perm_b32 v133, v52, v48, %[shi]\n\t"
      "v_perm_b32 v130, v60, v56, %[slo]\n\t"
      "v_perm_b32 v134, v60, v56, %[shi]\n\t"
      "v_perm_b32 v131, v68, v64, %[slo]\n\t"
      "v_perm_b32 v135, v68, v64, %[shi]\n\t"
      "v_perm_b32 v136, v45, v41, %[slo]\n\t"
      "v_perm_b32 v140, v45, v41, %[shi]\n\t"
      "v_perm_b32 v137, v53, v49, %[slo]\n\t"
      "v_perm_b32 v141, v53, v49, %[shi]\n\t"
      "v_perm_b32 v138, v61, v57, %[slo]\n\t"
      "v_perm_b32 v142, v61, v57, %[shi]\n\t"
      "v_perm_b32 v139, v69, v65, %[slo]\n\t"
      "v_perm_b32 v143, v69, v65, %[shi]\n\t"
      "v_perm_b32 v144, v46, v42, %[slo]\n\t"
      "v_perm_b32 v148, v46, v42, %[shi]\n\t"
      "v_perm_b32 v145, v54, v50, %[slo]\n\t"
      "v_perm_b32 v149, v54, v50, %[shi]\n\t"
      "v_perm_b32 v146, v62, v58, %[slo]\n\t"
      "v_perm_b32 v150, v62, v58, %[shi]\n\t"
      "v_perm_b32 v147, v70, v66, %[slo]\n\t"
      "v_perm_b32 v151, v70, v66, %[shi]\n\t"
      "v_perm_b32 v152, v47, v43, %[slo]\n\t"
      "v_perm_b32 v156, v47, v43, %[shi]\n\t"
      "v_perm_b32 v153, v55, v51, %[slo]\n\t"
      "v_perm_b32 v157, v55, v51, %[shi]\n\t"
      "v_perm_b32 v154, v63, v59, %[slo]\n\t"
      "v_perm_b32 v158, v63, v59, %[shi]\n\t"
      "v_perm_b32 v155, v71, v67, %[slo]\n\t"
      "v_perm_b32 v159, v71, v67, %[shi]\n\t"
      "v_cndmask_b32 v26, 0, v34, %[m0]\n\t"
      "v_cndmask_b32 v27, 0, v35, %[m0]\n\t"
      "v_cndmask_b32 v28, 0, v36, %[m0]\n\t"
      "v_cndmask_b32 v29, 0, v37, %[m0]\n\t"
      "v_cndmask_b32 v30, 0, v34, %[m1]\n\t"
      "v_cndmask_b32 v31, 0, v35, %[m1]\n\t"
      "v_cndmask_b32 v32, 0, v36, %[m1]\n\t"
      "v_cndmask_b32 v33, 0, v37, %[m1]\n\t"
      "v_mfma_f32_16x16x32_bf16 v[12:15], v[128:131], v[26:29], v[12:15]\n\t"
      "v_mfma_f32_16x16x32_bf16 v[16:19], v[144:147], v[26:29], v[16:19]\n\t"
      "v_cndmask_b32 v26, 0, v34, %[m2]\n\t"
      "v_cndmask_b32 v27, 0, v35, %[m2]\n\t"
      "v_cndmask_b32 v28, 0, v36, %[m2]\n\t"
      "v_cndmask_b32 v29, 0, v37, %[m2]\n\t"
      "v_mfma_f32_16x16x32_bf16 v[12:15], v[132:135], v[30:33], v[12:15]\n\t"
      "v_mfma_f32_16x16x32_bf16 v[16:19], v[148:151], v[30:33], v[16:19]\n\t"
      "v_cndmask_b32 v30, 0, v34, %[m3]\n\t"
      "v_cndmask_b32 v31, 0, v35, %[m3]\n\t"
      "v_cndmask_b32 v32, 0, v36, %[m3]\n\t"
      "v_cndmask_b32 v33, 0, v37, %[m3]\n\t"
      "v_mfma_f32_16x16x32_bf16 v[12:15], v[136:139], v[26:29], v[12:15]\n\t"
      "v_mfma_f32_16x16x32_bf16 v[16:19], v[152:155], v[26:29], v[16:19]\n\t"
      "v_mfma_f32_16x16x32_bf16 v[12:15], v[140:143], v[30:33], v[12:15]\n\t"
      "v_mfma_f32_16x16x32_bf16 v[16:19], v[156:159], v[30:33], v[16:19]\n\t"
      "ds_write_b128 %[lds], v[128:131] offset:16384\n\t"
      "ds_write_b128 %[lds], v[132:135] offset:17408\n\t"
      "ds_write_b128 %[lds], v[136:139] offset:18432\n\t"
      "ds_write_b128 %[lds], v[140:143] offset:19456\n\t"
      "ds_write_b128 %[lds], v[144:147] offset:20480\n\t"
      "ds_write_b128 %[lds], v[148:151] offset:21504\n\t"
      "ds_write_b128 %[lds], v[152:155] offset:22528\n\t"
      "ds_write_b128 %[lds], v[156:159] offset:23552\n\t"
      "v_perm_b32 v192, v164, v160, %[slo]\n\t"
      "v_perm_b32 v196, v164, v160, %[shi]\n\t"
      "v_perm_b32 v193, v172, v168, %[slo]\n\t"
      "v_perm_b32 v197, v172, v168, %[shi]\n\t"
      "v_perm_b32 v194, v180, v176, %[slo]\n\t"
      "v_perm_b32 v198, v180, v176, %[shi]\n\t"
      "v_perm_b32 v195, v188, v184, %[slo]\n\t"
      "v_perm_b32 v199, v188, v184, %[shi]\n\t"
      "v_perm_b32 v200, v165, v161, %[slo]\n\t"
      "v_perm_b32 v204, v165, v161, %[shi]\n\t"
      "v_perm_b32 v201, v173, v169, %[slo]\n\t"
      "v_perm_b32 v205, v173, v169, %[shi]\n\t"
      "v_perm_b32 v202, v181, v177, %[slo]\n\t"
      "v_perm_b32 v206, v181, v177, %[shi]\n\t"
      "v_perm_b32 v203, v189, v185, %[slo]\n\t"
      "v_perm_b32 v207, v189, v185, %[shi]\n\t"
      "v_perm_b32 v208, v166, v162, %[slo]\n\t"
      "v_perm_b32 v212, v166, v162, %[shi]\n\t"
      "v_perm_b32 v209, v174, v170, %[slo]\n\t"
      "v_perm_b32 v213, v174, v170, %[shi]\n\t"
      "v_perm_b32 v210, v182, v178, %[slo]\n\t"
      "v_perm_b32 v214, v182, v178, %[shi]\n\t"
      "v_perm_b32 v211, v190, v186, %[slo]\n\t"
      "v_perm_b32 v215, v190, v186, %[shi]\n\t"
      "v_perm_b32 v216, v167, v163, %[slo]\n\t"
      "v_perm_b32 v220, v167, v163, %[shi]\n\t"
      "v_perm_b32 v217, v175, v171, %[slo]\n\t"
      "v_perm_b32 v221, v175, v171, %[shi]\n\t"
      "v_perm_b32 v218, v183, v179, %[slo]\n\t"
      "v_perm_b32 v222, v183, v179, %[shi]\n\t"
      "v_perm_b32 v219, v191, v187, %[slo]\n\t"
      "v_perm_b32 v223, v191, v187, %[shi]\n\t"
      "v_cndmask_b32 v26, 0, v38, %[m0]\n\t"
      "v_cndmask_b32 v27, 0, v39, %[m0]\n\t"
      "v_cndmask_b32 v28, 0, v24, %[m0]\n\t"
      "v_cndmask_b32 v29, 0, v25, %[m0]\n\t"
      "v_cndmask_b32 v30, 0, v38, %[m1]\n\t"
      "v_cndmask_b32 v31, 0, v39, %[m1]\n\t"
      "v_cndmask_b32 v32, 0, v24, %[m1]\n\t"
      "v_cndmask_b32 v33, 0, v25, %[m1]\n\t"
      "v_mfma_f32_16x16x32_bf16 v[12:15], v[192:195], v[26:29], v[12:15]\n\t"
      "v_mfma_f32_16x16x32_bf16 v[16:19], v[208:211], v[26:29], v[16:19]\n\t"
      "v_cndmask_b32 v26, 0, v38, %[m2]\n\t"
      "v_cndmask_b32 v27, 0, v39, %[m2]\n\t"
      "v_cndmask_b32 v28, 0, v24, %[m2]\n\t"
      "v_cndmask_b32 v29, 0, v25, %[m2]\n\t"
      "v_mfma_f32_16x16x32_bf16 v[12:15], v[196:199], v[30:33], v[12:15]\n\t"
      "v_mfma_f32_16x16x32_bf16 v[16:19], v[212:215], v[30:33], v[16:19]\n\t"
      "v_cndmask_b32 v30, 0, v38, %[m3]\n\t"
      "v_cndmask_b32 v31, 0, v39, %[m3]\n\t"
      "v_cndmask_b32 v32, 0, v24, %[m3]\n\t"
      "v_cndmask_b32 v33, 0, v25, %[m3]\n\t"
      "v_mfma_f32_16x16x32_bf16 v[12:15], v[200:203], v[26:29], v[12:15]\n\t"
      "v_mfma_f32_16x16x32_bf16 v[16:19], v[216:219], v[26:29], v[16:19]\n\t"
      "v_mfma_f32_16x16x32_bf16 v[12:15], v[204:207], v[30:33], v[12:15]\n\t"
      "v_mfma_f32_16x16x32_bf16 v[16:19], v[220:223], v[30:33], v[16:19]\n\t"
      "ds_write_b128 %[lds], v[192:195] offset:24576\n\t"
      "ds_write_b128 %[lds], v[196:199] offset:25600\n\t"
      "ds_write_b128 %[lds], v[200:203] offset:26624\n\t"
      "ds_write_b128 %[lds], v[204:207] offset:27648\n\t"
      "ds_write_b128 %[lds], v[208:211] offset:28672\n\t"
      "ds_write_b128 %[lds], v[212:215] offset:29696\n\t"
      "ds_write_b128 %[lds], v[216:219] offset:30720\n\t"
      "ds_write_b128 %[lds], v[220:223] offset:31744\n\t"
      "1:\n\t"
      "s_memrealtime s[96:97]\n\t"
      "s_waitcnt lgkmcnt(0)\n\t"
      "s_sub_u32 %[so0], s96, %[due]\n\t"
      "s_cmp_lt_i32 %[so0], 0\n\t"
      "s_cbranch_scc0 2f\n\t"
      "s_sleep 1\n\t"
      "s_branch 1b\n\t"
      "2:\n\t"
      "s_add_u32 %[due], %[due], %[dlh]\n\t"
      "s_add_u32 %[so0], %[rot], 81920\n\t"
      "s_and_b32 %[so0], %[so0], 0x1ffff\n\t"
      "s_add_u32 %[so1], %[so0], 4096\n\t"
      "buffer_load_dwordx4 v[224:227], %[voff], %[rv], %[so0] offen offset:0 nt\n\t"
      "buffer_load_dwordx4 v[228:231], %[voff], %[rv], %[so0] offen offset:1024 nt\n\t"
      "buffer_load_dwordx4 v[232:235], %[voff], %[rv], %[so0] offen offset:2048 nt\n\t"
      "buffer_load_dwordx4 v[236:239], %[voff], %[rv], %[so0] offen offset:3072 nt\n\t"
      "buffer_load_dwordx4 v[240:243], %[voff], %[rv], %[so1] offen offset:0 nt\n\t"
      "buffer_load_dwordx4 v[244:247], %[voff], %[rv], %[so1] offen offset:1024 nt\n\t"
      "buffer_load_dwordx4 v[248:251], %[voff], %[rv], %[so1] offen offset:2048 nt\n\t"
      "buffer_load_dwordx4 v[252:255], %[voff], %[rv], %[so1] offen offset:3072 nt\n\t"
      "s_add_u32 %[so0], %[rot], 90112\n\t"
      "s_and_b32 %[so0], %[so0], 0x1ffff\n\t"
      "s_add_u32 %[so1], %[so0], 4096\n\t"
      "buffer_load_dwordx4 v[160:163], %[voff], %[rv], %[so0] offen offset:0 nt\n\t"
      "buffer_load_dwordx4 v[164:167], %[voff], %[rv], %[so0] offen offset:1024 nt\n\t"
      "buffer_load_dwordx4 v[168:171], %[voff], %[rv], %[so0] offen offset:2048 nt\n\t"
      "buffer_load_dwordx4 v[172:175], %[voff], %[rv], %[so0] offen offset:3072 nt\n\t"
      "buffer_load_dwordx4 v[176:179], %[voff], %[rv], %[so1] offen offset:0 nt\n\t"
      "buffer_load_dwordx4 v[180:183], %[voff], %[rv], %[so1] offen offset:1024 nt\n\t"
      "buffer_load_dwordx4 v[184:187], %[voff], %[rv], %[so1] offen offset:2048 nt\n\t"
      "buffer_load_dwordx4 v[188:191], %[voff], %[rv], %[so1] offen offset:3072 nt\n\t"
      "buffer_load_dword v120, %[wdoff], %[rwd], 0 offen offset:0 nt\n\t"
      "buffer_load_dword v121, %[wdoff], %[rwd], 0 offen offset:1024 nt\n\t"
      "buffer_load_dword v122, %[wdoff], %[rwd], 0 offen offset:2048 nt\n\t"
      "buffer_load_dword v123, %[wdoff], %[rwd], 0 offen offset:3072 nt\n\t"
      "s_waitcnt vmcnt(21)\n\t"
      "v_mfma_f32_16x16x32_bf16 v[128:131], a[64:67], v[8:11], 0\n\t"
      "v_mfma_f32_16x16x32_bf16 v[132:135], a[68:71], v[8:11], 0\n\t"
      "v_mfma_f32_16x16x32_bf16 v[136:139], a[72:75], v[8:11], 0\n\t"
      "v_mfma_f32_16x16x32_bf16 v[140:143], a[76:79], v[8:11], 0\n\t"
      "v_mfma_f32_16x16x32_bf16 v[144:147], a[80:83], v[8:11], 0\n\t"
      "v_mfma_f32_16x16x32_bf16 v[148:151], a[84:87], v[8:11], 0\n\t"
      "v_mfma_f32_16x16x32_bf16 v[152:155], a[88:91], v[8:11], 0\n\t"
      "v_mfma_f32_16x16x32_bf16 v[156:159], a[92:95], v[8:11], 0\n\t"
      "s_nop 1\n\t"
      "v_mov_b32_dpp v26, v128 quad_perm:[0,0,0,0] row_mask:0xf bank_mask:0xf\n\t"
      "v_mov_b32_dpp v27, v132 quad_perm:[0,0,0,0] row_mask:0xf bank_mask:0xf\n\t"
      "v_mov_b32_dpp v28, v136 quad_perm:[0,0,0,0] row_mask:0xf bank_mask:0xf\n\t"
      "v_mov_b32_dpp v29, v140 quad_perm:[0,0,0,0] row_mask:0xf bank_mask:0xf\n\t"
      "v_add_f32_dpp v26, v129, v26 quad_perm:[1,1,1,1] row_mask:0xf bank_mask:0xf\n\t"
      "v_add_f32_dpp v27, v133, v27 quad_perm:[1,1,1,1] row_mask:0xf bank_mask:0xf\n\t"
      "v_add_f32_dpp v28, v137, v28 quad_perm:[1,1,1,1] row_mask:0xf bank_mask:0xf\n\t"
      "v_add_f32_dpp v29, v141, v29 quad_perm:[1,1,1,1] row_mask:0xf bank_mask:0xf\n\t"
      "v_add_f32_dpp v26, v130, v26 quad_perm:[2,2,2,2] row_mask:0xf bank_mask:0xf\n\t"
      "v_add_f32_dpp v27, v134, v27 quad_perm:[2,2,2,2] row_mask:0xf bank_mask:0xf\n\t"
      "v_add_f32_dpp v28, v138, v28 quad_perm:[2,2,2,2] row_mask:0xf bank_mask:0xf\n\t"
      "v_add_f32_dpp v29, v142, v29 quad_perm:[2,2,2,2] row_mask:0xf bank_mask:0xf\n\t"
      "v_add_f32_dpp v26, v131, v26 quad_perm:[3,3,3,3] row_mask:0xf bank_mask:0xf\n\t"
      "v_add_f32_dpp v27, v135, v27 quad_perm:[3,3,3,3] row_mask:0xf bank_mask:0xf\n\t"
      "v_add_f32_dpp v28, v139, v28 quad_perm:[3,3,3,3] row_mask:0xf bank_mask:0xf\n\t"
      "v_add_f32_dpp v29, v143, v29 quad_perm:[3,3,3,3] row_mask:0xf bank_mask:0xf\n\t"
      "v_mov_b32_dpp v30, v144 quad_perm:[0,0,0,0] row_mask:0xf bank_mask:0xf\n\t"
      "v_mov_b32_dpp v31, v148 quad_perm:[0,0,0,0] row_mask:0xf bank_mask:0xf\n\t"
      "v_mov_b32_dpp v32, v152 quad_perm:[0,0,0,0] row_mask:0xf bank_mask:0xf\n\t"
      "v_mov_b32_dpp v33, v156 quad_perm:[0,0,0,0] row_mask:0xf bank_mask:0xf\n\t"
      "v_add_f32_dpp v30, v145, v30 quad_perm:[1,1,1,1] row_mask:0xf bank_mask:0xf\n\t"
      "v_add_f32_dpp v31, v149, v31 quad_perm:[1,1,1,1] row_mask:0xf bank_mask:0xf\n\t"
      "v_add_f32_dpp v32, v153, v32 quad_perm:[1,1,1,1] row_mask:0xf bank_mask:0xf\n\t"
      "v_add_f32_dpp v33, v157, v33 quad_perm:[1,1,1,1] row_mask:0xf bank_mask:0xf\n\t"
      "v_add_f32_dpp v30, v146, v30 quad_perm:[2,2,2,2] row_mask:0xf bank_mask:0xf\n\t"
      "v_add_f32_dpp v31, v150, v31 quad_perm:[2,2,2,2] row_mask:0xf bank_mask:0xf\n\t"
      "v_add_f32_dpp v32, v154, v32 quad_perm:[2,2,2,2] row_mask:0xf bank_mask:0xf\n\t"
      "v_add_f32_dpp v33, v158, v33 quad_perm:[2,2,2,2] row_mask:0xf bank_mask:0xf\n\t"
      "v_add_f32_dpp v30, v147, v30 quad_perm:[3,3,3,3] row_mask:0xf bank_mask:0xf\n\t"
      "v_add_f32_dpp v31, v151, v31 quad_perm:[3,3,3,3] row_mask:0xf bank_mask:0xf\n\t"
      "v_add_f32_dpp v32, v155, v32 quad_perm:[3,3,3,3] row_mask:0xf bank_mask:0xf\n\t"
      "v_add_f32_dpp v33, v159, v33 quad_perm:[3,3,3,3] row_mask:0xf bank_mask:0xf\n\t"
      "v_mfma_f32_16x16x32_bf16 v[128:131], a[96:99], v[8:11], 0\n\t"
      "v_mfma_f32_16x16x32_bf16 v[132:135], a[100:103], v[8:11], 0\n\t"
      "v_mfma_f32_16x16x32_bf16 v[136:139], a[104:107], v[8:11], 0\n\t"
      "v_mfma_f32_16x16x32_bf16 v[140:143], a[108:111], v[8:11], 0\n\t"
      "v_mfma_f32_16x16x32_bf16 v[144:147], a[112:115], v[8:11], 0\n\t"
      "v_mfma_f32_16x16x32_bf16 v[148:151], a[116:119], v[8:11], 0\n\t"
      "v_mfma_f32_16x16x32_bf16 v[152:155], a[120:123], v[8:11], 0\n\t"
      "v_mfma_f32_16x16x32_bf16 v[156:159], a[124:127], v[8:11], 0\n\t"
      "s_nop 1\n\t"
      "v_mov_b32_dpp v128, v128 quad_perm:[0,0,0,0] row_mask:0xf bank_mask:0xf\n\t"
      "v_mov_b32_dpp v132, v132 quad_perm:[0,0,0,0] row_mask:0xf bank_mask:0xf\n\t"
      "v_mov_b32_dpp v136, v136 quad_perm:[0,0,0,0] row_mask:0xf bank_mask:0xf\n\t"
      "v_mov_b32_dpp v140, v140 quad_perm:[0,0,0,0] row_mask:0xf bank_mask:0xf\n\t"
      "v_add_f32_dpp v128, v129, v128 quad_perm:[1,1,1,1] row_mask:0xf bank_mask:0xf\n\t"
      "v_add_f32_dpp v132, v133, v132 quad_perm:[1,1,1,1] row_mask:0xf bank_mask:0xf\n\t"
      "v_add_f32_dpp v136, v137, v136 quad_perm:[1,1,1,1] row_mask:0xf bank_mask:0xf\n\t"
      "v_add_f32_dpp v140, v141, v140 quad_perm:[1,1,1,1] row_mask:0xf bank_mask:0xf\n\t"
      "v_add_f32_dpp v128, v130, v128 quad_perm:[2,2,2,2] row_mask:0xf bank_mask:0xf\n\t"
      "v_add_f32_dpp v132, v134, v132 quad_perm:[2,2,2,2] row_mask:0xf bank_mask:0xf\n\t"
      "v_add_f32_dpp v136, v138, v136 quad_perm:[2,2,2,2] row_mask:0xf bank_mask:0xf\n\t"
      "v_add_f32_dpp v140, v142, v140 quad_perm:[2,2,2,2] row_mask:0xf bank_mask:0xf\n\t"
      "v_add_f32_dpp v128, v131, v128 quad_perm:[3,3,3,3] row_mask:0xf bank_mask:0xf\n\t"
      "v_add_f32_dpp v132, v135, v132 quad_perm:[3,3,3,3] row_mask:0xf bank_mask:0xf\n\t"
      "v_add_f32_dpp v136, v139, v136 quad_perm:[3,3,3,3] row_mask:0xf bank_mask:0xf\n\t"
      "v_add_f32_dpp v140, v143, v140 quad_perm:[3,3,3,3] row_mask:0xf bank_mask:0xf\n\t"
      "v_mov_b32_dpp v144, v144 quad_perm:[0,0,0,0] row_mask:0xf bank_mask:0xf\n\t"
      "v_mov_b32_dpp v148, v148 quad_perm:[0,0,0,0] row_mask:0xf bank_mask:0xf\n\t"
      "v_mov_b32_dpp v152, v152 quad_perm:[0,0,0,0] row_mask:0xf bank_mask:0xf\n\t"
      "v_mov_b32_dpp v156, v156 quad_perm:[0,0,0,0] row_mask:0xf bank_mask:0xf\n\t"
      "v_add_f32_dpp v144, v145, v144 quad_perm:[1,1,1,1] row_mask:0xf bank_mask:0xf\n\t"
      "v_add_f32_dpp v148, v149, v148 quad_perm:[1,1,1,1] row_mask:0xf bank_mask:0xf\n\t"
      "v_add_f32_dpp v152, v153, v152 quad_perm:[1,1,1,1] row_mask:0xf bank_mask:0xf\n\t"
      "v_add_f32_dpp v156, v157, v156 quad_perm:[1,1,1,1] row_mask:0xf bank_mask:0xf\n\t"
      "v_add_f32_dpp v144, v146, v144 quad_perm:[2,2,2,2] row_mask:0xf bank_mask:0xf\n\t"
      "v_add_f32_dpp v148, v150, v148 quad_perm:[2,2,2,2] row_mask:0xf bank_mask:0xf\n\t"
      "v_add_f32_dpp v152, v154, v152 quad_perm:[2,2,2,2] row_mask:0xf bank_mask:0xf\n\t"
      "v_add_f32_dpp v156, v158, v156 quad_perm:[2,2,2,2] row_mask:0xf bank_mask:0xf\n\t"
      "v_add_f32_dpp v144, v147, v144 quad_perm:[3,3,3,3] row_mask:0xf bank_mask:0xf\n\t"
      "v_add_f32_dpp v148, v151, v148 quad_perm:[3,3,3,3] row_mask:0xf bank_mask:0xf\n\t"
      "v_add_f32_dpp v152, v155, v152 quad_perm:[3,3,3,3] row_mask:0xf bank_mask:0xf\n\t"
      "v_add_f32_dpp v156, v159, v156 quad_perm:[3,3,3,3] row_mask:0xf bank_mask:0xf\n\t"
      "v_max3_f32 v22, v26, v27, v20\n\t"
      "v_max3_f32 v23, v28, v29, v30\n\t"
      "v_max3_f32 v22, v22, v31, v32\n\t"
      "v_max3_f32 v23, v23, v33, v128\n\t"
      "v_max3_f32 v22, v22, v132, v136\n\t"
      "v_max3_f32 v23, v23, v140, v144\n\t"
      "v_max3_f32 v22, v22, v148, v152\n\t"
      "v_max3_f32 v22, v22, v23, v156\n\t"
      "v_mov_b32 v23, v22\n\t"
      "s_nop 1\n\t"
      "v_permlane16_swap_b32 v22, v23\n\t"
      "s_nop 0\n\t"
      "v_max_f32 v22, v22, v23\n\t"
      "v_mov_b32 v23, v22\n\t"
      "s_nop 1\n\t"
      "v_permlane32_swap_b32 v22, v23\n\t"
      "s_nop 0\n\t"
      "v_max_f32 v22, v22, v23\n\t"
      "v_sub_f32 v23, v20, v22\n\t"
      "v_mul_f32 v24, %[nc2], v22\n\t"
      "v_mul_f32 v23, %[c2], v23\n\t"
      "v_mov_b32 v20, v22\n\t"
      "v_exp_f32 v23, v23\n\t"
      "v_fma_f32 v26, v26, %[c2], v24\n\t"
      "v_fma_f32 v27, v27, %[c2], v24\n\t"
      "v_fma_f32 v28, v28, %[c2], v24\n\t"
      "v_fma_f32 v29, v29, %[c2], v24\n\t"
      "v_fma_f32 v30, v30, %[c2], v24\n\t"
      "v_fma_f32 v31, v31, %[c2], v24\n\t"
      "v_fma_f32 v32, v32, %[c2], v24\n\t"
      "v_fma_f32 v33, v33, %[c2], v24\n\t"
      "v_fma_f32 v128, v128, %[c2], v24\n\t"
      "v_fma_f32 v132, v132, %[c2], v24\n\t"
      "v_fma_f32 v136, v136, %[c2], v24\n\t"
      "v_fma_f32 v140, v140, %[c2], v24\n\t"
      "v_fma_f32 v144, v144, %[c2], v24\n\t"
      "v_fma_f32 v148, v148, %[c2], v24\n\t"
      "v_fma_f32 v152, v152, %[c2], v24\n\t"
      "v_fma_f32 v156, v156, %[c2], v24\n\t"
      "v_exp_f32 v26, v26\n\t"
      "v_mul_f32 v12, v23, v12\n\t"
      "v_exp_f32 v27, v27\n\t"
      "v_mul_f32 v13, v23, v13\n\t"
      "v_exp_f32 v28, v28\n\t"
      "v_mul_f32 v14, v23, v14\n\t"
      "v_exp_f32 v29, v29\n\t"
      "v_mul_f32 v15, v23, v15\n\t"
      "v_exp_f32 v30, v30\n\t"
      "v_mul_f32 v16, v23, v16\n\t"
      "v_exp_f32 v31, v31\n\t"
      "v_mul_f32 v17, v23, v17\n\t"
      "v_exp_f32 v32, v32\n\t"
      "v_mul_f32 v18, v23, v18\n\t"
      "v_exp_f32 v33, v33\n\t"
      "v_mul_f32 v19, v23, v19\n\t"
      "v_exp_f32 v128, v128\n\t"
      "v_exp_f32 v132, v132\n\t"
      "v_exp_f32 v136, v136\n\t"
      "v_exp_f32 v140, v140\n\t"
      "v_exp_f32 v144, v144\n\t"
      "v_exp_f32 v148, v148\n\t"
      "v_exp_f32 v152, v152\n\t"
      "v_exp_f32 v156, v156\n\t"
      "v_mul_f32 v21, v21, v23\n\t"
      "v_add_f32 v22, v26, v27\n\t"
      "v_add_f32 v23, v28, v29\n\t"
      "v_add_f32 v22, v22, v30\n\t"
      "v_add_f32 v23, v23, v31\n\t"
      "v_add_f32 v22, v22, v32\n\t"
      "v_add_f32 v23, v23, v33\n\t"
      "v_add_f32 v22, v22, v128\n\t"
      "v_add_f32 v23, v23, v132\n\t"
      "v_add_f32 v22, v22, v136\n\t"
      "v_add_f32 v23, v23, v140\n\t"
      "v_add_f32 v22, v22, v144\n\t"
      "v_add_f32 v23, v23, v148\n\t"
      "v_add_f32 v22, v22, v152\n\t"
      "v_add_f32 v23, v23, v156\n\t"
      "v_add_f32 v21, v21, v22\n\t"
      "v_cvt_pk_bf16_f32 v34, v26, v27\n\t"
      "v_cvt_pk_bf16_f32 v35, v28, v29\n\t"
      "v_cvt_pk_bf16_f32 v36, v30, v31\n\t"
      "v_cvt_pk_bf16_f32 v37, v32, v33\n\t"
      "v_add_f32 v21, v21, v23\n\t"
      "v_cvt_pk_bf16_f32 v38, v128, v132\n\t"
      "v_cvt_pk_bf16_f32 v39, v136, v140\n\t"
      "v_cvt_pk_bf16_f32 v24, v144, v148\n\t"
      "v_cvt_pk_bf16_f32 v25, v152, v156\n\t"
      "1:\n\t"
      "s_memrealtime s[96:97]\n\t"
      "s_waitcnt lgkmcnt(0)\n\t"
      "s_sub_u32 %[so0], s96, %[due]\n\t"
      "s_cmp_lt_i32 %[so0], 0\n\t"
      "s_cbranch_scc0 2f\n\t"
      "s_sleep 1\n\t"
      "s_branch 1b\n\t"
      "2:\n\t"
      "s_add_u32 %[due], %[due], %[dlh]\n\t"
      "s_add_u32 %[so0], %[rot], 98304\n\t"
      "s_and_b32 %[so0], %[so0], 0x1ffff\n\t"
      "s_add_u32 %[so1], %[so0], 4096\n\t"
      "buffer_load_dwordx4 a[128:131], %[koff], %[rk], %[so0] offen offset:0 nt\n\t"
      "buffer_load_dwordx4 a[132:135], %[koff], %[rk], %[so0] offen offset:1024 nt\n\t"
      "buffer_load_dwordx4 a[136:139], %[koff], %[rk], %[so0] offen offset:2048 nt\n\t"
      "buffer_load_dwordx4 a[140:143], %[koff], %[rk], %[so0] offen offset:3072 nt\n\t"
      "buffer_load_dwordx4 a[144:147], %[koff], %[rk], %[so1] offen offset:0 nt\n\t"
      "buffer_load_dwordx4 a[148:151], %[koff], %[rk], %[so1] offen offset:1024 nt\n\t"
      "buffer_load_dwordx4 a[152:155], %[koff], %[rk], %[so1] offen offset:2048 nt\n\t"
      "buffer_load_dwordx4 a[156:159], %[koff], %[rk], %[so1] offen offset:3072 nt\n\t"
      "s_add_u32 %[so0], %[rot], 106496\n\t"
      "s_and_b32 %[so0], %[so0], 0x1ffff\n\t"
      "s_add_u32 %[so1], %[so0], 4096\n\t"
      "buffer_load_dwordx4 a[160:163], %[koff], %[rk], %[so0] offen offset:0\n\t"
      "buffer_load_dwordx4 a[164:167], %[koff], %[rk], %[so0] offen offset:1024\n\t"
      "buffer_load_dwordx4 a[168:171], %[koff], %[rk], %[so0] offen offset:2048\n\t"
      "buffer_load_dwordx4 a[172:175], %[koff], %[rk], %[so0] offen offset:3072\n\t"
      "buffer_load_dwordx4 a[176:179], %[koff], %[rk], %[so1] offen offset:0\n\t"
      "buffer_load_dwordx4 a[180:183], %[koff], %[rk], %[so1] offen offset:1024\n\t"
      "buffer_load_dwordx4 a[184:187], %[koff], %[rk], %[so1] offen offset:2048\n\t"
      "buffer_load_dwordx4 a[188:191], %[koff], %[rk], %[so1] offen offset:3072\n\t"
      "s_mov_b32 %[so0], 4096\n\t"
      "buffer_load_dword v124, %[wdoff], %[rwd], %[so0] offen offset:0 nt\n\t"
      "buffer_load_dword v125, %[wdoff], %[rwd], %[so0] offen offset:1024 nt\n\t"
      "buffer_load_dword v126, %[wdoff], %[rwd], %[so0] offen offset:2048 nt\n\t"
      "buffer_load_dword v127, %[wdoff], %[rwd], %[so0] offen offset:3072 nt\n\t"
      "s_waitcnt vmcnt(24)\n\t"
      "v_perm_b32 v192, v228, v224, %[slo]\n\t"
      "v_perm_b32 v196, v228, v224, %[shi]\n\t"
      "v_perm_b32 v193, v236, v232, %[slo]\n\t"
      "v_perm_b32 v197, v236, v232, %[shi]\n\t"
      "v_perm_b32 v194, v244, v240, %[slo]\n\t"
      "v_perm_b32 v198, v244, v240, %[shi]\n\t"
      "v_perm_b32 v195, v252, v248, %[slo]\n\t"
      "v_perm_b32 v199, v252, v248, %[shi]\n\t"
      "v_perm_b32 v200, v229, v225, %[slo]\n\t"
      "v_perm_b32 v204, v229, v225, %[shi]\n\t"
      "v_perm_b32 v201, v237, v233, %[slo]\n\t"
      "v_perm_b32 v205, v237, v233, %[shi]\n\t"
      "v_perm_b32 v202, v245, v241, %[slo]\n\t"
      "v_perm_b32 v206, v245, v241, %[shi]\n\t"
      "v_perm_b32 v203, v253, v249, %[slo]\n\t"
      "v_perm_b32 v207, v253, v249, %[shi]\n\t"
      "v_perm_b32 v208, v230, v226, %[slo]\n\t"
      "v_perm_b32 v212, v230, v226, %[shi]\n\t"
      "v_perm_b32 v209, v238, v234, %[slo]\n\t"
      "v_perm_b32 v213, v238, v234, %[shi]\n\t"
      "v_perm_b32 v210, v246, v242, %[slo]\n\t"
      "v_perm_b32 v214, v246, v242, %[shi]\n\t"
      "v_perm_b32 v211, v254, v250, %[slo]\n\t"
      "v_perm_b32 v215, v254, v250, %[shi]\n\t"
      "v_perm_b32 v216, v231, v227, %[slo]\n\t"
      "v_perm_b32 v220, v231, v227, %[shi]\n\t"
      "v_perm_b32 v217, v239, v235, %[slo]\n\t"
      "v_perm_b32 v221, v239, v235, %[shi]\n\t"
      "v_perm_b32 v218, v247, v243, %[slo]\n\t"
      "v_perm_b32 v222, v247, v243, %[shi]\n\t"
      "v_perm_b32 v219, v255, v251, %[slo]\n\t"
      "v_perm_b32 v223, v255, v251, %[shi]\n\t"
      "v_cndmask_b32 v26, 0, v34, %[m0]\n\t"
      "v_cndmask_b32 v27, 0, v35, %[m0]\n\t"
      "v_cndmask_b32 v28, 0, v36, %[m0]\n\t"
      "v_cndmask_b32 v29, 0, v37, %[m0]\n\t"
      "v_cndmask_b32 v30, 0, v34, %[m1]\n\t"
      "v_cndmask_b32 v31, 0, v35, %[m1]\n\t"
      "v_cndmask_b32 v32, 0, v36, %[m1]\n\t"
      "v_cndmask_b32 v33, 0, v37, %[m1]\n\t"
      "v_mfma_f32_16x16x32_bf16 v[12:15], v[192:195], v[26:29], v[12:15]\n\t"
      "v_mfma_f32_16x16x32_bf16 v[16:19], v[208:211], v[26:29], v[16:19]\n\t"
      "v_cndmask_b32 v26, 0, v34, %[m2]\n\t"
      "v_cndmask_b32 v27, 0, v35, %[m2]\n\t"
      "v_cndmask_b32 v28, 0, v36, %[m2]\n\t"
      "v_cndmask_b32 v29, 0, v37, %[m2]\n\t"
      "v_mfma_f32_16x16x32_bf16 v[12:15], v[196:199], v[30:33], v[12:15]\n\t"
      "v_mfma_f32_16x16x32_bf16 v[16:19], v[212:215], v[30:33], v[16:19]\n\t"
      "v_cndmask_b32 v30, 0, v34, %[m3]\n\t"
      "v_cndmask_b32 v31, 0, v35, %[m3]\n\t"
      "v_cndmask_b32 v32, 0, v36, %[m3]\n\t"
      "v_cndmask_b32 v33, 0, v37, %[m3]\n\t"
      "v_mfma_f32_16x16x32_bf16 v[12:15], v[200:203], v[26:29], v[12:15]\n\t"
      "v_mfma_f32_16x16x32_bf16 v[16:19], v[216:219], v[26:29], v[16:19]\n\t"
      "v_mfma_f32_16x16x32_bf16 v[12:15], v[204:207], v[30:33], v[12:15]\n\t"
      "v_mfma_f32_16x16x32_bf16 v[16:19], v[220:223], v[30:33], v[16:19]\n\t"
      "v_perm_b32 v128, v164, v160, %[slo]\n\t"
      "v_perm_b32 v132, v164, v160, %[shi]\n\t"
      "v_perm_b32 v129, v172, v168, %[slo]\n\t"
      "v_perm_b32 v133, v172, v168, %[shi]\n\t"
      "v_perm_b32 v130, v180, v176, %[slo]\n\t"
      "v_perm_b32 v134, v180, v176, %[shi]\n\t"
      "v_perm_b32 v131, v188, v184, %[slo]\n\t"
      "v_perm_b32 v135, v188, v184, %[shi]\n\t"
      "v_perm_b32 v136, v165, v161, %[slo]\n\t"
      "v_perm_b32 v140, v165, v161, %[shi]\n\t"
      "v_perm_b32 v137, v173, v169, %[slo]\n\t"
      "v_perm_b32 v141, v173, v169, %[shi]\n\t"
      "v_perm_b32 v138, v181, v177, %[slo]\n\t"
      "v_perm_b32 v142, v181, v177, %[shi]\n\t"
      "v_perm_b32 v139, v189, v185, %[slo]\n\t"
      "v_perm_b32 v143, v189, v185, %[shi]\n\t"
      "v_perm_b32 v144, v166, v162, %[slo]\n\t"
      "v_perm_b32 v148, v166, v162, %[shi]\n\t"
      "v_perm_b32 v145, v174, v170, %[slo]\n\t"
      "v_perm_b32 v149, v174, v170, %[shi]\n\t"
      "v_perm_b32 v146, v182, v178, %[slo]\n\t"
      "v_perm_b32 v150, v182, v178, %[shi]\n\t"
      "v_perm_b32 v147, v190, v186, %[slo]\n\t"
      "v_perm_b32 v151, v190, v186, %[shi]\n\t"
      "v_perm_b32 v152, v167, v163, %[slo]\n\t"
      "v_perm_b32 v156, v167, v163, %[shi]\n\t"
      "v_perm_b32 v153, v175, v171, %[slo]\n\t"
      "v_perm_b32 v157, v175, v171, %[shi]\n\t"
      "v_perm_b32 v154, v183, v179, %[slo]\n\t"
      "v_perm_b32 v158, v183, v179, %[shi]\n\t"
      "v_perm_b32 v155, v191, v187, %[slo]\n\t"
      "v_perm_b32 v159, v191, v187, %[shi]\n\t"
      "v_cndmask_b32 v26, 0, v38, %[m0]\n\t"
      "v_cndmask_b32 v27, 0, v39, %[m0]\n\t"
      "v_cndmask_b32 v28, 0, v24, %[m0]\n\t"
      "v_cndmask_b32 v29, 0, v25, %[m0]\n\t"
      "v_cndmask_b32 v30, 0, v38, %[m1]\n\t"
      "v_cndmask_b32 v31, 0, v39, %[m1]\n\t"
      "v_cndmask_b32 v32, 0, v24, %[m1]\n\t"
      "v_cndmask_b32 v33, 0, v25, %[m1]\n\t"
      "v_mfma_f32_16x16x32_bf16 v[12:15], v[128:131], v[26:29], v[12:15]\n\t"
      "v_mfma_f32_16x16x32_bf16 v[16:19], v[144:147], v[26:29], v[16:19]\n\t"
      "v_cndmask_b32 v26, 0, v38, %[m2]\n\t"
      "v_cndmask_b32 v27, 0, v39, %[m2]\n\t"
      "v_cndmask_b32 v28, 0, v24, %[m2]\n\t"
      "v_cndmask_b32 v29, 0, v25, %[m2]\n\t"
      "v_mfma_f32_16x16x32_bf16 v[12:15], v[132:135], v[30:33], v[12:15]\n\t"
      "v_mfma_f32_16x16x32_bf16 v[16:19], v[148:151], v[30:33], v[16:19]\n\t"
      "v_cndmask_b32 v30, 0, v38, %[m3]\n\t"
      "v_cndmask_b32 v31, 0, v39, %[m3]\n\t"
      "v_cndmask_b32 v32, 0, v24, %[m3]\n\t"
      "v_cndmask_b32 v33, 0, v25, %[m3]\n\t"
      "v_mfma_f32_16x16x32_bf16 v[12:15], v[136:139], v[26:29], v[12:15]\n\t"
      "v_mfma_f32_16x16x32_bf16 v[16:19], v[152:155], v[26:29], v[16:19]\n\t"
      "v_mfma_f32_16x16x32_bf16 v[12:15], v[140:143], v[30:33], v[12:15]\n\t"
      "v_mfma_f32_16x16x32_bf16 v[16:19], v[156:159], v[30:33], v[16:19]\n\t"
      "1:\n\t"
      "s_memrealtime s[96:97]\n\t"
      "s_waitcnt lgkmcnt(0)\n\t"
      "s_sub_u32 %[so0], s96, %[due]\n\t"
      "s_cmp_lt_i32 %[so0], 0\n\t"
      "s_cbranch_scc0 2f\n\t"
      "s_sleep 1\n\t"
      "s_branch 1b\n\t"
      "2:\n\t"
      "s_add_u32 %[due], %[due], %[dlh]\n\t"
      "s_add_u32 %[so0], %[rot], 98304\n\t"
      "s_and_b32 %[so0], %[so0], 0x1ffff\n\t"
      "s_add_u32 %[so1], %[so0], 4096\n\t"
      "buffer_load_dwordx4 v[224:227], %[voff], %[rv], %[so0] offen offset:0\n\t"
      "buffer_load_dwordx4 v[228:231], %[voff], %[rv], %[so0] offen offset:1024\n\t"
      "buffer_load_dwordx4 v[232:235], %[voff], %[rv], %[so0] offen offset:2048\n\t"
      "buffer_load_dwordx4 v[236:239], %[voff], %[rv], %[so0] offen offset:3072\n\t"
      "buffer_load_dwordx4 v[240:243], %[voff], %[rv], %[so1] offen offset:0\n\t"
      "buffer_load_dwordx4 v[244:247], %[voff], %[rv], %[so1] offen offset:1024\n\t"
      "buffer_load_dwordx4 v[248:251], %[voff], %[rv], %[so1] offen offset:2048\n\t"
      "buffer_load_dwordx4 v[252:255], %[voff], %[rv], %[so1] offen offset:3072\n\t"
      "s_add_u32 %[so0], %[rot], 106496\n\t"
      "s_and_b32 %[so0], %[so0], 0x1ffff\n\t"
      "s_add_u32 %[so1], %[so0], 4096\n\t"
      "buffer_load_dwordx4 v[40:43], %[voff], %[rv], %[so0] offen offset:0\n\t"
      "buffer_load_dwordx4 v[44:47], %[voff], %[rv], %[so0] offen offset:1024\n\t"
      "buffer_load_dwordx4 v[48:51], %[voff], %[rv], %[so0] offen offset:2048\n\t"
      "buffer_load_dwordx4 v[52:55], %[voff], %[rv], %[so0] offen offset:3072\n\t"
      "buffer_load_dwordx4 v[56:59], %[voff], %[rv], %[so1] offen offset:0\n\t"
      "buffer_load_dwordx4 v[60:63], %[voff], %[rv], %[so1] offen offset:1024\n\t"
      "buffer_load_dwordx4 v[64:67], %[voff], %[rv], %[so1] offen offset:2048\n\t"
      "buffer_load_dwordx4 v[68:71], %[voff], %[rv], %[so1] offen offset:3072\n\t"
      "s_waitcnt vmcnt(20)\n\t"
      "v_mfma_f32_16x16x32_bf16 v[160:163], a[128:131], v[8:11], 0\n\t"
      "v_mfma_f32_16x16x32_bf16 v[164:167], a[132:135], v[8:11], 0\n\t"
      "v_mfma_f32_16x16x32_bf16 v[168:171], a[136:139], v[8:11], 0\n\t"
      "v_mfma_f32_16x16x32_bf16 v[172:175], a[140:143], v[8:11], 0\n\t"
      "v_mfma_f32_16x16x32_bf16 v[176:179], a[144:147], v[8:11], 0\n\t"
      "v_mfma_f32_16x16x32_bf16 v[180:183], a[148:151], v[8:11], 0\n\t"
      "v_mfma_f32_16x16x32_bf16 v[184:187], a[152:155], v[8:11], 0\n\t"
      "v_mfma_f32_16x16x32_bf16 v[188:191], a[156:159], v[8:11], 0\n\t"
      "s_nop 1\n\t"
      "v_mov_b32_dpp v26, v160 quad_perm:[0,0,0,0] row_mask:0xf bank_mask:0xf\n\t"
      "v_mov_b32_dpp v27, v164 quad_perm:[0,0,0,0] row_mask:0xf bank_mask:0xf\n\t"
      "v_mov_b32_dpp v28, v168 quad_perm:[0,0,0,0] row_mask:0xf bank_mask:0xf\n\t"
      "v_mov_b32_dpp v29, v172 quad_perm:[0,0,0,0] row_mask:0xf bank_mask:0xf\n\t"
      "v_add_f32_dpp v26, v161, v26 quad_perm:[1,1,1,1] row_mask:0xf bank_mask:0xf\n\t"
      "v_add_f32_dpp v27, v165, v27 quad_perm:[1,1,1,1] row_mask:0xf bank_mask:0xf\n\t"
      "v_add_f32_dpp v28, v169, v28 quad_perm:[1,1,1,1] row_mask:0xf bank_mask:0xf\n\t"
      "v_add_f32_dpp v29, v173, v29 quad_perm:[1,1,1,1] row_mask:0xf bank_mask:0xf\n\t"
      "v_add_f32_dpp v26, v162, v26 quad_perm:[2,2,2,2] row_mask:0xf bank_mask:0xf\n\t"
      "v_add_f32_dpp v27, v166, v27 quad_perm:[2,2,2,2] row_mask:0xf bank_mask:0xf\n\t"
      "v_add_f32_dpp v28, v170, v28 quad_perm:[2,2,2,2] row_mask:0xf bank_mask:0xf\n\t"
      "v_add_f32_dpp v29, v174, v29 quad_perm:[2,2,2,2] row_mask:0xf bank_mask:0xf\n\t"
      "v_add_f32_dpp v26, v163, v26 quad_perm:[3,3,3,3] row_mask:0xf bank_mask:0xf\n\t"
      "v_add_f32_dpp v27, v167, v27 quad_perm:[3,3,3,3] row_mask:0xf bank_mask:0xf\n\t"
      "v_add_f32_dpp v28, v171, v28 quad_perm:[3,3,3,3] row_mask:0xf bank_mask:0xf\n\t"
      "v_add_f32_dpp v29, v175, v29 quad_perm:[3,3,3,3] row_mask:0xf bank_mask:0xf\n\t"
      "v_mov_b32_dpp v30, v176 quad_perm:[0,0,0,0] row_mask:0xf bank_mask:0xf\n\t"
      "v_mov_b32_dpp v31, v180 quad_perm:[0,0,0,0] row_mask:0xf bank_mask:0xf\n\t"
      "v_mov_b32_dpp v32, v184 quad_perm:[0,0,0,0] row_mask:0xf bank_mask:0xf\n\t"
      "v_mov_b32_dpp v33, v188 quad_perm:[0,0,0,0] row_mask:0xf bank_mask:0xf\n\t"
      "v_add_f32_dpp v30, v177, v30 quad_perm:[1,1,1,1] row_mask:0xf bank_mask:0xf\n\t"
      "v_add_f32_dpp v31, v181, v31 quad_perm:[1,1,1,1] row_mask:0xf bank_mask:0xf\n\t"
      "v_add_f32_dpp v32, v185, v32 quad_perm:[1,1,1,1] row_mask:0xf bank_mask:0xf\n\t"
      "v_add_f32_dpp v33, v189, v33 quad_perm:[1,1,1,1] row_mask:0xf bank_mask:0xf\n\t"
      "v_add_f32_dpp v30, v178, v30 quad_perm:[2,2,2,2] row_mask:0xf bank_mask:0xf\n\t"
      "v_add_f32_dpp v31, v182, v31 quad_perm:[2,2,2,2] row_mask:0xf bank_mask:0xf\n\t"
      "v_add_f32_dpp v32, v186, v32 quad_perm:[2,2,2,2] row_mask:0xf bank_mask:0xf\n\t"
      "v_add_f32_dpp v33, v190, v33 quad_perm:[2,2,2,2] row_mask:0xf bank_mask:0xf\n\t"
      "v_add_f32_dpp v30, v179, v30 quad_perm:[3,3,3,3] row_mask:0xf bank_mask:0xf\n\t"
      "v_add_f32_dpp v31, v183, v31 quad_perm:[3,3,3,3] row_mask:0xf bank_mask:0xf\n\t"
      "v_add_f32_dpp v32, v187, v32 quad_perm:[3,3,3,3] row_mask:0xf bank_mask:0xf\n\t"
      "v_add_f32_dpp v33, v191, v33 quad_perm:[3,3,3,3] row_mask:0xf bank_mask:0xf\n\t"
      "v_mfma_f32_16x16x32_bf16 v[160:163], a[160:163], v[8:11], 0\n\t"
      "v_mfma_f32_16x16x32_bf16 v[164:167], a[164:167], v[8:11], 0\n\t"
      "v_mfma_f32_16x16x32_bf16 v[168:171], a[168:171], v[8:11], 0\n\t"
      "v_mfma_f32_16x16x32_bf16 v[172:175], a[172:175], v[8:11], 0\n\t"
      "v_mfma_f32_16x16x32_bf16 v[176:179], a[176:179], v[8:11], 0\n\t"
      "v_mfma_f32_16x16x32_bf16 v[180:183], a[180:183], v[8:11], 0\n\t"
      "v_mfma_f32_16x16x32_bf16 v[184:187], a[184:187], v[8:11], 0\n\t"
      "v_mfma_f32_16x16x32_bf16 v[188:191], a[188:191], v[8:11], 0\n\t"
      "s_nop 1\n\t"
      "v_mov_b32_dpp v160, v160 quad_perm:[0,0,0,0] row_mask:0xf bank_mask:0xf\n\t"
      "v_mov_b32_dpp v164, v164 quad_perm:[0,0,0,0] row_mask:0xf bank_mask:0xf\n\t"
      "v_mov_b32_dpp v168, v168 quad_perm:[0,0,0,0] row_mask:0xf bank_mask:0xf\n\t"
      "v_mov_b32_dpp v172, v172 quad_perm:[0,0,0,0] row_mask:0xf bank_mask:0xf\n\t"
      "v_add_f32_dpp v160, v161, v160 quad_perm:[1,1,1,1] row_mask:0xf bank_mask:0xf\n\t"
      "v_add_f32_dpp v164, v165, v164 quad_perm:[1,1,1,1] row_mask:0xf bank_mask:0xf\n\t"
      "v_add_f32_dpp v168, v169, v168 quad_perm:[1,1,1,1] row_mask:0xf bank_mask:0xf\n\t"
      "v_add_f32_dpp v172, v173, v172 quad_perm:[1,1,1,1] row_mask:0xf bank_mask:0xf\n\t"
      "v_add_f32_dpp v160, v162, v160 quad_perm:[2,2,2,2] row_mask:0xf bank_mask:0xf\n\t"
      "v_add_f32_dpp v164, v166, v164 quad_perm:[2,2,2,2] row_mask:0xf bank_mask:0xf\n\t"
      "v_add_f32_dpp v168, v170, v168 quad_perm:[2,2,2,2] row_mask:0xf bank_mask:0xf\n\t"
      "v_add_f32_dpp v172, v174, v172 quad_perm:[2,2,2,2] row_mask:0xf bank_mask:0xf\n\t"
      "v_add_f32_dpp v160, v163, v160 quad_perm:[3,3,3,3] row_mask:0xf bank_mask:0xf\n\t"
      "v_add_f32_dpp v164, v167, v164 quad_perm:[3,3,3,3] row_mask:0xf bank_mask:0xf\n\t"
      "v_add_f32_dpp v168, v171, v168 quad_perm:[3,3,3,3] row_mask:0xf bank_mask:0xf\n\t"
      "v_add_f32_dpp v172, v175, v172 quad_perm:[3,3,3,3] row_mask:0xf bank_mask:0xf\n\t"
      "v_mov_b32_dpp v176, v176 quad_perm:[0,0,0,0] row_mask:0xf bank_mask:0xf\n\t"
      "v_mov_b32_dpp v180, v180 quad_perm:[0,0,0,0] row_mask:0xf bank_mask:0xf\n\t"
      "v_mov_b32_dpp v184, v184 quad_perm:[0,0,0,0] row_mask:0xf bank_mask:0xf\n\t"
      "v_mov_b32_dpp v188, v188 quad_perm:[0,0,0,0] row_mask:0xf bank_mask:0xf\n\t"
      "v_add_f32_dpp v176, v177, v176 quad_perm:[1,1,1,1] row_mask:0xf bank_mask:0xf\n\t"
      "v_add_f32_dpp v180, v181, v180 quad_perm:[1,1,1,1] row_mask:0xf bank_mask:0xf\n\t"
      "v_add_f32_dpp v184, v185, v184 quad_perm:[1,1,1,1] row_mask:0xf bank_mask:0xf\n\t"
      "v_add_f32_dpp v188, v189, v188 quad_perm:[1,1,1,1] row_mask:0xf bank_mask:0xf\n\t"
      "v_add_f32_dpp v176, v178, v176 quad_perm:[2,2,2,2] row_mask:0xf bank_mask:0xf\n\t"
      "v_add_f32_dpp v180, v182, v180 quad_perm:[2,2,2,2] row_mask:0xf bank_mask:0xf\n\t"
      "v_add_f32_dpp v184, v186, v184 quad_perm:[2,2,2,2] row_mask:0xf bank_mask:0xf\n\t"
      "v_add_f32_dpp v188, v190, v188 quad_perm:[2,2,2,2] row_mask:0xf bank_mask:0xf\n\t"
      "v_add_f32_dpp v176, v179, v176 quad_perm:[3,3,3,3] row_mask:0xf bank_mask:0xf\n\t"
      "v_add_f32_dpp v180, v183, v180 quad_perm:[3,3,3,3] row_mask:0xf bank_mask:0xf\n\t"
      "v_add_f32_dpp v184, v187, v184 quad_perm:[3,3,3,3] row_mask:0xf bank_mask:0xf\n\t"
      "v_add_f32_dpp v188, v191, v188 quad_perm:[3,3,3,3] row_mask:0xf bank_mask:0xf\n\t"
      "v_max3_f32 v22, v26, v27, v20\n\t"
      "v_max3_f32 v23, v28, v29, v30\n\t"
      "v_max3_f32 v22, v22, v31, v32\n\t"
      "v_max3_f32 v23, v23, v33, v160\n\t"
      "v_max3_f32 v22, v22, v164, v168\n\t"
      "v_max3_f32 v23, v23, v172, v176\n\t"
      "v_max3_f32 v22, v22, v180, v184\n\t"
      "v_max3_f32 v22, v22, v23, v188\n\t"
      "v_mov_b32 v23, v22\n\t"
      "s_nop 1\n\t"
      "v_permlane16_swap_b32 v22, v23\n\t"
      "s_nop 0\n\t"
      "v_max_f32 v22, v22, v23\n\t"
      "v_mov_b32 v23, v22\n\t"
      "s_nop 1\n\t"
      "v_permlane32_swap_b32 v22, v23\n\t"
      "s_nop 0\n\t"
      "v_max_f32 v22, v22, v23\n\t"
      "v_sub_f32 v23, v20, v22\n\t"
      "v_mul_f32 v24, %[nc2], v22\n\t"
      "v_mul_f32 v23, %[c2], v23\n\t"
      "v_mov_b32 v20, v22\n\t"
      "v_exp_f32 v23, v23\n\t"
      "v_fma_f32 v26, v26, %[c2], v24\n\t"
      "v_fma_f32 v27, v27, %[c2], v24\n\t"
      "v_fma_f32 v28, v28, %[c2], v24\n\t"
      "v_fma_f32 v29, v29, %[c2], v24\n\t"
      "v_fma_f32 v30, v30, %[c2], v24\n\t"
      "v_fma_f32 v31, v31, %[c2], v24\n\t"
      "v_fma_f32 v32, v32, %[c2], v24\n\t"
      "v_fma_f32 v33, v33, %[c2], v24\n\t"
      "v_fma_f32 v160, v160, %[c2], v24\n\t"
      "v_fma_f32 v164, v164, %[c2], v24\n\t"
      "v_fma_f32 v168, v168, %[c2], v24\n\t"
      "v_fma_f32 v172, v172, %[c2], v24\n\t"
      "v_fma_f32 v176, v176, %[c2], v24\n\t"
      "v_fma_f32 v180, v180, %[c2], v24\n\t"
      "v_fma_f32 v184, v184, %[c2], v24\n\t"
      "v_fma_f32 v188, v188, %[c2], v24\n\t"
      "v_exp_f32 v26, v26\n\t"
      "v_mul_f32 v12, v23, v12\n\t"
      "v_exp_f32 v27, v27\n\t"
      "v_mul_f32 v13, v23, v13\n\t"
      "v_exp_f32 v28, v28\n\t"
      "v_mul_f32 v14, v23, v14\n\t"
      "v_exp_f32 v29, v29\n\t"
      "v_mul_f32 v15, v23, v15\n\t"
      "v_exp_f32 v30, v30\n\t"
      "v_mul_f32 v16, v23, v16\n\t"
      "v_exp_f32 v31, v31\n\t"
      "v_mul_f32 v17, v23, v17\n\t"
      "v_exp_f32 v32, v32\n\t"
      "v_mul_f32 v18, v23, v18\n\t"
      "v_exp_f32 v33, v33\n\t"
      "v_mul_f32 v19, v23, v19\n\t"
      "v_exp_f32 v160, v160\n\t"
      "v_exp_f32 v164, v164\n\t"
      "v_exp_f32 v168, v168\n\t"
      "v_exp_f32 v172, v172\n\t"
      "v_exp_f32 v176, v176\n\t"
      "v_exp_f32 v180, v180\n\t"
      "v_exp_f32 v184, v184\n\t"
      "v_exp_f32 v188, v188\n\t"
      "v_mul_f32 v21, v21, v23\n\t"
      "v_add_f32 v22, v26, v27\n\t"
      "v_add_f32 v23, v28, v29\n\t"
      "v_add_f32 v22, v22, v30\n\t"
      "v_add_f32 v23, v23, v31\n\t"
      "v_add_f32 v22, v22, v32\n\t"
      "v_add_f32 v23, v23, v33\n\t"
      "v_add_f32 v22, v22, v160\n\t"
      "v_add_f32 v23, v23, v164\n\t"
      "v_add_f32 v22, v22, v168\n\t"
      "v_add_f32 v23, v23, v172\n\t"
      "v_add_f32 v22, v22, v176\n\t"
      "v_add_f32 v23, v23, v180\n\t"
      "v_add_f32 v22, v22, v184\n\t"
      "v_add_f32 v23, v23, v188\n\t"
      "v_add_f32 v21, v21, v22\n\t"
      "v_cvt_pk_bf16_f32 v34, v26, v27\n\t"
      "v_cvt_pk_bf16_f32 v35, v28, v29\n\t"
      "v_cvt_pk_bf16_f32 v36, v30, v31\n\t"
      "v_cvt_pk_bf16_f32 v37, v32, v33\n\t"
      "v_add_f32 v21, v21, v23\n\t"
      "v_cvt_pk_bf16_f32 v38, v160, v164\n\t"
      "v_cvt_pk_bf16_f32 v39, v168, v172\n\t"
      "v_cvt_pk_bf16_f32 v24, v176, v180\n\t"
      "v_cvt_pk_bf16_f32 v25, v184, v188\n\t"
      "1:\n\t"
      "s_memrealtime s[96:97]\n\t"
      "s_waitcnt lgkmcnt(0)\n\t"
      "s_sub_u32 %[so0], s96, %[due]\n\t"
      "s_cmp_lt_i32 %[so0], 0\n\t"
      "s_cbranch_scc0 2f\n\t"
      "s_sleep 1\n\t"
      "s_branch 1b\n\t"
      "2:\n\t"
      "s_add_u32 %[due], %[due], %[dl]\n\t"
      "s_add_u32 %[so0], %[rot], 114688\n\t"
      "s_and_b32 %[so0], %[so0], 0x1ffff\n\t"
      "s_add_u32 %[so1], %[so0], 4096\n\t"
      "buffer_load_dwordx4 a[128:131], %[koff], %[rk], %[so0] offen offset:0 nt\n\t"
      "buffer_load_dwordx4 a[132:135], %[koff], %[rk], %[so0] offen offset:1024 nt\n\t"
      "buffer_load_dwordx4 a[136:139], %[koff], %[rk], %[so0] offen offset:2048 nt\n\t"
      "buffer_load_dwordx4 a[140:143], %[koff], %[rk], %[so0] offen offset:3072 nt\n\t"
      "buffer_load_dwordx4 a[144:147], %[koff], %[rk], %[so1] offen offset:0 nt\n\t"
      "buffer_load_dwordx4 a[148:151], %[koff], %[rk], %[so1] offen offset:1024 nt\n\t"
      "buffer_load_dwordx4 a[152:155], %[koff], %[rk], %[so1] offen offset:2048 nt\n\t"
      "buffer_load_dwordx4 a[156:159], %[koff], %[rk], %[so1] offen offset:3072 nt\n\t"
      "s_add_u32 %[so0], %[rot], 122880\n\t"
      "s_and_b32 %[so0], %[so0], 0x1ffff\n\t"
      "s_add_u32 %[so1], %[so0], 4096\n\t"
      "buffer_load_dwordx4 a[160:163], %[koff], %[rk], %[so0] offen offset:0 nt\n\t"
      "buffer_load_dwordx4 a[164:167], %[koff], %[rk], %[so0] offen offset:1024 nt\n\t"
      "buffer_load_dwordx4 a[168:171], %[koff], %[rk], %[so0] offen offset:2048 nt\n\t"
      "buffer_load_dwordx4 a[172:175], %[koff], %[rk], %[so0] offen offset:3072 nt\n\t"
      "buffer_load_dwordx4 a[176:179], %[koff], %[rk], %[so1] offen offset:0 nt\n\t"
      "buffer_load_dwordx4 a[180:183], %[koff], %[rk], %[so1] offen offset:1024 nt\n\t"
      "buffer_load_dwordx4 a[184:187], %[koff], %[rk], %[so1] offen offset:2048 nt\n\t"
      "buffer_load_dwordx4 a[188:191], %[koff], %[rk], %[so1] offen offset:3072 nt\n\t"
      "s_waitcnt vmcnt(16)\n\t"
      "v_perm_b32 v22, v73, v72, %[slo]\n\t"
      "v_perm_b32 v73, v73, v72, %[shi]\n\t"
      "v_mov_b32 v72, v22\n\t"
      "v_perm_b32 v23, v75, v74, %[slo]\n\t"
      "v_perm_b32 v75, v75, v74, %[shi]\n\t"
      "v_mov_b32 v74, v23\n\t"
      "v_perm_b32 v22, v77, v76, %[slo]\n\t"
      "v_perm_b32 v77, v77, v76, %[shi]\n\t"
      "v_mov_b32 v76, v22\n\t"
      "v_perm_b32 v23, v79, v78, %[slo]\n\t"
      "v_perm_b32 v79, v79, v78, %[shi]\n\t"
      "v_mov_b32 v78, v23\n\t"
      "v_perm_b32 v22, v81, v80, %[slo]\n\t"
      "v_perm_b32 v81, v81, v80, %[shi]\n\t"
      "v_mov_b32 v80, v22\n\t"
      "v_perm_b32 v23, v83, v82, %[slo]\n\t"
      "v_perm_b32 v83, v83, v82, %[shi]\n\t"
      "v_mov_b32 v82, v23\n\t"
      "v_perm_b32 v22, v85, v84, %[slo]\n\t"
      "v_perm_b32 v85, v85, v84, %[shi]\n\t"
      "v_mov_b32 v84, v22\n\t"
      "v_perm_b32 v23, v87, v86, %[slo]\n\t"
      "v_perm_b32 v87, v87, v86, %[shi]\n\t"
      "v_mov_b32 v86, v23\n\t"
      "v_perm_b32 v22, v121, v120, %[slo]\n\t"
      "v_perm_b32 v121, v121, v120, %[shi]\n\t"
      "v_mov_b32 v120, v22\n\t"
      "v_perm_b32 v23, v123, v122, %[slo]\n\t"
      "v_perm_b32 v123, v123, v122, %[shi]\n\t"
      "v_mov_b32 v122, v23\n\t"
      "v_perm_b32 v22, v125, v124, %[slo]\n\t"
      "v_perm_b32 v125, v125, v124, %[shi]\n\t"
      "v_mov_b32 v124, v22\n\t"
      "v_perm_b32 v23, v127, v126, %[slo]\n\t"
      "v_perm_b32 v127, v127, v126, %[shi]\n\t"
      "v_mov_b32 v126, v23\n\t"
      "v_perm_b32 v22, v92, v88, %[slo]\n\t"
      "v_perm_b32 v92, v92, v88, %[shi]\n\t"
      "v_mov_b32 v88, v22\n\t"
      "v_perm_b32 v23, v93, v89, %[slo]\n\t"
      "v_perm_b32 v93, v93, v89, %[shi]\n\t"
      "v_mov_b32 v89, v23\n\t"
      "v_perm_b32 v22, v94, v90, %[slo]\n\t"
      "v_perm_b32 v94, v94, v90, %[shi]\n\t"
      "v_mov_b32 v90, v22\n\t"
      "v_perm_b32 v23, v95, v91, %[slo]\n\t"
      "v_perm_b32 v95, v95, v91, %[shi]\n\t"
      "v_mov_b32 v91, v23\n\t"
      "v_perm_b32 v22, v100, v96, %[slo]\n\t"
      "v_perm_b32 v100, v100, v96, %[shi]\n\t"
      "v_mov_b32 v96, v22\n\t"
      "v_perm_b32 v23, v101, v97, %[slo]\n\t"
      "v_perm_b32 v101, v101, v97, %[shi]\n\t"
      "v_mov_b32 v97, v23\n\t"
      "v_perm_b32 v22, v102, v98, %[slo]\n\t"
      "v_perm_b32 v102, v102, v98, %[shi]\n\t"
      "v_mov_b32 v98, v22\n\t"
      "v_perm_b32 v23, v103, v99, %[slo]\n\t"
      "v_perm_b32 v103, v103, v99, %[shi]\n\t"
      "v_mov_b32 v99, v23\n\t"
      "v_perm_b32 v160, v228, v224, %[slo]\n\t"
      "v_perm_b32 v164, v228, v224, %[shi]\n\t"
      "v_perm_b32 v161, v236, v232, %[slo]\n\t"
      "v_perm_b32 v165, v236, v232, %[shi]\n\t"
      "v_perm_b32 v162, v244, v240, %[slo]\n\t"
      "v_perm_b32 v166, v244, v240, %[shi]\n\t"
      "v_perm_b32 v163, v252, v248, %[slo]\n\t"
      "v_perm_b32 v167, v252, v248, %[shi]\n\t"
      "v_perm_b32 v168, v229, v225, %[slo]\n\t"
      "v_perm_b32 v172, v229, v225, %[shi]\n\t"
      "v_perm_b32 v169, v237, v233, %[slo]\n\t"
      "v_perm_b32 v173, v237, v233, %[shi]\n\t"
      "v_perm_b32 v170, v245, v241, %[slo]\n\t"
      "v_perm_b32 v174, v245, v241, %[shi]\n\t"
      "v_perm_b32 v171, v253, v249, %[slo]\n\t"
      "v_perm_b32 v175, v253, v249, %[shi]\n\t"
      "v_perm_b32 v176, v230, v226, %[slo]\n\t"
      "v_perm_b32 v180, v230, v226, %[shi]\n\t"
      "v_perm_b32 v177, v238, v234, %[slo]\n\t"
      "v_perm_b32 v181, v238, v234, %[shi]\n\t"
      "v_perm_b32 v178, v246, v242, %[slo]\n\t"
      "v_perm_b32 v182, v246, v242, %[shi]\n\t"
      "v_perm_b32 v179, v254, v250, %[slo]\n\t"
      "v_perm_b32 v183, v254, v250, %[shi]\n\t"
      "v_perm_b32 v184, v231, v227, %[slo]\n\t"
      "v_perm_b32 v188, v231, v227, %[shi]\n\t"
      "v_perm_b32 v185, v239, v235, %[slo]\n\t"
      "v_perm_b32 v189, v239, v235, %[shi]\n\t"
      "v_perm_b32 v186, v247, v243, %[slo]\n\t"
      "v_perm_b32 v190, v247, v243, %[shi]\n\t"
      "v_perm_b32 v187, v255, v251, %[slo]\n\t"
      "v_perm_b32 v191, v255, v251, %[shi]\n\t"
      "v_cndmask_b32 v26, 0, v34, %[m0]\n\t"
      "v_cndmask_b32 v27, 0, v35, %[m0]\n\t"
      "v_cndmask_b32 v28, 0, v36, %[m0]\n\t"
      "v_cndmask_b32 v29, 0, v37, %[m0]\n\t"
      "v_cndmask_b32 v30, 0, v34, %[m1]\n\t"
      "v_cndmask_b32 v31, 0, v35, %[m1]\n\t"
      "v_cndmask_b32 v32, 0, v36, %[m1]\n\t"
      "v_cndmask_b32 v33, 0, v37, %[m1]\n\t"
      "v_mfma_f32_16x16x32_bf16 v[12:15], v[160:163], v[26:29], v[12:15]\n\t"
      "v_mfma_f32_16x16x32_bf16 v[16:19], v[176:179], v[26:29], v[16:19]\n\t"
      "v_cndmask_b32 v26, 0, v34, %[m2]\n\t"
      "v_cndmask_b32 v27, 0, v35, %[m2]\n\t"
      "v_cndmask_b32 v28, 0, v36, %[m2]\n\t"
      "v_cndmask_b32 v29, 0, v37, %[m2]\n\t"
      "v_mfma_f32_16x16x32_bf16 v[12:15], v[164:167], v[30:33], v[12:15]\n\t"
      "v_mfma_f32_16x16x32_bf16 v[16:19], v[180:183], v[30:33], v[16:19]\n\t"
      "v_cndmask_b32 v30, 0, v34, %[m3]\n\t"
      "v_cndmask_b32 v31, 0, v35, %[m3]\n\t"
      "v_cndmask_b32 v32, 0, v36, %[m3]\n\t"
      "v_cndmask_b32 v33, 0, v37, %[m3]\n\t"
      "v_mfma_f32_16x16x32_bf16 v[12:15], v[168:171], v[26:29], v[12:15]\n\t"
      "v_mfma_f32_16x16x32_bf16 v[16:19], v[184:187], v[26:29], v[16:19]\n\t"
      "v_mfma_f32_16x16x32_bf16 v[12:15], v[172:175], v[30:33], v[12:15]\n\t"
      "v_mfma_f32_16x16x32_bf16 v[16:19], v[188:191], v[30:33], v[16:19]\n\t"
      "v_perm_b32 v224, v44, v40, %[slo]\n\t"
      "v_perm_b32 v228, v44, v40, %[shi]\n\t"
      "v_perm_b32 v225, v52, v48, %[slo]\n\t"
      "v_perm_b32 v229, v52, v48, %[shi]\n\t"
      "v_perm_b32 v226, v60, v56, %[slo]\n\t"
      "v_perm_b32 v230, v60, v56, %[shi]\n\t"
      "v_perm_b32 v227, v68, v64, %[slo]\n\t"
      "v_perm_b32 v231, v68, v64, %[shi]\n\t"
      "v_perm_b32 v232, v45, v41, %[slo]\n\t"
      "v_perm_b32 v236, v45, v41, %[shi]\n\t"
      "v_perm_b32 v233, v53, v49, %[slo]\n\t"
      "v_perm_b32 v237, v53, v49, %[shi]\n\t"
      "v_perm_b32 v234, v61, v57, %[slo]\n\t"
      "v_perm_b32 v238, v61, v57, %[shi]\n\t"
      "v_perm_b32 v235, v69, v65, %[slo]\n\t"
      "v_perm_b32 v239, v69, v65, %[shi]\n\t"
      "v_perm_b32 v240, v46, v42, %[slo]\n\t"
      "v_perm_b32 v244, v46, v42, %[shi]\n\t"
      "v_perm_b32 v241, v54, v50, %[slo]\n\t"
      "v_perm_b32 v245, v54, v50, %[shi]\n\t"
      "v_perm_b32 v242, v62, v58, %[slo]\n\t"
      "v_perm_b32 v246, v62, v58, %[shi]\n\t"
      "v_perm_b32 v243, v70, v66, %[slo]\n\t"
      "v_perm_b32 v247, v70, v66, %[shi]\n\t"
      "v_perm_b32 v248, v47, v43, %[slo]\n\t"
      "v_perm_b32 v252, v47, v43, %[shi]\n\t"
      "v_perm_b32 v249, v55, v51, %[slo]\n\t"
      "v_perm_b32 v253, v55, v51, %[shi]\n\t"
      "v_perm_b32 v250, v63, v59, %[slo]\n\t"
      "v_perm_b32 v254, v63, v59, %[shi]\n\t"
      "v_perm_b32 v251, v71, v67, %[slo]\n\t"
      "v_perm_b32 v255, v71, v67, %[shi]\n\t"
      "v_cndmask_b32 v26, 0, v38, %[m0]\n\t"
      "v_cndmask_b32 v27, 0, v39, %[m0]\n\t"
      "v_cndmask_b32 v28, 0, v24, %[m0]\n\t"
      "v_cndmask_b32 v29, 0, v25, %[m0]\n\t"
      "v_cndmask_b32 v30, 0, v38, %[m1]\n\t"
      "v_cndmask_b32 v31, 0, v39, %[m1]\n\t"
      "v_cndmask_b32 v32, 0, v24, %[m1]\n\t"
      "v_cndmask_b32 v33, 0, v25, %[m1]\n\t"
      "v_mfma_f32_16x16x32_bf16 v[12:15], v[224:227], v[26:29], v[12:15]\n\t"
      "v_mfma_f32_16x16x32_bf16 v[16:19], v[240:243], v[26:29], v[16:19]\n\t"
      "v_cndmask_b32 v26, 0, v38, %[m2]\n\t"
      "v_cndmask_b32 v27, 0, v39, %[m2]\n\t"
      "v_cndmask_b32 v28, 0, v24, %[m2]\n\t"
      "v_cndmask_b32 v29, 0, v25, %[m2]\n\t"
      "v_mfma_f32_16x16x32_bf16 v[12:15], v[228:231], v[30:33], v[12:15]\n\t"
      "v_mfma_f32_16x16x32_bf16 v[16:19], v[244:247], v[30:33], v[16:19]\n\t"
      "v_cndmask_b32 v30, 0, v38, %[m3]\n\t"
      "v_cndmask_b32 v31, 0, v39, %[m3]\n\t"
      "v_cndmask_b32 v32, 0, v24, %[m3]\n\t"
      "v_cndmask_b32 v33, 0, v25, %[m3]\n\t"
      "v_mfma_f32_16x16x32_bf16 v[12:15], v[232:235], v[26:29], v[12:15]\n\t"
      "v_mfma_f32_16x16x32_bf16 v[16:19], v[248:251], v[26:29], v[16:19]\n\t"
      "v_mfma_f32_16x16x32_bf16 v[12:15], v[236:239], v[30:33], v[12:15]\n\t"
      "v_mfma_f32_16x16x32_bf16 v[16:19], v[252:255], v[30:33], v[16:19]\n\t"
      "1:\n\t"
      "s_memrealtime s[96:97]\n\t"
      "s_waitcnt lgkmcnt(0)\n\t"
      "s_sub_u32 %[so0], s96, %[due]\n\t"
      "s_cmp_lt_i32 %[so0], 0\n\t"
      "s_cbranch_scc0 2f\n\t"
      "s_sleep 1\n\t"
      "s_branch 1b\n\t"
      "2:\n\t"
      "s_add_u32 %[due], %[due], %[dl]\n\t"
      "s_add_u32 %[so0], %[rot], 114688\n\t"
      "s_and_b32 %[so0], %[so0], 0x1ffff\n\t"
      "s_add_u32 %[so1], %[so0], 4096\n\t"
      "buffer_load_dwordx4 v[160:163], %[voff], %[rv], %[so0] offen offset:0 nt\n\t"
      "buffer_load_dwordx4 v[164:167], %[voff], %[rv], %[so0] offen offset:1024 nt\n\t"
      "buffer_load_dwordx4 v[168:171], %[voff], %[rv], %[so0] offen offset:2048 nt\n\t"
      "buffer_load_dwordx4 v[172:175], %[voff], %[rv], %[so0] offen offset:3072 nt\n\t"
      "buffer_load_dwordx4 v[176:179], %[voff], %[rv], %[so1] offen offset:0 nt\n\t"
      "buffer_load_dwordx4 v[180:183], %[voff], %[rv], %[so1] offen offset:1024 nt\n\t"
      "buffer_load_dwordx4 v[184:187], %[voff], %[rv], %[so1] offen offset:2048 nt\n\t"
      "buffer_load_dwordx4 v[188:191], %[voff], %[rv], %[so1] offen offset:3072 nt\n\t"
      "s_add_u32 %[so0], %[rot], 122880\n\t"
      "s_and_b32 %[so0], %[so0], 0x1ffff\n\t"
      "s_add_u32 %[so1], %[so0], 4096\n\t"
      "buffer_load_dwordx4 v[40:43], %[voff], %[rv], %[so0] offen offset:0 nt\n\t"
      "buffer_load_dwordx4 v[44:47], %[voff], %[rv], %[so0] offen offset:1024 nt\n\t"
      "buffer_load_dwordx4 v[48:51], %[voff], %[rv], %[so0] offen offset:2048 nt\n\t"
      "buffer_load_dwordx4 v[52:55], %[voff], %[rv], %[so0] offen offset:3072 nt\n\t"
      "buffer_load_dwordx4 v[56:59], %[voff], %[rv], %[so1] offen offset:0 nt\n\t"
      "buffer_load_dwordx4 v[60:63], %[voff], %[rv], %[so1] offen offset:1024 nt\n\t"
      "buffer_load_dwordx4 v[64:67], %[voff], %[rv], %[so1] offen offset:2048 nt\n\t"
      "buffer_load_dwordx4 v[68:71], %[voff], %[rv], %[so1] offen offset:3072 nt\n\t"
      "s_waitcnt vmcnt(16)\n\t"
      "v_mfma_f32_16x16x32_bf16 v[224:227], a[128:131], v[8:11], 0\n\t"
      "v_mfma_f32_16x16x32_bf16 v[228:231], a[132:135], v[8:11], 0\n\t"
      "v_mfma_f32_16x16x32_bf16 v[232:235], a[136:139], v[8:11], 0\n\t"
      "v_mfma_f32_16x16x32_bf16 v[236:239], a[140:143], v[8:11], 0\n\t"
      "v_mfma_f32_16x16x32_bf16 v[240:243], a[144:147], v[8:11], 0\n\t"
      "v_mfma_f32_16x16x32_bf16 v[244:247], a[148:151], v[8:11], 0\n\t"
      "v_mfma_f32_16x16x32_bf16 v[248:251], a[152:155], v[8:11], 0\n\t"
      "v_mfma_f32_16x16x32_bf16 v[252:255], a[156:159], v[8:11], 0\n\t"
      "s_nop 1\n\t"
      "v_mov_b32_dpp v26, v224 quad_perm:[0,0,0,0] row_mask:0xf bank_mask:0xf\n\t"
      "v_mov_b32_dpp v27, v228 quad_perm:[0,0,0,0] row_mask:0xf bank_mask:0xf\n\t"
      "v_mov_b32_dpp v28, v232 quad_perm:[0,0,0,0] row_mask:0xf bank_mask:0xf\n\t"
      "v_mov_b32_dpp v29, v236 quad_perm:[0,0,0,0] row_mask:0xf bank_mask:0xf\n\t"
      "v_add_f32_dpp v26, v225, v26 quad_perm:[1,1,1,1] row_mask:0xf bank_mask:0xf\n\t"
      "v_add_f32_dpp v27, v229, v27 quad_perm:[1,1,1,1] row_mask:0xf bank_mask:0xf\n\t"
      "v_add_f32_dpp v28, v233, v28 quad_perm:[1,1,1,1] row_mask:0xf bank_mask:0xf\n\t"
      "v_add_f32_dpp v29, v237, v29 quad_perm:[1,1,1,1] row_mask:0xf bank_mask:0xf\n\t"
      "v_add_f32_dpp v26, v226, v26 quad_perm:[2,2,2,2] row_mask:0xf bank_mask:0xf\n\t"
      "v_add_f32_dpp v27, v230, v27 quad_perm:[2,2,2,2] row_mask:0xf bank_mask:0xf\n\t"
      "v_add_f32_dpp v28, v234, v28 quad_perm:[2,2,2,2] row_mask:0xf bank_mask:0xf\n\t"
      "v_add_f32_dpp v29, v238, v29 quad_perm:[2,2,2,2] row_mask:0xf bank_mask:0xf\n\t"
      "v_add_f32_dpp v26, v227, v26 quad_perm:[3,3,3,3] row_mask:0xf bank_mask:0xf\n\t"
      "v_add_f32_dpp v27, v231, v27 quad_perm:[3,3,3,3] row_mask:0xf bank_mask:0xf\n\t"
      "v_add_f32_dpp v28, v235, v28 quad_perm:[3,3,3,3] row_mask:0xf bank_mask:0xf\n\t"
      "v_add_f32_dpp v29, v239, v29 quad_perm:[3,3,3,3] row_mask:0xf bank_mask:0xf\n\t"
      "v_mov_b32_dpp v30, v240 quad_perm:[0,0,0,0] row_mask:0xf bank_mask:0xf\n\t"
      "v_mov_b32_dpp v31, v244 quad_perm:[0,0,0,0] row_mask:0xf bank_mask:0xf\n\t"
      "v_mov_b32_dpp v32, v248 quad_perm:[0,0,0,0] row_mask:0xf bank_mask:0xf\n\t"
      "v_mov_b32_dpp v33, v252 quad_perm:[0,0,0,0] row_mask:0xf bank_mask:0xf\n\t"
      "v_add_f32_dpp v30, v241, v30 quad_perm:[1,1,1,1] row_mask:0xf bank_mask:0xf\n\t"
      "v_add_f32_dpp v31, v245, v31 quad_perm:[1,1,1,1] row_mask:0xf bank_mask:0xf\n\t"
      "v_add_f32_dpp v32, v249, v32 quad_perm:[1,1,1,1] row_mask:0xf bank_mask:0xf\n\t"
      "v_add_f32_dpp v33, v253, v33 quad_perm:[1,1,1,1] row_mask:0xf bank_mask:0xf\n\t"
      "v_add_f32_dpp v30, v242, v30 quad_perm:[2,2,2,2] row_mask:0xf bank_mask:0xf\n\t"
      "v_add_f32_dpp v31, v246, v31 quad_perm:[2,2,2,2] row_mask:0xf bank_mask:0xf\n\t"
      "v_add_f32_dpp v32, v250, v32 quad_perm:[2,2,2,2] row_mask:0xf bank_mask:0xf\n\t"
      "v_add_f32_dpp v33, v254, v33 quad_perm:[2,2,2,2] row_mask:0xf bank_mask:0xf\n\t"
      "v_add_f32_dpp v30, v243, v30 quad_perm:[3,3,3,3] row_mask:0xf bank_mask:0xf\n\t"
      "v_add_f32_dpp v31, v247, v31 quad_perm:[3,3,3,3] row_mask:0xf bank_mask:0xf\n\t"
      "v_add_f32_dpp v32, v251, v32 quad_perm:[3,3,3,3] row_mask:0xf bank_mask:0xf\n\t"
      "v_add_f32_dpp v33, v255, v33 quad_perm:[3,3,3,3] row_mask:0xf bank_mask:0xf\n\t"
      "v_mfma_f32_16x16x32_bf16 v[224:227], a[160:163], v[8:11], 0\n\t"
      "v_mfma_f32_16x16x32_bf16 v[228:231], a[164:167], v[8:11], 0\n\t"
      "v_mfma_f32_16x16x32_bf16 v[232:235], a[168:171], v[8:11], 0\n\t"
      "v_mfma_f32_16x16x32_bf16 v[236:239], a[172:175], v[8:11], 0\n\t"
      "v_mfma_f32_16x16x32_bf16 v[240:243], a[176:179], v[8:11], 0\n\t"
      "v_mfma_f32_16x16x32_bf16 v[244:247], a[180:183], v[8:11], 0\n\t"
      "v_mfma_f32_16x16x32_bf16 v[248:251], a[184:187], v[8:11], 0\n\t"
      "v_mfma_f32_16x16x32_bf16 v[252:255], a[188:191], v[8:11], 0\n\t"
      "s_nop 1\n\t"
      "v_mov_b32_dpp v224, v224 quad_perm:[0,0,0,0] row_mask:0xf bank_mask:0xf\n\t"
      "v_mov_b32_dpp v228, v228 quad_perm:[0,0,0,0] row_mask:0xf bank_mask:0xf\n\t"
      "v_mov_b32_dpp v232, v232 quad_perm:[0,0,0,0] row_mask:0xf bank_mask:0xf\n\t"
      "v_mov_b32_dpp v236, v236 quad_perm:[0,0,0,0] row_mask:0xf bank_mask:0xf\n\t"
      "v_add_f32_dpp v224, v225, v224 quad_perm:[1,1,1,1] row_mask:0xf bank_mask:0xf\n\t"
      "v_add_f32_dpp v228, v229, v228 quad_perm:[1,1,1,1] row_mask:0xf bank_mask:0xf\n\t"
      "v_add_f32_dpp v232, v233, v232 quad_perm:[1,1,1,1] row_mask:0xf bank_mask:0xf\n\t"
      "v_add_f32_dpp v236, v237, v236 quad_perm:[1,1,1,1] row_mask:0xf bank_mask:0xf\n\t"
      "v_add_f32_dpp v224, v226, v224 quad_perm:[2,2,2,2] row_mask:0xf bank_mask:0xf\n\t"
      "v_add_f32_dpp v228, v230, v228 quad_perm:[2,2,2,2] row_mask:0xf bank_mask:0xf\n\t"
      "v_add_f32_dpp v232, v234, v232 quad_perm:[2,2,2,2] row_mask:0xf bank_mask:0xf\n\t"
      "v_add_f32_dpp v236, v238, v236 quad_perm:[2,2,2,2] row_mask:0xf bank_mask:0xf\n\t"
      "v_add_f32_dpp v224, v227, v224 quad_perm:[3,3,3,3] row_mask:0xf bank_mask:0xf\n\t"
      "v_add_f32_dpp v228, v231, v228 quad_perm:[3,3,3,3] row_mask:0xf bank_mask:0xf\n\t"
      "v_add_f32_dpp v232, v235, v232 quad_perm:[3,3,3,3] row_mask:0xf bank_mask:0xf\n\t"
      "v_add_f32_dpp v236, v239, v236 quad_perm:[3,3,3,3] row_mask:0xf bank_mask:0xf\n\t"
      "v_mov_b32_dpp v240, v240 quad_perm:[0,0,0,0] row_mask:0xf bank_mask:0xf\n\t"
      "v_mov_b32_dpp v244, v244 quad_perm:[0,0,0,0] row_mask:0xf bank_mask:0xf\n\t"
      "v_mov_b32_dpp v248, v248 quad_perm:[0,0,0,0] row_mask:0xf bank_mask:0xf\n\t"
      "v_mov_b32_dpp v252, v252 quad_perm:[0,0,0,0] row_mask:0xf bank_mask:0xf\n\t"
      "v_add_f32_dpp v240, v241, v240 quad_perm:[1,1,1,1] row_mask:0xf bank_mask:0xf\n\t"
      "v_add_f32_dpp v244, v245, v244 quad_perm:[1,1,1,1] row_mask:0xf bank_mask:0xf\n\t"
      "v_add_f32_dpp v248, v249, v248 quad_perm:[1,1,1,1] row_mask:0xf bank_mask:0xf\n\t"
      "v_add_f32_dpp v252, v253, v252 quad_perm:[1,1,1,1] row_mask:0xf bank_mask:0xf\n\t"
      "v_add_f32_dpp v240, v242, v240 quad_perm:[2,2,2,2] row_mask:0xf bank_mask:0xf\n\t"
      "v_add_f32_dpp v244, v246, v244 quad_perm:[2,2,2,2] row_mask:0xf bank_mask:0xf\n\t"
      "v_add_f32_dpp v248, v250, v248 quad_perm:[2,2,2,2] row_mask:0xf bank_mask:0xf\n\t"
      "v_add_f32_dpp v252, v254, v252 quad_perm:[2,2,2,2] row_mask:0xf bank_mask:0xf\n\t"
      "v_add_f32_dpp v240, v243, v240 quad_perm:[3,3,3,3] row_mask:0xf bank_mask:0xf\n\t"
      "v_add_f32_dpp v244, v247, v244 quad_perm:[3,3,3,3] row_mask:0xf bank_mask:0xf\n\t"
      "v_add_f32_dpp v248, v251, v248 quad_perm:[3,3,3,3] row_mask:0xf bank_mask:0xf\n\t"
      "v_add_f32_dpp v252, v255, v252 quad_perm:[3,3,3,3] row_mask:0xf bank_mask:0xf\n\t"
      "v_max3_f32 v22, v26, v27, v20\n\t"
      "v_max3_f32 v23, v28, v29, v30\n\t"
      "v_max3_f32 v22, v22, v31, v32\n\t"
      "v_max3_f32 v23, v23, v33, v224\n\t"
      "v_max3_f32 v22, v22, v228, v232\n\t"
      "v_max3_f32 v23, v23, v236, v240\n\t"
      "v_max3_f32 v22, v22, v244, v248\n\t"
      "v_max3_f32 v22, v22, v23, v252\n\t"
      "v_mov_b32 v23, v22\n\t"
      "s_nop 1\n\t"
      "v_permlane16_swap_b32 v22, v23\n\t"
      "s_nop 0\n\t"
      "v_max_f32 v22, v22, v23\n\t"
      "v_mov_b32 v23, v22\n\t"
      "s_nop 1\n\t"
      "v_permlane32_swap_b32 v22, v23\n\t"
      "s_nop 0\n\t"
      "v_max_f32 v22, v22, v23\n\t"
      "v_sub_f32 v23, v20, v22\n\t"
      "v_mul_f32 v24, %[nc2], v22\n\t"
      "v_mul_f32 v23, %[c2], v23\n\t"
      "v_mov_b32 v20, v22\n\t"
      "v_exp_f32 v23, v23\n\t"
      "v_fma_f32 v26, v26, %[c2], v24\n\t"
      "v_fma_f32 v27, v27, %[c2], v24\n\t"
      "v_fma_f32 v28, v28, %[c2], v24\n\t"
      "v_fma_f32 v29, v29, %[c2], v24\n\t"
      "v_fma_f32 v30, v30, %[c2], v24\n\t"
      "v_fma_f32 v31, v31, %[c2], v24\n\t"
      "v_fma_f32 v32, v32, %[c2], v24\n\t"
      "v_fma_f32 v33, v33, %[c2], v24\n\t"
      "v_fma_f32 v224, v224, %[c2], v24\n\t"
      "v_fma_f32 v228, v228, %[c2], v24\n\t"
      "v_fma_f32 v232, v232, %[c2], v24\n\t"
      "v_fma_f32 v236, v236, %[c2], v24\n\t"
      "v_fma_f32 v240, v240, %[c2], v24\n\t"
      "v_fma_f32 v244, v244, %[c2], v24\n\t"
      "v_fma_f32 v248, v248, %[c2], v24\n\t"
      "v_fma_f32 v252, v252, %[c2], v24\n\t"
      "v_exp_f32 v26, v26\n\t"
      "v_mul_f32 v12, v23, v12\n\t"
      "v_exp_f32 v27, v27\n\t"
      "v_mul_f32 v13, v23, v13\n\t"
      "v_exp_f32 v28, v28\n\t"
      "v_mul_f32 v14, v23, v14\n\t"
      "v_exp_f32 v29, v29\n\t"
      "v_mul_f32 v15, v23, v15\n\t"
      "v_exp_f32 v30, v30\n\t"
      "v_mul_f32 v16, v23, v16\n\t"
      "v_exp_f32 v31, v31\n\t"
      "v_mul_f32 v17, v23, v17\n\t"
      "v_exp_f32 v32, v32\n\t"
      "v_mul_f32 v18, v23, v18\n\t"
      "v_exp_f32 v33, v33\n\t"
      "v_mul_f32 v19, v23, v19\n\t"
      "v_exp_f32 v224, v224\n\t"
      "v_exp_f32 v228, v228\n\t"
      "v_exp_f32 v232, v232\n\t"
      "v_exp_f32 v236, v236\n\t"
      "v_exp_f32 v240, v240\n\t"
      "v_exp_f32 v244, v244\n\t"
      "v_exp_f32 v248, v248\n\t"
      "v_exp_f32 v252, v252\n\t"
      "v_mul_f32 v21, v21, v23\n\t"
      "v_add_f32 v22, v26, v27\n\t"
      "v_add_f32 v23, v28, v29\n\t"
      "v_add_f32 v22, v22, v30\n\t"
      "v_add_f32 v23, v23, v31\n\t"
      "v_add_f32 v22, v22, v32\n\t"
      "v_add_f32 v23, v23, v33\n\t"
      "v_add_f32 v22, v22, v224\n\t"
      "v_add_f32 v23, v23, v228\n\t"
      "v_add_f32 v22, v22, v232\n\t"
      "v_add_f32 v23, v23, v236\n\t"
      "v_add_f32 v22, v22, v240\n\t"
      "v_add_f32 v23, v23, v244\n\t"
      "v_add_f32 v22, v22, v248\n\t"
      "v_add_f32 v23, v23, v252\n\t"
      "v_add_f32 v21, v21, v22\n\t"
      "v_cvt_pk_bf16_f32 v34, v26, v27\n\t"
      "v_cvt_pk_bf16_f32 v35, v28, v29\n\t"
      "v_cvt_pk_bf16_f32 v36, v30, v31\n\t"
      "v_cvt_pk_bf16_f32 v37, v32, v33\n\t"
      "v_add_f32 v21, v21, v23\n\t"
      "v_cvt_pk_bf16_f32 v38, v224, v228\n\t"
      "v_cvt_pk_bf16_f32 v39, v232, v236\n\t"
      "v_cvt_pk_bf16_f32 v24, v240, v244\n\t"
      "v_cvt_pk_bf16_f32 v25, v248, v252\n\t"
      "s_waitcnt vmcnt(8)\n\t"
      "v_perm_b32 v224, v164, v160, %[slo]\n\t"
      "v_perm_b32 v228, v164, v160, %[shi]\n\t"
      "v_perm_b32 v225, v172, v168, %[slo]\n\t"
      "v_perm_b32 v229, v172, v168, %[shi]\n\t"
      "v_perm_b32 v226, v180, v176, %[slo]\n\t"
      "v_perm_b32 v230, v180, v176, %[shi]\n\t"
      "v_perm_b32 v227, v188, v184, %[slo]\n\t"
      "v_perm_b32 v231, v188, v184, %[shi]\n\t"
      "v_perm_b32 v232, v165, v161, %[slo]\n\t"
      "v_perm_b32 v236, v165, v161, %[shi]\n\t"
      "v_perm_b32 v233, v173, v169, %[slo]\n\t"
      "v_perm_b32 v237, v173, v169, %[shi]\n\t"
      "v_perm_b32 v234, v181, v177, %[slo]\n\t"
      "v_perm_b32 v238, v181, v177, %[shi]\n\t"
      "v_perm_b32 v235, v189, v185, %[slo]\n\t"
      "v_perm_b32 v239, v189, v185, %[shi]\n\t"
      "v_perm_b32 v240, v166, v162, %[slo]\n\t"
      "v_perm_b32 v244, v166, v162, %[shi]\n\t"
      "v_perm_b32 v241, v174, v170, %[slo]\n\t"
      "v_perm_b32 v245, v174, v170, %[shi]\n\t"
      "v_perm_b32 v242, v182, v178, %[slo]\n\t"
      "v_perm_b32 v246, v182, v178, %[shi]\n\t"
      "v_perm_b32 v243, v190, v186, %[slo]\n\t"
      "v_perm_b32 v247, v190, v186, %[shi]\n\t"
      "v_perm_b32 v248, v167, v163, %[slo]\n\t"
      "v_perm_b32 v252, v167, v163, %[shi]\n\t"
      "v_perm_b32 v249, v175, v171, %[slo]\n\t"
      "v_perm_b32 v253, v175, v171, %[shi]\n\t"
      "v_perm_b32 v250, v183, v179, %[slo]\n\t"
      "v_perm_b32 v254, v183, v179, %[shi]\n\t"
      "v_perm_b32 v251, v191, v187, %[slo]\n\t"
      "v_perm_b32 v255, v191, v187, %[shi]\n\t"
      "v_cndmask_b32 v26, 0, v34, %[m0]\n\t"
      "v_cndmask_b32 v27, 0, v35, %[m0]\n\t"
      "v_cndmask_b32 v28, 0, v36, %[m0]\n\t"
      "v_cndmask_b32 v29, 0, v37, %[m0]\n\t"
      "v_cndmask_b32 v30, 0, v34, %[m1]\n\t"
      "v_cndmask_b32 v31, 0, v35, %[m1]\n\t"
      "v_cndmask_b32 v32, 0, v36, %[m1]\n\t"
      "v_cndmask_b32 v33, 0, v37, %[m1]\n\t"
      "v_mfma_f32_16x16x32_bf16 v[12:15], v[224:227], v[26:29], v[12:15]\n\t"
      "v_mfma_f32_16x16x32_bf16 v[16:19], v[240:243], v[26:29], v[16:19]\n\t"
      "v_cndmask_b32 v26, 0, v34, %[m2]\n\t"
      "v_cndmask_b32 v27, 0, v35, %[m2]\n\t"
      "v_cndmask_b32 v28, 0, v36, %[m2]\n\t"
      "v_cndmask_b32 v29, 0, v37, %[m2]\n\t"
      "v_mfma_f32_16x16x32_bf16 v[12:15], v[228:231], v[30:33], v[12:15]\n\t"
      "v_mfma_f32_16x16x32_bf16 v[16:19], v[244:247], v[30:33], v[16:19]\n\t"
      "v_cndmask_b32 v30, 0, v34, %[m3]\n\t"
      "v_cndmask_b32 v31, 0, v35, %[m3]\n\t"
      "v_cndmask_b32 v32, 0, v36, %[m3]\n\t"
      "v_cndmask_b32 v33, 0, v37, %[m3]\n\t"
      "v_mfma_f32_16x16x32_bf16 v[12:15], v[232:235], v[26:29], v[12:15]\n\t"
      "v_mfma_f32_16x16x32_bf16 v[16:19], v[248:251], v[26:29], v[16:19]\n\t"
      "v_mfma_f32_16x16x32_bf16 v[12:15], v[236:239], v[30:33], v[12:15]\n\t"
      "v_mfma_f32_16x16x32_bf16 v[16:19], v[252:255], v[30:33], v[16:19]\n\t"
      "s_waitcnt vmcnt(0)\n\t"
      "v_perm_b32 v160, v44, v40, %[slo]\n\t"
      "v_perm_b32 v164, v44, v40, %[shi]\n\t"
      "v_perm_b32 v161, v52, v48, %[slo]\n\t"
      "v_perm_b32 v165, v52, v48, %[shi]\n\t"
      "v_perm_b32 v162, v60, v56, %[slo]\n\t"
      "v_perm_b32 v166, v60, v56, %[shi]\n\t"
      "v_perm_b32 v163, v68, v64, %[slo]\n\t"
      "v_perm_b32 v167, v68, v64, %[shi]\n\t"
      "v_perm_b32 v168, v45, v41, %[slo]\n\t"
      "v_perm_b32 v172, v45, v41, %[shi]\n\t"
      "v_perm_b32 v169, v53, v49, %[slo]\n\t"
      "v_perm_b32 v173, v53, v49, %[shi]\n\t"
      "v_perm_b32 v170, v61, v57, %[slo]\n\t"
      "v_perm_b32 v174, v61, v57, %[shi]\n\t"
      "v_perm_b32 v171, v69, v65, %[slo]\n\t"
      "v_perm_b32 v175, v69, v65, %[shi]\n\t"
      "v_perm_b32 v176, v46, v42, %[slo]\n\t"
      "v_perm_b32 v180, v46, v42, %[shi]\n\t"
      "v_perm_b32 v177, v54, v50, %[slo]\n\t"
      "v_perm_b32 v181, v54, v50, %[shi]\n\t"
      "v_perm_b32 v178, v62, v58, %[slo]\n\t"
      "v_perm_b32 v182, v62, v58, %[shi]\n\t"
      "v_perm_b32 v179, v70, v66, %[slo]\n\t"
      "v_perm_b32 v183, v70, v66, %[shi]\n\t"
      "v_perm_b32 v184, v47, v43, %[slo]\n\t"
      "v_perm_b32 v188, v47, v43, %[shi]\n\t"
      "v_perm_b32 v185, v55, v51, %[slo]\n\t"
      "v_perm_b32 v189, v55, v51, %[shi]\n\t"
      "v_perm_b32 v186, v63, v59, %[slo]\n\t"
      "v_perm_b32 v190, v63, v59, %[shi]\n\t"
      "v_perm_b32 v187, v71, v67, %[slo]\n\t"
      "v_perm_b32 v191, v71, v67, %[shi]\n\t"
      "v_cndmask_b32 v26, 0, v38, %[m0]\n\t"
      "v_cndmask_b32 v27, 0, v39, %[m0]\n\t"
      "v_cndmask_b32 v28, 0, v24, %[m0]\n\t"
      "v_cndmask_b32 v29, 0, v25, %[m0]\n\t"
      "v_cndmask_b32 v30, 0, v38, %[m1]\n\t"
      "v_cndmask_b32 v31, 0, v39, %[m1]\n\t"
      "v_cndmask_b32 v32, 0, v24, %[m1]\n\t"
      "v_cndmask_b32 v33, 0, v25, %[m1]\n\t"
      "v_mfma_f32_16x16x32_bf16 v[12:15], v[160:163], v[26:29], v[12:15]\n\t"
      "v_mfma_f32_16x16x32_bf16 v[16:19], v[176:179], v[26:29], v[16:19]\n\t"
      "v_cndmask_b32 v26, 0, v38, %[m2]\n\t"
      "v_cndmask_b32 v27, 0, v39, %[m2]\n\t"
      "v_cndmask_b32 v28, 0, v24, %[m2]\n\t"
      "v_cndmask_b32 v29, 0, v25, %[m2]\n\t"
      "v_mfma_f32_16x16x32_bf16 v[12:15], v[164:167], v[30:33], v[12:15]\n\t"
      "v_mfma_f32_16x16x32_bf16 v[16:19], v[180:183], v[30:33], v[16:19]\n\t"
      "v_cndmask_b32 v30, 0, v38, %[m3]\n\t"
      "v_cndmask_b32 v31, 0, v39, %[m3]\n\t"
      "v_cndmask_b32 v32, 0, v24, %[m3]\n\t"
      "v_cndmask_b32 v33, 0, v25, %[m3]\n\t"
      "v_mfma_f32_16x16x32_bf16 v[12:15], v[168:171], v[26:29], v[12:15]\n\t"
      "v_mfma_f32_16x16x32_bf16 v[16:19], v[184:187], v[26:29], v[16:19]\n\t"
      "v_mfma_f32_16x16x32_bf16 v[12:15], v[172:175], v[30:33], v[12:15]\n\t"
      "v_mfma_f32_16x16x32_bf16 v[16:19], v[188:191], v[30:33], v[16:19]\n\t"
      "s_waitcnt lgkmcnt(0)\n\t"
      "s_nop 7\n\t"
      : "=&{v[12:15]}"(st.a0), "=&{v[16:19]}"(st.a1), "=&{v20}"(st.m), "=&{v21}"(st.l), [so0] "=&s"(so0), [so1] "=&s"(so1), [due] "+s"(due)
      : "{v[8:11]}"(qf), PASS_IN, PASS_CONST, [dl] "s"(DL1), [dlh] "s"(DLW), [wooff] "v"(wo_off(c)), [rwo] "s"(c.rwo), [wgoff] "v"((2 * c.tid * FF + ffn_slot(c) * 8) * 2), [rwg] "s"(c.rwg), [rwu] "s"(c.rwu), [wdoff] "v"(ffn_slot(c) * 8192 + c.tid * 4), [rwd] "s"(c.rwd)
      : "memory", "v22", "v23", "v24", "v25", "v26", "v27", "v28", "v29", "v30", "v31", "v32", "v33", "v34", "v35", "v36", "v37", "v38", "v39", "v40", "v41", "v42", "v43", "v44", "v45", "v46", "v47", "v48", "v49", "v50", "v51", "v52", "v53", "v54", "v55", "v56", "v57", "v58", "v59", "v60", "v61", "v62", "v63", "v64", "v65", "v66", "v67", "v68", "v69", "v70", "v71", "scc", "vcc", "s96", "s97", "v72", "v73", "v74", "v75", "v76", "v77", "v78", "v79", "v80", "v81", "v82", "v83", "v84", "v85", "v86", "v87", "v88", "v89", "v90", "v91", "v92", "v93", "v94", "v95", "v96", "v97", "v98", "v99", "v100", "v101", "v102", "v103", "v104", "v105", "v106", "v107", "v108", "v109", "v110", "v111", "v112", "v113", "v114", "v115", "v116", "v117", "v118", "v119", "v120", "v121", "v122", "v123", "v124", "v125", "v126", "v127", "v128", "v129", "v130", "v131", "v132", "v133", "v134", "v135", "v136", "v137", "v138", "v139", "v140", "v141", "v142", "v143", "v144", "v145", "v146", "v147", "v148", "v149", "v150", "v151", "v152", "v153", "v154", "v155", "v156", "v157", "v158", "v159", "v160", "v161", "v162", "v163", "v164", "v165", "v166", "v167", "v168", "v169", "v170", "v171", "v172", "v173", "v174", "v175", "v176", "v177", "v178", "v179", "v180", "v181", "v182", "v183", "v184", "v185", "v186", "v187", "v188", "v189", "v190", "v191", "v192", "v193", "v194", "v195", "v196", "v197", "v198", "v199", "v200", "v201", "v202", "v203", "v204", "v205", "v206", "v207", "v208", "v209", "v210", "v211", "v212", "v213", "v214", "v215", "v216", "v217", "v218", "v219", "v220", "v221", "v222", "v223", "v224", "v225", "v226", "v227", "v228", "v229", "v230", "v231", "v232", "v233", "v234", "v235", "v236", "v237", "v238", "v239", "v240", "v241", "v242", "v243", "v244", "v245", "v246", "v247", "v248", "v249", "v250", "v251", "v252", "v253", "v254", "v255", "a0", "a1", "a2", "a3", "a4", "a5", "a6", "a7", "a8", "a9", "a10", "a11", "a12", "a13", "a14", "a15", "a16", "a17", "a18", "a19", "a20", "a21", "a22", "a23", "a24", "a25", "a26", "a27", "a28", "a29", "a30", "a31", "a32", "a33", "a34", "a35", "a36", "a37", "a38", "a39", "a40", "a41", "a42", "a43", "a44", "a45", "a46", "a47", "a48", "a49", "a50", "a51", "a52", "a53", "a54", "a55", "a56", "a57", "a58", "a59", "a60", "a61", "a62", "a63", "a64", "a65", "a66", "a67", "a68", "a69", "a70", "a71", "a72", "a73", "a74", "a75", "a76", "a77", "a78", "a79", "a80", "a81", "a82", "a83", "a84", "a85", "a86", "a87", "a88", "a89", "a90", "a91", "a92", "a93", "a94", "a95", "a96", "a97", "a98", "a99", "a100", "a101", "a102", "a103", "a104", "a105", "a106", "a107", "a108", "a109", "a110", "a111", "a112", "a113", "a114", "a115", "a116", "a117", "a118", "a119", "a120", "a121", "a122", "a123", "a124", "a125", "a126", "a127", "a128", "a129", "a130", "a131", "a132", "a133", "a134", "a135", "a136", "a137", "a138", "a139", "a140", "a141", "a142", "a143", "a144", "a145", "a146", "a147", "a148", "a149", "a150", "a151", "a152", "a153", "a154", "a155", "a156", "a157", "a158", "a159", "a160", "a161", "a162", "a163", "a164", "a165", "a166", "a167", "a168", "a169", "a170", "a171", "a172", "a173", "a174", "a175", "a176", "a177", "a178", "a179", "a180", "a181", "a182", "a183", "a184", "a185", "a186", "a187", "a188", "a189", "a190", "a191", "a192", "a193", "a194", "a195", "a196", "a197", "a198", "a199", "a200", "a201", "a202", "a203", "a204", "a205", "a206", "a207", "a208", "a209", "a210", "a211", "a212", "a213", "a214", "a215", "a216", "a217", "a218", "a219", "a220", "a221", "a222", "a223", "a224", "a225", "a226", "a227", "a228", "a229", "a230", "a231", "a232", "a233", "a234", "a235", "a236", "a237", "a238", "a239", "a240", "a241", "a242", "a243", "a244", "a245", "a246", "a247", "a248", "a249", "a250", "a251", "a252", "a253", "a254", "a255");
  LAUNDER();
  if (c.wave != 0) CODE_TOUCH("v70", "v71", c.tid + 104, c.tid + 296);
  middle<0>(c, st, xres, out, due - ((stg16 * DLW) >> 7) + TPOLL);
  {
    const int o = q_off(c, 1);
    const int offq[2] = {o, o + 16};
    v4u xq[2];
    gather2<DELAY_Q>(c.ws, offq, c.tag, xq);
    qf = v4u{xq[0][0], xq[0][2], xq[1][0], xq[1][2]};
  }
  LAUNDER();
  PASS_ADDR();
  due = (unsigned)__builtin_amdgcn_s_memrealtime() + ((stg16 * DL2) >> 7);
  asm volatile(
      "v_mov_b32 v12, 0\n\t"
      "v_mov_b32 v13, 0\n\t"
      "v_mov_b32 v14, 0\n\t"
      "v_mov_b32 v15, 0\n\t"
      "v_mov_b32 v16, 0\n\t"
      "v_mov_b32 v17, 0\n\t"
      "v_mov_b32 v18, 0\n\t"
      "v_mov_b32 v19, 0\n\t"
      "v_mov_b32 v20, 0xff800000\n\t"
      "v_mov_b32 v21, 0\n\t"
      "v_mfma_f32_16x16x32_bf16 v[40:43], a[128:131], v[8:11], 0\n\t"
      "v_mfma_f32_16x16x32_bf16 v[44:47], a[132:135], v[8:11], 0\n\t"
      "v_mfma_f32_16x16x32_bf16 v[48:51], a[136:139], v[8:11], 0\n\t"
      "v_mfma_f32_16x16x32_bf16 v[52:55], a[140:143], v[8:11], 0\n\t"
      "v_mfma_f32_16x16x32_bf16 v[56:59], a[144:147], v[8:11], 0\n\t"
      "v_mfma_f32_16x16x32_bf16 v[60:63], a[148:151], v[8:11], 0\n\t"
      "v_mfma_f32_16x16x32_bf16 v[64:67], a[152:155], v[8:11], 0\n\t"
      "v_mfma_f32_16x16x32_bf16 v[68:71], a[156:159], v[8:11], 0\n\t"
      "s_nop 1\n\t"
      "v_mov_b32_dpp v26, v40 quad_perm:[0,0,0,0] row_mask:0xf bank_mask:0xf\n\t"
      "v_mov_b32_dpp v27, v44 quad_perm:[0,0,0,0] row_mask:0xf bank_mask:0xf\n\t"
      "v_mov_b32_dpp v28, v48 quad_perm:[0,0,0,0] row_mask:0xf bank_mask:0xf\n\t"
      "v_mov_b32_dpp v29, v52 quad_perm:[0,0,0,0] row_mask:0xf bank_mask:0xf\n\t"
      "v_add_f32_dpp v26, v41, v26 quad_perm:[1,1,1,1] row_mask:0xf bank_mask:0xf\n\t"
      "v_add_f32_dpp v27, v45, v27 quad_perm:[1,1,1,1] row_mask:0xf bank_mask:0xf\n\t"
      "v_add_f32_dpp v28, v49, v28 quad_perm:[1,1,1,1] row_mask:0xf bank_mask:0xf\n\t"
      "v_add_f32_dpp v29, v53, v29 quad_perm:[1,1,1,1] row_mask:0xf bank_mask:0xf\n\t"
      "v_add_f32_dpp v26, v42, v26 quad_perm:[2,2,2,2] row_mask:0xf bank_mask:0xf\n\t"
      "v_add_f32_dpp v27, v46, v27 quad_perm:[2,2,2,2] row_mask:0xf bank_mask:0xf\n\t"
      "v_add_f32_dpp v28, v50, v28 quad_perm:[2,2,2,2] row_mask:0xf bank_mask:0xf\n\t"
      "v_add_f32_dpp v29, v54, v29 quad_perm:[2,2,2,2] row_mask:0xf bank_mask:0xf\n\t"
      "v_add_f32_dpp v26, v43, v26 quad_perm:[3,3,3,3] row_mask:0xf bank_mask:0xf\n\t"
      "v_add_f32_dpp v27, v47, v27 quad_perm:[3,3,3,3] row_mask:0xf bank_mask:0xf\n\t"
      "v_add_f32_dpp v28, v51, v28 quad_perm:[3,3,3,3] row_mask:0xf bank_mask:0xf\n\t"
      "v_add_f32_dpp v29, v55, v29 quad_perm:[3,3,3,3] row_mask:0xf bank_mask:0xf\n\t"
      "v_mov_b32_dpp v30, v56 quad_perm:[0,0,0,0] row_mask:0xf bank_mask:0xf\n\t"
      "v_mov_b32_dpp v31, v60 quad_perm:[0,0,0,0] row_mask:0xf bank_mask:0xf\n\t"
      "v_mov_b32_dpp v32, v64 quad_perm:[0,0,0,0] row_mask:0xf bank_mask:0xf\n\t"
      "v_mov_b32_dpp v33, v68 quad_perm:[0,0,0,0] row_mask:0xf bank_mask:0xf\n\t"
      "v_add_f32_dpp v30, v57, v30 quad_perm:[1,1,1,1] row_mask:0xf bank_mask:0xf\n\t"
      "v_add_f32_dpp v31, v61, v31 quad_perm:[1,1,1,1] row_mask:0xf bank_mask:0xf\n\t"
      "v_add_f32_dpp v32, v65, v32 quad_perm:[1,1,1,1] row_mask:0xf bank_mask:0xf\n\t"
      "v_add_f32_dpp v33, v69, v33 quad_perm:[1,1,1,1] row_mask:0xf bank_mask:0xf\n\t"
      "v_add_f32_dpp v30, v58, v30 quad_perm:[2,2,2,2] row_mask:0xf bank_mask:0xf\n\t"
      "v_add_f32_dpp v31, v62, v31 quad_perm:[2,2,2,2] row_mask:0xf bank_mask:0xf\n\t"
      "v_add_f32_dpp v32, v66, v32 quad_perm:[2,2,2,2] row_mask:0xf bank_mask:0xf\n\t"
      "v_add_f32_dpp v33, v70, v33 quad_perm:[2,2,2,2] row_mask:0xf bank_mask:0xf\n\t"
      "v_add_f32_dpp v30, v59, v30 quad_perm:[3,3,3,3] row_mask:0xf bank_mask:0xf\n\t"
      "v_add_f32_dpp v31, v63, v31 quad_perm:[3,3,3,3] row_mask:0xf bank_mask:0xf\n\t"
      "v_add_f32_dpp v32, v67, v32 quad_perm:[3,3,3,3] row_mask:0xf bank_mask:0xf\n\t"
      "v_add_f32_dpp v33, v71, v33 quad_perm:[3,3,3,3] row_mask:0xf bank_mask:0xf\n\t"
      "v_mfma_f32_16x16x32_bf16 v[40:43], a[160:163], v[8:11], 0\n\t"
      "v_mfma_f32_16x16x32_bf16 v[44:47], a[164:167], v[8:11], 0\n\t"
      "v_mfma_f32_16x16x32_bf16 v[48:51], a[168:171], v[8:11], 0\n\t"
      "v_mfma_f32_16x16x32_bf16 v[52:55], a[172:175], v[8:11], 0\n\t"
      "v_mfma_f32_16x16x32_bf16 v[56:59], a[176:179], v[8:11], 0\n\t"
      "v_mfma_f32_16x16x32_bf16 v[60:63], a[180:183], v[8:11], 0\n\t"
      "v_mfma_f32_16x16x32_bf16 v[64:67], a[184:187], v[8:11], 0\n\t"
      "v_mfma_f32_16x16x32_bf16 v[68:71], a[188:191], v[8:11], 0\n\t"
      "s_nop 1\n\t"
      "v_mov_b32_dpp v40, v40 quad_perm:[0,0,0,0] row_mask:0xf bank_mask:0xf\n\t"
      "v_mov_b32_dpp v44, v44 quad_perm:[0,0,0,0] row_mask:0xf bank_mask:0xf\n\t"
      "v_mov_b32_dpp v48, v48 quad_perm:[0,0,0,0] row_mask:0xf bank_mask:0xf\n\t"
      "v_mov_b32_dpp v52, v52 quad_perm:[0,0,0,0] row_mask:0xf bank_mask:0xf\n\t"
      "v_add_f32_dpp v40, v41, v40 quad_perm:[1,1,1,1] row_mask:0xf bank_mask:0xf\n\t"
      "v_add_f32_dpp v44, v45, v44 quad_perm:[1,1,1,1] row_mask:0xf bank_mask:0xf\n\t"
      "v_add_f32_dpp v48, v49, v48 quad_perm:[1,1,1,1] row_mask:0xf bank_mask:0xf\n\t"
      "v_add_f32_dpp v52, v53, v52 quad_perm:[1,1,1,1] row_mask:0xf bank_mask:0xf\n\t"
      "v_add_f32_dpp v40, v42, v40 quad_perm:[2,2,2,2] row_mask:0xf bank_mask:0xf\n\t"
      "v_add_f32_dpp v44, v46, v44 quad_perm:[2,2,2,2] row_mask:0xf bank_mask:0xf\n\t"
      "v_add_f32_dpp v48, v50, v48 quad_perm:[2,2,2,2] row_mask:0xf bank_mask:0xf\n\t"
      "v_add_f32_dpp v52, v54, v52 quad_perm:[2,2,2,2] row_mask:0xf bank_mask:0xf\n\t"
      "v_add_f32_dpp v40, v43, v40 quad_perm:[3,3,3,3] row_mask:0xf bank_mask:0xf\n\t"
      "v_add_f32_dpp v44, v47, v44 quad_perm:[3,3,3,3] row_mask:0xf bank_mask:0xf\n\t"
      "v_add_f32_dpp v48, v51, v48 quad_perm:[3,3,3,3] row_mask:0xf bank_mask:0xf\n\t"
      "v_add_f32_dpp v52, v55, v52 quad_perm:[3,3,3,3] row_mask:0xf bank_mask:0xf\n\t"
      "v_mov_b32_dpp v56, v56 quad_perm:[0,0,0,0] row_mask:0xf bank_mask:0xf\n\t"
      "v_mov_b32_dpp v60, v60 quad_perm:[0,0,0,0] row_mask:0xf bank_mask:0xf\n\t"
      "v_mov_b32_dpp v64, v64 quad_perm:[0,0,0,0] row_mask:0xf bank_mask:0xf\n\t"
      "v_mov_b32_dpp v68, v68 quad_perm:[0,0,0,0] row_mask:0xf bank_mask:0xf\n\t"
      "v_add_f32_dpp v56, v57, v56 quad_perm:[1,1,1,1] row_mask:0xf bank_mask:0xf\n\t"
      "v_add_f32_dpp v60, v61, v60 quad_perm:[1,1,1,1] row_mask:0xf bank_mask:0xf\n\t"
      "v_add_f32_dpp v64, v65, v64 quad_perm:[1,1,1,1] row_mask:0xf bank_mask:0xf\n\t"
      "v_add_f32_dpp v68, v69, v68 quad_perm:[1,1,1,1] row_mask:0xf bank_mask:0xf\n\t"
      "v_add_f32_dpp v56, v58, v56 quad_perm:[2,2,2,2] row_mask:0xf bank_mask:0xf\n\t"
      "v_add_f32_dpp v60, v62, v60 quad_perm:[2,2,2,2] row_mask:0xf bank_mask:0xf\n\t"
      "v_add_f32_dpp v64, v66, v64 quad_perm:[2,2,2,2] row_mask:0xf bank_mask:0xf\n\t"
      "v_add_f32_dpp v68, v70, v68 quad_perm:[2,2,2,2] row_mask:0xf bank_mask:0xf\n\t"
      "v_add_f32_dpp v56, v59, v56 quad_perm:[3,3,3,3] row_mask:0xf bank_mask:0xf\n\t"
      "v_add_f32_dpp v60, v63, v60 quad_perm:[3,3,3,3] row_mask:0xf bank_mask:0xf\n\t"
      "v_add_f32_dpp v64, v67, v64 quad_perm:[3,3,3,3] row_mask:0xf bank_mask:0xf\n\t"
      "v_add_f32_dpp v68, v71, v68 quad_perm:[3,3,3,3] row_mask:0xf bank_mask:0xf\n\t"
      "v_max3_f32 v22, v26, v27, v20\n\t"
      "v_max3_f32 v23, v28, v29, v30\n\t"
      "v_max3_f32 v22, v22, v31, v32\n\t"
      "v_max3_f32 v23, v23, v33, v40\n\t"
      "v_max3_f32 v22, v22, v44, v48\n\t"
      "v_max3_f32 v23, v23, v52, v56\n\t"
      "v_max3_f32 v22, v22, v60, v64\n\t"
      "v_max3_f32 v22, v22, v23, v68\n\t"
      "v_mov_b32 v23, v22\n\t"
      "s_nop 1\n\t"
      "v_permlane16_swap_b32 v22, v23\n\t"
      "s_nop 0\n\t"
      "v_max_f32 v22, v22, v23\n\t"
      "v_mov_b32 v23, v22\n\t"
      "s_nop 1\n\t"
      "v_permlane32_swap_b32 v22, v23\n\t"
      "s_nop 0\n\t"
      "v_max_f32 v22, v22, v23\n\t"
      "v_sub_f32 v23, v20, v22\n\t"
      "v_mul_f32 v24, %[nc2], v22\n\t"
      "v_mul_f32 v23, %[c2], v23\n\t"
      "v_mov_b32 v20, v22\n\t"
      "v_exp_f32 v23, v23\n\t"
      "v_fma_f32 v26, v26, %[c2], v24\n\t"
      "v_fma_f32 v27, v27, %[c2], v24\n\t"
      "v_fma_f32 v28, v28, %[c2], v24\n\t"
      "v_fma_f32 v29, v29, %[c2], v24\n\t"
      "v_fma_f32 v30, v30, %[c2], v24\n\t"
      "v_fma_f32 v31, v31, %[c2], v24\n\t"
      "v_fma_f32 v32, v32, %[c2], v24\n\t"
      "v_fma_f32 v33, v33, %[c2], v24\n\t"
      "v_fma_f32 v40, v40, %[c2], v24\n\t"
      "v_fma_f32 v44, v44, %[c2], v24\n\t"
      "v_fma_f32 v48, v48, %[c2], v24\n\t"
      "v_fma_f32 v52, v52, %[c2], v24\n\t"
      "v_fma_f32 v56, v56, %[c2], v24\n\t"
      "v_fma_f32 v60, v60, %[c2], v24\n\t"
      "v_fma_f32 v64, v64, %[c2], v24\n\t"
      "v_fma_f32 v68, v68, %[c2], v24\n\t"
      "v_exp_f32 v26, v26\n\t"
      "v_mul_f32 v12, v23, v12\n\t"
      "v_exp_f32 v27, v27\n\t"
      "v_mul_f32 v13, v23, v13\n\t"
      "v_exp_f32 v28, v28\n\t"
      "v_mul_f32 v14, v23, v14\n\t"
      "v_exp_f32 v29, v29\n\t"
      "v_mul_f32 v15, v23, v15\n\t"
      "v_exp_f32 v30, v30\n\t"
      "v_mul_f32 v16, v23, v16\n\t"
      "v_exp_f32 v31, v31\n\t"
      "v_mul_f32 v17, v23, v17\n\t"
      "v_exp_f32 v32, v32\n\t"
      "v_mul_f32 v18, v23, v18\n\t"
      "v_exp_f32 v33, v33\n\t"
      "v_mul_f32 v19, v23, v19\n\t"
      "v_exp_f32 v40, v40\n\t"
      "v_exp_f32 v44, v44\n\t"
      "v_exp_f32 v48, v48\n\t"
      "v_exp_f32 v52, v52\n\t"
      "v_exp_f32 v56, v56\n\t"
      "v_exp_f32 v60, v60\n\t"
      "v_exp_f32 v64, v64\n\t"
      "v_exp_f32 v68, v68\n\t"
      "v_mul_f32 v21, v21, v23\n\t"
      "v_add_f32 v22, v26, v27\n\t"
      "v_add_f32 v23, v28, v29\n\t"
      "v_add_f32 v22, v22, v30\n\t"
      "v_add_f32 v23, v23, v31\n\t"
      "v_add_f32 v22, v22, v32\n\t"
      "v_add_f32 v23, v23, v33\n\t"
      "v_add_f32 v22, v22, v40\n\t"
      "v_add_f32 v23, v23, v44\n\t"
      "v_add_f32 v22, v22, v48\n\t"
      "v_add_f32 v23, v23, v52\n\t"
      "v_add_f32 v22, v22, v56\n\t"
      "v_add_f32 v23, v23, v60\n\t"
      "v_add_f32 v22, v22, v64\n\t"
      "v_add_f32 v23, v23, v68\n\t"
      "v_add_f32 v21, v21, v22\n\t"
      "v_cvt_pk_bf16_f32 v34, v26, v27\n\t"
      "v_cvt_pk_bf16_f32 v35, v28, v29\n\t"
      "v_cvt_pk_bf16_f32 v36, v30, v31\n\t"
      "v_cvt_pk_bf16_f32 v37, v32, v33\n\t"
      "v_add_f32 v21, v21, v23\n\t"
      "v_cvt_pk_bf16_f32 v38, v40, v44\n\t"
      "v_cvt_pk_bf16_f32 v39, v48, v52\n\t"
      "v_cvt_pk_bf16_f32 v24, v56, v60\n\t"
      "v_cvt_pk_bf16_f32 v25, v64, v68\n\t"
      "1:\n\t"
      "s_memrealtime s[96:97]\n\t"
      "s_waitcnt lgkmcnt(0)\n\t"
      "s_sub_u32 %[so0], s96, %[due]\n\t"
      "s_cmp_lt_i32 %[so0], 0\n\t"
      "s_cbranch_scc0 2f\n\t"
      "s_sleep 1\n\t"
      "s_branch 1b\n\t"
      "2:\n\t"
      "s_add_u32 %[due], %[due], %[dlx]\n\t"
      "s_add_u32 %[so0], %[rot], 0\n\t"
      "s_and_b32 %[so0], %[so0], 0x1ffff\n\t"
      "s_add_u32 %[so1], %[so0], 4096\n\t"
      "buffer_load_dwordx4 a[152:155], %[koff], %[rk], %[so1] offen offset:2048 nt\n\t"
      "buffer_load_dwordx4 a[156:159], %[koff], %[rk], %[so1] offen offset:3072 nt\n\t"
      "ds_read_b128 a[128:131], %[lds] offset:32768\n\t"
      "ds_read_b128 a[132:135], %[lds] offset:33792\n\t"
      "ds_read_b128 a[136:139], %[lds] offset:34816\n\t"
      "ds_read_b128 a[140:143], %[lds] offset:35840\n\t"
      "ds_read_b128 a[144:147], %[lds] offset:36864\n\t"
      "ds_read_b128 a[148:151], %[lds] offset:37888\n\t"
      "s_add_u32 %[so0], %[rot], 8192\n\t"
      "s_and_b32 %[so0], %[so0], 0x1ffff\n\t"
      "s_add_u32 %[so1], %[so0], 4096\n\t"
      "buffer_load_dwordx4 a[160:163], %[koff], %[rk], %[so0] offen offset:0 nt\n\t"
      "buffer_load_dwordx4 a[164:167], %[koff], %[rk], %[so0] offen offset:1024 nt\n\t"
      "buffer_load_dwordx4 a[168:171], %[koff], %[rk], %[so0] offen offset:2048 nt\n\t"
      "buffer_load_dwordx4 a[172:175], %[koff], %[rk], %[so0] offen offset:3072 nt\n\t"
      "buffer_load_dwordx4 a[176:179], %[koff], %[rk], %[so1] offen offset:0 nt\n\t"
      "buffer_load_dwordx4 a[180:183], %[koff], %[rk], %[so1] offen offset:1024 nt\n\t"
      "buffer_load_dwordx4 a[184:187], %[koff], %[rk], %[so1] offen offset:2048 nt\n\t"
      "buffer_load_dwordx4 a[188:191], %[koff], %[rk], %[so1] offen offset:3072 nt\n\t"
      "v_cndmask_b32 v26, 0, v34, %[m0]\n\t"
      "v_cndmask_b32 v27, 0, v35, %[m0]\n\t"
      "v_cndmask_b32 v28, 0, v36, %[m0]\n\t"
      "v_cndmask_b32 v29, 0, v37, %[m0]\n\t"
      "v_cndmask_b32 v30, 0, v34, %[m1]\n\t"
      "v_cndmask_b32 v31, 0, v35, %[m1]\n\t"
      "v_cndmask_b32 v32, 0, v36, %[m1]\n\t"
      "v_cndmask_b32 v33, 0, v37, %[m1]\n\t"
      "v_mfma_f32_16x16x32_bf16 v[12:15], v[224:227], v[26:29], v[12:15]\n\t"
      "v_mfma_f32_16x16x32_bf16 v[16:19], v[240:243], v[26:29], v[16:19]\n\t"
      "v_cndmask_b32 v26, 0, v34, %[m2]\n\t"
      "v_cndmask_b32 v27, 0, v35, %[m2]\n\t"
      "v_cndmask_b32 v28, 0, v36, %[m2]\n\t"
      "v_cndmask_b32 v29, 0, v37, %[m2]\n\t"
      "v_mfma_f32_16x16x32_bf16 v[12:15], v[228:231], v[30:33], v[12:15]\n\t"
      "v_mfma_f32_16x16x32_bf16 v[16:19], v[244:247], v[30:33], v[16:19]\n\t"
      "v_cndmask_b32 v30, 0, v34, %[m3]\n\t"
      "v_cndmask_b32 v31, 0, v35, %[m3]\n\t"
      "v_cndmask_b32 v32, 0, v36, %[m3]\n\t"
      "v_cndmask_b32 v33, 0, v37, %[m3]\n\t"
      "v_mfma_f32_16x16x32_bf16 v[12:15], v[232:235], v[26:29], v[12:15]\n\t"
      "v_mfma_f32_16x16x32_bf16 v[16:19], v[248:251], v[26:29], v[16:19]\n\t"
      "v_mfma_f32_16x16x32_bf16 v[12:15], v[236:239], v[30:33], v[12:15]\n\t"
      "v_mfma_f32_16x16x32_bf16 v[16:19], v[252:255], v[30:33], v[16:19]\n\t"
      "v_cndmask_b32 v26, 0, v38, %[m0]\n\t"
      "v_cndmask_b32 v27, 0, v39, %[m0]\n\t"
      "v_cndmask_b32 v28, 0, v24, %[m0]\n\t"
      "v_cndmask_b32 v29, 0, v25, %[m0]\n\t"
      "v_cndmask_b32 v30, 0, v38, %[m1]\n\t"
      "v_cndmask_b32 v31, 0, v39, %[m1]\n\t"
      "v_cndmask_b32 v32, 0, v24, %[m1]\n\t"
      "v_cndmask_b32 v33, 0, v25, %[m1]\n\t"
      "v_mfma_f32_16x16x32_bf16 v[12:15], v[160:163], v[26:29], v[12:15]\n\t"
      "v_mfma_f32_16x16x32_bf16 v[16:19], v[176:179], v[26:29], v[16:19]\n\t"
      "v_cndmask_b32 v26, 0, v38, %[m2]\n\t"
      "v_cndmask_b32 v27, 0, v39, %[m2]\n\t"
      "v_cndmask_b32 v28, 0, v24, %[m2]\n\t"
      "v_cndmask_b32 v29, 0, v25, %[m2]\n\t"
      "v_mfma_f32_16x16x32_bf16 v[12:15], v[164:167], v[30:33], v[12:15]\n\t"
      "v_mfma_f32_16x16x32_bf16 v[16:19], v[180:183], v[30:33], v[16:19]\n\t"
      "v_cndmask_b32 v30, 0, v38, %[m3]\n\t"
      "v_cndmask_b32 v31, 0, v39, %[m3]\n\t"
      "v_cndmask_b32 v32, 0, v24, %[m3]\n\t"
      "v_cndmask_b32 v33, 0, v25, %[m3]\n\t"
      "v_mfma_f32_16x16x32_bf16 v[12:15], v[168:171], v[26:29], v[12:15]\n\t"
      "v_mfma_f32_16x16x32_bf16 v[16:19], v[184:187], v[26:29], v[16:19]\n\t"
      "v_mfma_f32_16x16x32_bf16 v[12:15], v[172:175], v[30:33], v[12:15]\n\t"
      "v_mfma_f32_16x16x32_bf16 v[16:19], v[188:191], v[30:33], v[16:19]\n\t"
      "v_mfma_f32_16x16x32_bf16 v[40:43], a[64:67], v[8:11], 0\n\t"
      "v_mfma_f32_16x16x32_bf16 v[44:47], a[68:71], v[8:11], 0\n\t"
      "v_mfma_f32_16x16x32_bf16 v[48:51], a[72:75], v[8:11], 0\n\t"
      "v_mfma_f32_16x16x32_bf16 v[52:55], a[76:79], v[8:11], 0\n\t"
      "v_mfma_f32_16x16x32_bf16 v[56:59], a[80:83], v[8:11], 0\n\t"
      "v_mfma_f32_16x16x32_bf16 v[60:63], a[84:87], v[8:11], 0\n\t"
      "v_mfma_f32_16x16x32_bf16 v[64:67], a[88:91], v[8:11], 0\n\t"
      "v_mfma_f32_16x16x32_bf16 v[68:71], a[92:95], v[8:11], 0\n\t"
      "s_nop 1\n\t"
      "v_mov_b32_dpp v26, v40 quad_perm:[0,0,0,0] row_mask:0xf bank_mask:0xf\n\t"
      "v_mov_b32_dpp v27, v44 quad_perm:[0,0,0,0] row_mask:0xf bank_mask:0xf\n\t"
      "v_mov_b32_dpp v28, v48 quad_perm:[0,0,0,0] row_mask:0xf bank_mask:0xf\n\t"
      "v_mov_b32_dpp v29, v52 quad_perm:[0,0,0,0] row_mask:0xf bank_mask:0xf\n\t"
      "v_add_f32_dpp v26, v41, v26 quad_perm:[1,1,1,1] row_mask:0xf bank_mask:0xf\n\t"
      "v_add_f32_dpp v27, v45, v27 quad_perm:[1,1,1,1] row_mask:0xf bank_mask:0xf\n\t"
      "v_add_f32_dpp v28, v49, v28 quad_perm:[1,1,1,1] row_mask:0xf bank_mask:0xf\n\t"
      "v_add_f32_dpp v29, v53, v29 quad_perm:[1,1,1,1] row_mask:0xf bank_mask:0xf\n\t"
      "v_add_f32_dpp v26, v42, v26 quad_perm:[2,2,2,2] row_mask:0xf bank_mask:0xf\n\t"
      "v_add_f32_dpp v27, v46, v27 quad_perm:[2,2,2,2] row_mask:0xf bank_mask:0xf\n\t"
      "v_add_f32_dpp v28, v50, v28 quad_perm:[2,2,2,2] row_mask:0xf bank_mask:0xf\n\t"
      "v_add_f32_dpp v29, v54, v29 quad_perm:[2,2,2,2] row_mask:0xf bank_mask:0xf\n\t"
      "v_add_f32_dpp v26, v43, v26 quad_perm:[3,3,3,3] row_mask:0xf bank_mask:0xf\n\t"
      "v_add_f32_dpp v27, v47, v27 quad_perm:[3,3,3,3] row_mask:0xf bank_mask:0xf\n\t"
      "v_add_f32_dpp v28, v51, v28 quad_perm:[3,3,3,3] row_mask:0xf bank_mask:0xf\n\t"
      "v_add_f32_dpp v29, v55, v29 quad_perm:[3,3,3,3] row_mask:0xf bank_mask:0xf\n\t"
      "v_mov_b32_dpp v30, v56 quad_perm:[0,0,0,0] row_mask:0xf bank_mask:0xf\n\t"
      "v_mov_b32_dpp v31, v60 quad_perm:[0,0,0,0] row_mask:0xf bank_mask:0xf\n\t"
      "v_mov_b32_dpp v32, v64 quad_perm:[0,0,0,0] row_mask:0xf bank_mask:0xf\n\t"
      "v_mov_b32_dpp v33, v68 quad_perm:[0,0,0,0] row_mask:0xf bank_mask:0xf\n\t"
      "v_add_f32_dpp v30, v57, v30 quad_perm:[1,1,1,1] row_mask:0xf bank_mask:0xf\n\t"
      "v_add_f32_dpp v31, v61, v31 quad_perm:[1,1,1,1] row_mask:0xf bank_mask:0xf\n\t"
      "v_add_f32_dpp v32, v65, v32 quad_perm:[1,1,1,1] row_mask:0xf bank_mask:0xf\n\t"
      "v_add_f32_dpp v33, v69, v33 quad_perm:[1,1,1,1] row_mask:0xf bank_mask:0xf\n\t"
      "v_add_f32_dpp v30, v58, v30 quad_perm:[2,2,2,2] row_mask:0xf bank_mask:0xf\n\t"
      "v_add_f32_dpp v31, v62, v31 quad_perm:[2,2,2,2] row_mask:0xf bank_mask:0xf\n\t"
      "v_add_f32_dpp v32, v66, v32 quad_perm:[2,2,2,2] row_mask:0xf bank_mask:0xf\n\t"
      "v_add_f32_dpp v33, v70, v33 quad_perm:[2,2,2,2] row_mask:0xf bank_mask:0xf\n\t"
      "v_add_f32_dpp v30, v59, v30 quad_perm:[3,3,3,3] row_mask:0xf bank_mask:0xf\n\t"
      "v_add_f32_dpp v31, v63, v31 quad_perm:[3,3,3,3] row_mask:0xf bank_mask:0xf\n\t"
      "v_add_f32_dpp v32, v67, v32 quad_perm:[3,3,3,3] row_mask:0xf bank_mask:0xf\n\t"
      "v_add_f32_dpp v33, v71, v33 quad_perm:[3,3,3,3] row_mask:0xf bank_mask:0xf\n\t"
      "v_mfma_f32_16x16x32_bf16 v[40:43], a[96:99], v[8:11], 0\n\t"
      "v_mfma_f32_16x16x32_bf16 v[44:47], a[100:103], v[8:11], 0\n\t"
      "v_mfma_f32_16x16x32_bf16 v[48:51], a[104:107], v[8:11], 0\n\t"
      "v_mfma_f32_16x16x32_bf16 v[52:55], a[108:111], v[8:11], 0\n\t"
      "v_mfma_f32_16x16x32_bf16 v[56:59], a[112:115], v[8:11], 0\n\t"
      "v_mfma_f32_16x16x32_bf16 v[60:63], a[116:119], v[8:11], 0\n\t"
      "v_mfma_f32_16x16x32_bf16 v[64:67], a[120:123], v[8:11], 0\n\t"
      "v_mfma_f32_16x16x32_bf16 v[68:71], a[124:127], v[8:11], 0\n\t"
      "s_nop 1\n\t"
      "v_mov_b32_dpp v40, v40 quad_perm:[0,0,0,0] row_mask:0xf bank_mask:0xf\n\t"
      "v_mov_b32_dpp v44, v44 quad_perm:[0,0,0,0] row_mask:0xf bank_mask:0xf\n\t"
      "v_mov_b32_dpp v48, v48 quad_perm:[0,0,0,0] row_mask:0xf bank_mask:0xf\n\t"
      "v_mov_b32_dpp v52, v52 quad_perm:[0,0,0,0] row_mask:0xf bank_mask:0xf\n\t"
      "v_add_f32_dpp v40, v41, v40 quad_perm:[1,1,1,1] row_mask:0xf bank_mask:0xf\n\t"
      "v_add_f32_dpp v44, v45, v44 quad_perm:[1,1,1,1] row_mask:0xf bank_mask:0xf\n\t"
      "v_add_f32_dpp v48, v49, v48 quad_perm:[1,1,1,1] row_mask:0xf bank_mask:0xf\n\t"
      "v_add_f32_dpp v52, v53, v52 quad_perm:[1,1,1,1] row_mask:0xf bank_mask:0xf\n\t"
      "v_add_f32_dpp v40, v42, v40 quad_perm:[2,2,2,2] row_mask:0xf bank_mask:0xf\n\t"
      "v_add_f32_dpp v44, v46, v44 quad_perm:[2,2,2,2] row_mask:0xf bank_mask:0xf\n\t"
      "v_add_f32_dpp v48, v50, v48 quad_perm:[2,2,2,2] row_mask:0xf bank_mask:0xf\n\t"
      "v_add_f32_dpp v52, v54, v52 quad_perm:[2,2,2,2] row_mask:0xf bank_mask:0xf\n\t"
      "v_add_f32_dpp v40, v43, v40 quad_perm:[3,3,3,3] row_mask:0xf bank_mask:0xf\n\t"
      "v_add_f32_dpp v44, v47, v44 quad_perm:[3,3,3,3] row_mask:0xf bank_mask:0xf\n\t"
      "v_add_f32_dpp v48, v51, v48 quad_perm:[3,3,3,3] row_mask:0xf bank_mask:0xf\n\t"
      "v_add_f32_dpp v52, v55, v52 quad_perm:[3,3,3,3] row_mask:0xf bank_mask:0xf\n\t"
      "v_mov_b32_dpp v56, v56 quad_perm:[0,0,0,0] row_mask:0xf bank_mask:0xf\n\t"
      "v_mov_b32_dpp v60, v60 quad_perm:[0,0,0,0] row_mask:0xf bank_mask:0xf\n\t"
      "v_mov_b32_dpp v64, v64 quad_perm:[0,0,0,0] row_mask:0xf bank_mask:0xf\n\t"
      "v_mov_b32_dpp v68, v68 quad_perm:[0,0,0,0] row_mask:0xf bank_mask:0xf\n\t"
      "v_add_f32_dpp v56, v57, v56 quad_perm:[1,1,1,1] row_mask:0xf bank_mask:0xf\n\t"
      "v_add_f32_dpp v60, v61, v60 quad_perm:[1,1,1,1] row_mask:0xf bank_mask:0xf\n\t"
      "v_add_f32_dpp v64, v65, v64 quad_perm:[1,1,1,1] row_mask:0xf bank_mask:0xf\n\t"
      "v_add_f32_dpp v68, v69, v68 quad_perm:[1,1,1,1] row_mask:0xf bank_mask:0xf\n\t"
      "v_add_f32_dpp v56, v58, v56 quad_perm:[2,2,2,2] row_mask:0xf bank_mask:0xf\n\t"
      "v_add_f32_dpp v60, v62, v60 quad_perm:[2,2,2,2] row_mask:0xf bank_mask:0xf\n\t"
      "v_add_f32_dpp v64, v66, v64 quad_perm:[2,2,2,2] row_mask:0xf bank_mask:0xf\n\t"
      "v_add_f32_dpp v68, v70, v68 quad_perm:[2,2,2,2] row_mask:0xf bank_mask:0xf\n\t"
      "v_add_f32_dpp v56, v59, v56 quad_perm:[3,3,3,3] row_mask:0xf bank_mask:0xf\n\t"
      "v_add_f32_dpp v60, v63, v60 quad_perm:[3,3,3,3] row_mask:0xf bank_mask:0xf\n\t"
      "v_add_f32_dpp v64, v67, v64 quad_perm:[3,3,3,3] row_mask:0xf bank_mask:0xf\n\t"
      "v_add_f32_dpp v68, v71, v68 quad_perm:[3,3,3,3] row_mask:0xf bank_mask:0xf\n\t"
      "v_max3_f32 v22, v26, v27, v20\n\t"
      "v_max3_f32 v23, v28, v29, v30\n\t"
      "v_max3_f32 v22, v22, v31, v32\n\t"
      "v_max3_f32 v23, v23, v33, v40\n\t"
      "v_max3_f32 v22, v22, v44, v48\n\t"
      "v_max3_f32 v23, v23, v52, v56\n\t"
      "v_max3_f32 v22, v22, v60, v64\n\t"
      "v_max3_f32 v22, v22, v23, v68\n\t"
      "v_mov_b32 v23, v22\n\t"
      "s_nop 1\n\t"
      "v_permlane16_swap_b32 v22, v23\n\t"
      "s_nop 0\n\t"
      "v_max_f32 v22, v22, v23\n\t"
      "v_mov_b32 v23, v22\n\t"
      "s_nop 1\n\t"
      "v_permlane32_swap_b32 v22, v23\n\t"
      "s_nop 0\n\t"
      "v_max_f32 v22, v22, v23\n\t"
      "v_sub_f32 v23, v20, v22\n\t"
      "v_mul_f32 v24, %[nc2], v22\n\t"
      "v_mul_f32 v23, %[c2], v23\n\t"
      "v_mov_b32 v20, v22\n\t"
      "v_exp_f32 v23, v23\n\t"
      "v_fma_f32 v26, v26, %[c2], v24\n\t"
      "v_fma_f32 v27, v27, %[c2], v24\n\t"
      "v_fma_f32 v28, v28, %[c2], v24\n\t"
      "v_fma_f32 v29, v29, %[c2], v24\n\t"
      "v_fma_f32 v30, v30, %[c2], v24\n\t"
      "v_fma_f32 v31, v31, %[c2], v24\n\t"
      "v_fma_f32 v32, v32, %[c2], v24\n\t"
      "v_fma_f32 v33, v33, %[c2], v24\n\t"
      "v_fma_f32 v40, v40, %[c2], v24\n\t"
      "v_fma_f32 v44, v44, %[c2], v24\n\t"
      "v_fma_f32 v48, v48, %[c2], v24\n\t"
      "v_fma_f32 v52, v52, %[c2], v24\n\t"
      "v_fma_f32 v56, v56, %[c2], v24\n\t"
      "v_fma_f32 v60, v60, %[c2], v24\n\t"
      "v_fma_f32 v64, v64, %[c2], v24\n\t"
      "v_fma_f32 v68, v68, %[c2], v24\n\t"
      "v_exp_f32 v26, v26\n\t"
      "v_mul_f32 v12, v23, v12\n\t"
      "v_exp_f32 v27, v27\n\t"
      "v_mul_f32 v13, v23, v13\n\t"
      "v_exp_f32 v28, v28\n\t"
      "v_mul_f32 v14, v23, v14\n\t"
      "v_exp_f32 v29, v29\n\t"
      "v_mul_f32 v15, v23, v15\n\t"
      "v_exp_f32 v30, v30\n\t"
      "v_mul_f32 v16, v23, v16\n\t"
      "v_exp_f32 v31, v31\n\t"
      "v_mul_f32 v17, v23, v17\n\t"
      "v_exp_f32 v32, v32\n\t"
      "v_mul_f32 v18, v23, v18\n\t"
      "v_exp_f32 v33, v33\n\t"
      "v_mul_f32 v19, v23, v19\n\t"
      "v_exp_f32 v40, v40\n\t"
      "v_exp_f32 v44, v44\n\t"
      "v_exp_f32 v48, v48\n\t"
      "v_exp_f32 v52, v52\n\t"
      "v_exp_f32 v56, v56\n\t"
      "v_exp_f32 v60, v60\n\t"
      "v_exp_f32 v64, v64\n\t"
      "v_exp_f32 v68, v68\n\t"
      "v_mul_f32 v21, v21, v23\n\t"
      "v_add_f32 v22, v26, v27\n\t"
      "v_add_f32 v23, v28, v29\n\t"
      "v_add_f32 v22, v22, v30\n\t"
      "v_add_f32 v23, v23, v31\n\t"
      "v_add_f32 v22, v22, v32\n\t"
      "v_add_f32 v23, v23, v33\n\t"
      "v_add_f32 v22, v22, v40\n\t"
      "v_add_f32 v23, v23, v44\n\t"
      "v_add_f32 v22, v22, v48\n\t"
      "v_add_f32 v23, v23, v52\n\t"
      "v_add_f32 v22, v22, v56\n\t"
      "v_add_f32 v23, v23, v60\n\t"
      "v_add_f32 v22, v22, v64\n\t"
      "v_add_f32 v23, v23, v68\n\t"
      "v_add_f32 v21, v21, v22\n\t"
      "v_cvt_pk_bf16_f32 v34, v26, v27\n\t"
      "v_cvt_pk_bf16_f32 v35, v28, v29\n\t"
      "v_cvt_pk_bf16_f32 v36, v30, v31\n\t"
      "v_cvt_pk_bf16_f32 v37, v32, v33\n\t"
      "v_add_f32 v21, v21, v23\n\t"
      "v_cvt_pk_bf16_f32 v38, v40, v44\n\t"
      "v_cvt_pk_bf16_f32 v39, v48, v52\n\t"
      "v_cvt_pk_bf16_f32 v24, v56, v60\n\t"
      "v_cvt_pk_bf16_f32 v25, v64, v68\n\t"
      "v_cndmask_b32 v26, 0, v34, %[m0]\n\t"
      "v_cndmask_b32 v27, 0, v35, %[m0]\n\t"
      "v_cndmask_b32 v28, 0, v36, %[m0]\n\t"
      "v_cndmask_b32 v29, 0, v37, %[m0]\n\t"
      "v_cndmask_b32 v30, 0, v34, %[m1]\n\t"
      "v_cndmask_b32 v31, 0, v35, %[m1]\n\t"
      "v_cndmask_b32 v32, 0, v36, %[m1]\n\t"
      "v_cndmask_b32 v33, 0, v37, %[m1]\n\t"
      "v_mfma_f32_16x16x32_bf16 v[12:15], v[192:195], v[26:29], v[12:15]\n\t"
      "v_mfma_f32_16x16x32_bf16 v[16:19], v[208:211], v[26:29], v[16:19]\n\t"
      "v_cndmask_b32 v26, 0, v34, %[m2]\n\t"
      "v_cndmask_b32 v27, 0, v35, %[m2]\n\t"
      "v_cndmask_b32 v28, 0, v36, %[m2]\n\t"
      "v_cndmask_b32 v29, 0, v37, %[m2]\n\t"
      "v_mfma_f32_16x16x32_bf16 v[12:15], v[196:199], v[30:33], v[12:15]\n\t"
      "v_mfma_f32_16x16x32_bf16 v[16:19], v[212:215], v[30:33], v[16:19]\n\t"
      "v_cndmask_b32 v30, 0, v34, %[m3]\n\t"
      "v_cndmask_b32 v31, 0, v35, %[m3]\n\t"
      "v_cndmask_b32 v32, 0, v36, %[m3]\n\t"
      "v_cndmask_b32 v33, 0, v37, %[m3]\n\t"
      "v_mfma_f32_16x16x32_bf16 v[12:15], v[200:203], v[26:29], v[12:15]\n\t"
      "v_mfma_f32_16x16x32_bf16 v[16:19], v[216:219], v[26:29], v[16:19]\n\t"
      "v_mfma_f32_16x16x32_bf16 v[12:15], v[204:207], v[30:33], v[12:15]\n\t"
      "v_mfma_f32_16x16x32_bf16 v[16:19], v[220:223], v[30:33], v[16:19]\n\t"
      "v_cndmask_b32 v26, 0, v38, %[m0]\n\t"
      "v_cndmask_b32 v27, 0, v39, %[m0]\n\t"
      "v_cndmask_b32 v28, 0, v24, %[m0]\n\t"
      "v_cndmask_b32 v29, 0, v25, %[m0]\n\t"
      "v_cndmask_b32 v30, 0, v38, %[m1]\n\t"
      "v_cndmask_b32 v31, 0, v39, %[m1]\n\t"
      "v_cndmask_b32 v32, 0, v24, %[m1]\n\t"
      "v_cndmask_b32 v33, 0, v25, %[m1]\n\t"
      "v_mfma_f32_16x16x32_bf16 v[12:15], v[128:131], v[26:29], v[12:15]\n\t"
      "v_mfma_f32_16x16x32_bf16 v[16:19], v[144:147], v[26:29], v[16:19]\n\t"
      "v_cndmask_b32 v26, 0, v38, %[m2]\n\t"
      "v_cndmask_b32 v27, 0, v39, %[m2]\n\t"
      "v_cndmask_b32 v28, 0, v24, %[m2]\n\t"
      "v_cndmask_b32 v29, 0, v25, %[m2]\n\t"
      "v_mfma_f32_16x16x32_bf16 v[12:15], v[132:135], v[30:33], v[12:15]\n\t"
      "v_mfma_f32_16x16x32_bf16 v[16:19], v[148:151], v[30:33], v[16:19]\n\t"
      "v_cndmask_b32 v30, 0, v38, %[m3]\n\t"
      "v_cndmask_b32 v31, 0, v39, %[m3]\n\t"
      "v_cndmask_b32 v32, 0, v24, %[m3]\n\t"
      "v_cndmask_b32 v33, 0, v25, %[m3]\n\t"
      "v_mfma_f32_16x16x32_bf16 v[12:15], v[136:139], v[26:29], v[12:15]\n\t"
      "v_mfma_f32_16x16x32_bf16 v[16:19], v[152:155], v[26:29], v[16:19]\n\t"
      "v_mfma_f32_16x16x32_bf16 v[12:15], v[140:143], v[30:33], v[12:15]\n\t"
      "v_mfma_f32_16x16x32_bf16 v[16:19], v[156:159], v[30:33], v[16:19]\n\t"
      "1:\n\t"
      "s_memrealtime s[96:97]\n\t"
      "s_waitcnt lgkmcnt(0)\n\t"
      "s_sub_u32 %[so0], s96, %[due]\n\t"
      "s_cmp_lt_i32 %[so0], 0\n\t"
      "s_cbranch_scc0 2f\n\t"
      "s_sleep 1\n\t"
      "s_branch 1b\n\t"
      "2:\n\t"
      "s_add_u32 %[due], %[due], %[dlh]\n\t"
      "s_add_u32 %[so0], %[rot], 98304\n\t"
      "s_and_b32 %[so0], %[so0], 0x1ffff\n\t"
      "s_add_u32 %[so1], %[so0], 4096\n\t"
      "buffer_load_dwordx4 a[64:67], %[koff], %[rk], %[so0] offen offset:0 nt\n\t"
      "buffer_load_dwordx4 a[68:71], %[koff], %[rk], %[so0] offen offset:1024 nt\n\t"
      "buffer_load_dwordx4 a[72:75], %[koff], %[rk], %[so0] offen offset:2048 nt\n\t"
      "buffer_load_dwordx4 a[76:79], %[koff], %[rk], %[so0] offen offset:3072 nt\n\t"
      "buffer_load_dwordx4 a[80:83], %[koff], %[rk], %[so1] offen offset:0 nt\n\t"
      "buffer_load_dwordx4 a[84:87], %[koff], %[rk], %[so1] offen offset:1024 nt\n\t"
      "buffer_load_dwordx4 a[88:91], %[koff], %[rk], %[so1] offen offset:2048 nt\n\t"
      "buffer_load_dwordx4 a[92:95], %[koff], %[rk], %[so1] offen offset:3072 nt\n\t"
      "s_add_u32 %[so0], %[rot], 106496\n\t"
      "s_and_b32 %[so0], %[so0], 0x1ffff\n\t"
      "s_add_u32 %[so1], %[so0], 4096\n\t"
      "buffer_load_dwordx4 a[96:99], %[koff], %[rk], %[so0] offen offset:0 nt\n\t"
      "buffer_load_dwordx4 a[100:103], %[koff], %[rk], %[so0] offen offset:1024 nt\n\t"
      "buffer_load_dwordx4 a[104:107], %[koff], %[rk], %[so0] offen offset:2048 nt\n\t"
      "buffer_load_dwordx4 a[108:111], %[koff], %[rk], %[so0] offen offset:3072 nt\n\t"
      "buffer_load_dwordx4 a[112:115], %[koff], %[rk], %[so1] offen offset:0 nt\n\t"
      "buffer_load_dwordx4 a[116:119], %[koff], %[rk], %[so1] offen offset:1024 nt\n\t"
      "buffer_load_dwordx4 a[120:123], %[koff], %[rk], %[so1] offen offset:2048 nt\n\t"
      "buffer_load_dwordx4 a[124:127], %[koff], %[rk], %[so1] offen offset:3072 nt\n\t"
      "s_add_u32 %[so0], %[rot], 98304\n\t"
      "s_and_b32 %[so0], %[so0], 0x1ffff\n\t"
      "s_add_u32 %[so1], %[so0], 4096\n\t"
      "buffer_load_dwordx4 v[224:227], %[voff], %[rv], %[so0] offen offset:0 nt\n\t"
      "buffer_load_dwordx4 v[228:231], %[voff], %[rv], %[so0] offen offset:1024 nt\n\t"
      "buffer_load_dwordx4 v[232:235], %[voff], %[rv], %[so0] offen offset:2048 nt\n\t"
      "buffer_load_dwordx4 v[236:239], %[voff], %[rv], %[so0] offen offset:3072 nt\n\t"
      "buffer_load_dwordx4 v[240:243], %[voff], %[rv], %[so1] offen offset:0 nt\n\t"
      "buffer_load_dwordx4 v[244:247], %[voff], %[rv], %[so1] offen offset:1024 nt\n\t"
      "buffer_load_dwordx4 v[248:251], %[voff], %[rv], %[so1] offen offset:2048 nt\n\t"
      "buffer_load_dwordx4 v[252:255], %[voff], %[rv], %[so1] offen offset:3072 nt\n\t"
      "s_add_u32 %[so0], %[rot], 106496\n\t"
      "s_and_b32 %[so0], %[so0], 0x1ffff\n\t"
      "s_add_u32 %[so1], %[so0], 4096\n\t"
      "buffer_load_dwordx4 v[160:163], %[voff], %[rv], %[so0] offen offset:0 nt\n\t"
      "buffer_load_dwordx4 v[164:167], %[voff], %[rv], %[so0] offen offset:1024 nt\n\t"
      "buffer_load_dwordx4 v[168:171], %[voff], %[rv], %[so0] offen offset:2048 nt\n\t"
      "buffer_load_dwordx4 v[172:175], %[voff], %[rv], %[so0] offen offset:3072 nt\n\t"
      "buffer_load_dwordx4 v[176:179], %[voff], %[rv], %[so1] offen offset:0 nt\n\t"
      "buffer_load_dwordx4 v[180:183], %[voff], %[rv], %[so1] offen offset:1024 nt\n\t"
      "buffer_load_dwordx4 v[184:187], %[voff], %[rv], %[so1] offen offset:2048 nt\n\t"
      "buffer_load_dwordx4 v[188:191], %[voff], %[rv], %[so1] offen offset:3072 nt\n\t"
      "v_mfma_f32_16x16x32_bf16 v[40:43], a[0:3], v[8:11], 0\n\t"
      "v_mfma_f32_16x16x32_bf16 v[44:47], a[4:7], v[8:11], 0\n\t"
      "v_mfma_f32_16x16x32_bf16 v[48:51], a[8:11], v[8:11], 0\n\t"
      "v_mfma_f32_16x16x32_bf16 v[52:55], a[12:15], v[8:11], 0\n\t"
      "v_mfma_f32_16x16x32_bf16 v[56:59], a[16:19], v[8:11], 0\n\t"
      "v_mfma_f32_16x16x32_bf16 v[60:63], a[20:23], v[8:11], 0\n\t"
      "v_mfma_f32_16x16x32_bf16 v[64:67], a[24:27], v[8:11], 0\n\t"
      "v_mfma_f32_16x16x32_bf16 v[68:71], a[28:31], v[8:11], 0\n\t"
      "s_nop 1\n\t"
      "v_mov_b32_dpp v26, v40 quad_perm:[0,0,0,0] row_mask:0xf bank_mask:0xf\n\t"
      "v_mov_b32_dpp v27, v44 quad_perm:[0,0,0,0] row_mask:0xf bank_mask:0xf\n\t"
      "v_mov_b32_dpp v28, v48 quad_perm:[0,0,0,0] row_mask:0xf bank_mask:0xf\n\t"
      "v_mov_b32_dpp v29, v52 quad_perm:[0,0,0,0] row_mask:0xf bank_mask:0xf\n\t"
      "v_add_f32_dpp v26, v41, v26 quad_perm:[1,1,1,1] row_mask:0xf bank_mask:0xf\n\t"
      "v_add_f32_dpp v27, v45, v27 quad_perm:[1,1,1,1] row_mask:0xf bank_mask:0xf\n\t"
      "v_add_f32_dpp v28, v49, v28 quad_perm:[1,1,1,1] row_mask:0xf bank_mask:0xf\n\t"
      "v_add_f32_dpp v29, v53, v29 quad_perm:[1,1,1,1] row_mask:0xf bank_mask:0xf\n\t"
      "v_add_f32_dpp v26, v42, v26 quad_perm:[2,2,2,2] row_mask:0xf bank_mask:0xf\n\t"
      "v_add_f32_dpp v27, v46, v27 quad_perm:[2,2,2,2] row_mask:0xf bank_mask:0xf\n\t"
      "v_add_f32_dpp v28, v50, v28 quad_perm:[2,2,2,2] row_mask:0xf bank_mask:0xf\n\t"
      "v_add_f32_dpp v29, v54, v29 quad_perm:[2,2,2,2] row_mask:0xf bank_mask:0xf\n\t"
      "v_add_f32_dpp v26, v43, v26 quad_perm:[3,3,3,3] row_mask:0xf bank_mask:0xf\n\t"
      "v_add_f32_dpp v27, v47, v27 quad_perm:[3,3,3,3] row_mask:0xf bank_mask:0xf\n\t"
      "v_add_f32_dpp v28, v51, v28 quad_perm:[3,3,3,3] row_mask:0xf bank_mask:0xf\n\t"
      "v_add_f32_dpp v29, v55, v29 quad_perm:[3,3,3,3] row_mask:0xf bank_mask:0xf\n\t"
      "v_mov_b32_dpp v30, v56 quad_perm:[0,0,0,0] row_mask:0xf bank_mask:0xf\n\t"
      "v_mov_b32_dpp v31, v60 quad_perm:[0,0,0,0] row_mask:0xf bank_mask:0xf\n\t"
      "v_mov_b32_dpp v32, v64 quad_perm:[0,0,0,0] row_mask:0xf bank_mask:0xf\n\t"
      "v_mov_b32_dpp v33, v68 quad_perm:[0,0,0,0] row_mask:0xf bank_mask:0xf\n\t"
      "v_add_f32_dpp v30, v57, v30 quad_perm:[1,1,1,1] row_mask:0xf bank_mask:0xf\n\t"
      "v_add_f32_dpp v31, v61, v31 quad_perm:[1,1,1,1] row_mask:0xf bank_mask:0xf\n\t"
      "v_add_f32_dpp v32, v65, v32 quad_perm:[1,1,1,1] row_mask:0xf bank_mask:0xf\n\t"
      "v_add_f32_dpp v33, v69, v33 quad_perm:[1,1,1,1] row_mask:0xf bank_mask:0xf\n\t"
      "v_add_f32_dpp v30, v58, v30 quad_perm:[2,2,2,2] row_mask:0xf bank_mask:0xf\n\t"
      "v_add_f32_dpp v31, v62, v31 quad_perm:[2,2,2,2] row_mask:0xf bank_mask:0xf\n\t"
      "v_add_f32_dpp v32, v66, v32 quad_perm:[2,2,2,2] row_mask:0xf bank_mask:0xf\n\t"
      "v_add_f32_dpp v33, v70, v33 quad_perm:[2,2,2,2] row_mask:0xf bank_mask:0xf\n\t"
      "v_add_f32_dpp v30, v59, v30 quad_perm:[3,3,3,3] row_mask:0xf bank_mask:0xf\n\t"
      "v_add_f32_dpp v31, v63, v31 quad_perm:[3,3,3,3] row_mask:0xf bank_mask:0xf\n\t"
      "v_add_f32_dpp v32, v67, v32 quad_perm:[3,3,3,3] row_mask:0xf bank_mask:0xf\n\t"
      "v_add_f32_dpp v33, v71, v33 quad_perm:[3,3,3,3] row_mask:0xf bank_mask:0xf\n\t"
      "v_mfma_f32_16x16x32_bf16 v[40:43], a[32:35], v[8:11], 0\n\t"
      "v_mfma_f32_16x16x32_bf16 v[44:47], a[36:39], v[8:11], 0\n\t"
      "v_mfma_f32_16x16x32_bf16 v[48:51], a[40:43], v[8:11], 0\n\t"
      "v_mfma_f32_16x16x32_bf16 v[52:55], a[44:47], v[8:11], 0\n\t"
      "v_mfma_f32_16x16x32_bf16 v[56:59], a[48:51], v[8:11], 0\n\t"
      "v_mfma_f32_16x16x32_bf16 v[60:63], a[52:55], v[8:11], 0\n\t"
      "v_mfma_f32_16x16x32_bf16 v[64:67], a[56:59], v[8:11], 0\n\t"
      "v_mfma_f32_16x16x32_bf16 v[68:71], a[60:63], v[8:11], 0\n\t"
      "s_nop 1\n\t"
      "v_mov_b32_dpp v40, v40 quad_perm:[0,0,0,0] row_mask:0xf bank_mask:0xf\n\t"
      "v_mov_b32_dpp v44, v44 quad_perm:[0,0,0,0] row_mask:0xf bank_mask:0xf\n\t"
      "v_mov_b32_dpp v48, v48 quad_perm:[0,0,0,0] row_mask:0xf bank_mask:0xf\n\t"
      "v_mov_b32_dpp v52, v52 quad_perm:[0,0,0,0] row_mask:0xf bank_mask:0xf\n\t"
      "v_add_f32_dpp v40, v41, v40 quad_perm:[1,1,1,1] row_mask:0xf bank_mask:0xf\n\t"
      "v_add_f32_dpp v44, v45, v44 quad_perm:[1,1,1,1] row_mask:0xf bank_mask:0xf\n\t"
      "v_add_f32_dpp v48, v49, v48 quad_perm:[1,1,1,1] row_mask:0xf bank_mask:0xf\n\t"
      "v_add_f32_dpp v52, v53, v52 quad_perm:[1,1,1,1] row_mask:0xf bank_mask:0xf\n\t"
      "v_add_f32_dpp v40, v42, v40 quad_perm:[2,2,2,2] row_mask:0xf bank_mask:0xf\n\t"
      "v_add_f32_dpp v44, v46, v44 quad_perm:[2,2,2,2] row_mask:0xf bank_mask:0xf\n\t"
      "v_add_f32_dpp v48, v50, v48 quad_perm:[2,2,2,2] row_mask:0xf bank_mask:0xf\n\t"
      "v_add_f32_dpp v52, v54, v52 quad_perm:[2,2,2,2] row_mask:0xf bank_mask:0xf\n\t"
      "v_add_f32_dpp v40, v43, v40 quad_perm:[3,3,3,3] row_mask:0xf bank_mask:0xf\n\t"
      "v_add_f32_dpp v44, v47, v44 quad_perm:[3,3,3,3] row_mask:0xf bank_mask:0xf\n\t"
      "v_add_f32_dpp v48, v51, v48 quad_perm:[3,3,3,3] row_mask:0xf bank_mask:0xf\n\t"
      "v_add_f32_dpp v52, v55, v52 quad_perm:[3,3,3,3] row_mask:0xf bank_mask:0xf\n\t"
      "v_mov_b32_dpp v56, v56 quad_perm:[0,0,0,0] row_mask:0xf bank_mask:0xf\n\t"
      "v_mov_b32_dpp v60, v60 quad_perm:[0,0,0,0] row_mask:0xf bank_mask:0xf\n\t"
      "v_mov_b32_dpp v64, v64 quad_perm:[0,0,0,0] row_mask:0xf bank_mask:0xf\n\t"
      "v_mov_b32_dpp v68, v68 quad_perm:[0,0,0,0] row_mask:0xf bank_mask:0xf\n\t"
      "v_add_f32_dpp v56, v57, v56 quad_perm:[1,1,1,1] row_mask:0xf bank_mask:0xf\n\t"
      "v_add_f32_dpp v60, v61, v60 quad_perm:[1,1,1,1] row_mask:0xf bank_mask:0xf\n\t"
      "v_add_f32_dpp v64, v65, v64 quad_perm:[1,1,1,1] row_mask:0xf bank_mask:0xf\n\t"
      "v_add_f32_dpp v68, v69, v68 quad_perm:[1,1,1,1] row_mask:0xf bank_mask:0xf\n\t"
      "v_add_f32_dpp v56, v58, v56 quad_perm:[2,2,2,2] row_mask:0xf bank_mask:0xf\n\t"
      "v_add_f32_dpp v60, v62, v60 quad_perm:[2,2,2,2] row_mask:0xf bank_mask:0xf\n\t"
      "v_add_f32_dpp v64, v66, v64 quad_perm:[2,2,2,2] row_mask:0xf bank_mask:0xf\n\t"
      "v_add_f32_dpp v68, v70, v68 quad_perm:[2,2,2,2] row_mask:0xf bank_mask:0xf\n\t"
      "v_add_f32_dpp v56, v59, v56 quad_perm:[3,3,3,3] row_mask:0xf bank_mask:0xf\n\t"
      "v_add_f32_dpp v60, v63, v60 quad_perm:[3,3,3,3] row_mask:0xf bank_mask:0xf\n\t"
      "v_add_f32_dpp v64, v67, v64 quad_perm:[3,3,3,3] row_mask:0xf bank_mask:0xf\n\t"
      "v_add_f32_dpp v68, v71, v68 quad_perm:[3,3,3,3] row_mask:0xf bank_mask:0xf\n\t"
      "v_max3_f32 v22, v26, v27, v20\n\t"
      "v_max3_f32 v23, v28, v29, v30\n\t"
      "v_max3_f32 v22, v22, v31, v32\n\t"
      "v_max3_f32 v23, v23, v33, v40\n\t"
      "v_max3_f32 v22, v22, v44, v48\n\t"
      "v_max3_f32 v23, v23, v52, v56\n\t"
      "v_max3_f32 v22, v22, v60, v64\n\t"
      "v_max3_f32 v22, v22, v23, v68\n\t"
      "v_mov_b32 v23, v22\n\t"
      "s_nop 1\n\t"
      "v_permlane16_swap_b32 v22, v23\n\t"
      "s_nop 0\n\t"
      "v_max_f32 v22, v22, v23\n\t"
      "v_mov_b32 v23, v22\n\t"
      "s_nop 1\n\t"
      "v_permlane32_swap_b32 v22, v23\n\t"
      "s_nop 0\n\t"
      "v_max_f32 v22, v22, v23\n\t"
      "v_sub_f32 v23, v20, v22\n\t"
      "v_mul_f32 v24, %[nc2], v22\n\t"
      "v_mul_f32 v23, %[c2], v23\n\t"
      "v_mov_b32 v20, v22\n\t"
      "v_exp_f32 v23, v23\n\t"
      "v_fma_f32 v26, v26, %[c2], v24\n\t"
      "v_fma_f32 v27, v27, %[c2], v24\n\t"
      "v_fma_f32 v28, v28, %[c2], v24\n\t"
      "v_fma_f32 v29, v29, %[c2], v24\n\t"
      "v_fma_f32 v30, v30, %[c2], v24\n\t"
      "v_fma_f32 v31, v31, %[c2], v24\n\t"
      "v_fma_f32 v32, v32, %[c2], v24\n\t"
      "v_fma_f32 v33, v33, %[c2], v24\n\t"
      "v_fma_f32 v40, v40, %[c2], v24\n\t"
      "v_fma_f32 v44, v44, %[c2], v24\n\t"
      "v_fma_f32 v48, v48, %[c2], v24\n\t"
      "v_fma_f32 v52, v52, %[c2], v24\n\t"
      "v_fma_f32 v56, v56, %[c2], v24\n\t"
      "v_fma_f32 v60, v60, %[c2], v24\n\t"
      "v_fma_f32 v64, v64, %[c2], v24\n\t"
      "v_fma_f32 v68, v68, %[c2], v24\n\t"
      "v_exp_f32 v26, v26\n\t"
      "v_mul_f32 v12, v23, v12\n\t"
      "v_exp_f32 v27, v27\n\t"
      "v_mul_f32 v13, v23, v13\n\t"
      "v_exp_f32 v28, v28\n\t"
      "v_mul_f32 v14, v23, v14\n\t"
      "v_exp_f32 v29, v29\n\t"
      "v_mul_f32 v15, v23, v15\n\t"
      "v_exp_f32 v30, v30\n\t"
      "v_mul_f32 v16, v23, v16\n\t"
      "v_exp_f32 v31, v31\n\t"
      "v_mul_f32 v17, v23, v17\n\t"
      "v_exp_f32 v32, v32\n\t"
      "v_mul_f32 v18, v23, v18\n\t"
      "v_exp_f32 v33, v33\n\t"
      "v_mul_f32 v19, v23, v19\n\t"
      "v_exp_f32 v40, v40\n\t"
      "v_exp_f32 v44, v44\n\t"
      "v_exp_f32 v48, v48\n\t"
      "v_exp_f32 v52, v52\n\t"
      "v_exp_f32 v56, v56\n\t"
      "v_exp_f32 v60, v60\n\t"
      "v_exp_f32 v64, v64\n\t"
      "v_exp_f32 v68, v68\n\t"
      "v_mul_f32 v21, v21, v23\n\t"
      "v_add_f32 v22, v26, v27\n\t"
      "v_add_f32 v23, v28, v29\n\t"
      "v_add_f32 v22, v22, v30\n\t"
      "v_add_f32 v23, v23, v31\n\t"
      "v_add_f32 v22, v22, v32\n\t"
      "v_add_f32 v23, v23, v33\n\t"
      "v_add_f32 v22, v22, v40\n\t"
      "v_add_f32 v23, v23, v44\n\t"
      "v_add_f32 v22, v22, v48\n\t"
      "v_add_f32 v23, v23, v52\n\t"
      "v_add_f32 v22, v22, v56\n\t"
      "v_add_f32 v23, v23, v60\n\t"
      "v_add_f32 v22, v22, v64\n\t"
      "v_add_f32 v23, v23, v68\n\t"
      "v_add_f32 v21, v21, v22\n\t"
      "v_cvt_pk_bf16_f32 v34, v26, v27\n\t"
      "v_cvt_pk_bf16_f32 v35, v28, v29\n\t"
      "v_cvt_pk_bf16_f32 v36, v30, v31\n\t"
      "v_cvt_pk_bf16_f32 v37, v32, v33\n\t"
      "v_add_f32 v21, v21, v23\n\t"
      "v_cvt_pk_bf16_f32 v38, v40, v44\n\t"
      "v_cvt_pk_bf16_f32 v39, v48, v52\n\t"
      "v_cvt_pk_bf16_f32 v24, v56, v60\n\t"
      "v_cvt_pk_bf16_f32 v25, v64, v68\n\t"
      "ds_read_b128 v[40:43], %[lds] offset:16384\n\t"
      "ds_read_b128 v[44:47], %[lds] offset:17408\n\t"
      "ds_read_b128 v[48:51], %[lds] offset:18432\n\t"
      "ds_read_b128 v[52:55], %[lds] offset:19456\n\t"
      "ds_read_b128 v[56:59], %[lds] offset:20480\n\t"
      "ds_read_b128 v[60:63], %[lds] offset:21504\n\t"
      "ds_read_b128 v[64:67], %[lds] offset:22528\n\t"
      "ds_read_b128 v[68:71], %[lds] offset:23552\n\t"
      "s_waitcnt lgkmcnt(0)\n\t"
      "v_cndmask_b32 v26, 0, v34, %[m0]\n\t"
      "v_cndmask_b32 v27, 0, v35, %[m0]\n\t"
      "v_cndmask_b32 v28, 0, v36, %[m0]\n\t"
      "v_cndmask_b32 v29, 0, v37, %[m0]\n\t"
      "v_cndmask_b32 v30, 0, v34, %[m1]\n\t"
      "v_cndmask_b32 v31, 0, v35, %[m1]\n\t"
      "v_cndmask_b32 v32, 0, v36, %[m1]\n\t"
      "v_cndmask_b32 v33, 0, v37, %[m1]\n\t"
      "v_mfma_f32_16x16x32_bf16 v[12:15], v[40:43], v[26:29], v[12:15]\n\t"
      "v_mfma_f32_16x16x32_bf16 v[16:19], v[56:59], v[26:29], v[16:19]\n\t"
      "v_cndmask_b32 v26, 0, v34, %[m2]\n\t"
      "v_cndmask_b32 v27, 0, v35, %[m2]\n\t"
      "v_cndmask_b32 v28, 0, v36, %[m2]\n\t"
      "v_cndmask_b32 v29, 0, v37, %[m2]\n\t"
      "v_mfma_f32_16x16x32_bf16 v[12:15], v[44:47], v[30:33], v[12:15]\n\t"
      "v_mfma_f32_16x16x32_bf16 v[16:19], v[60:63], v[30:33], v[16:19]\n\t"
      "v_cndmask_b32 v30, 0, v34, %[m3]\n\t"
      "v_cndmask_b32 v31, 0, v35, %[m3]\n\t"
      "v_cndmask_b32 v32, 0, v36, %[m3]\n\t"
      "v_cndmask_b32 v33, 0, v37, %[m3]\n\t"
      "v_mfma_f32_16x16x32_bf16 v[12:15], v[48:51], v[26:29], v[12:15]\n\t"
      "v_mfma_f32_16x16x32_bf16 v[16:19], v[64:67], v[26:29], v[16:19]\n\t"
      "v_mfma_f32_16x16x32_bf16 v[12:15], v[52:55], v[30:33], v[12:15]\n\t"
      "v_mfma_f32_16x16x32_bf16 v[16:19], v[68:71], v[30:33], v[16:19]\n\t"
      "ds_read_b128 v[40:43], %[lds] offset:24576\n\t"
      "ds_read_b128 v[44:47], %[lds] offset:25600\n\t"
      "ds_read_b128 v[48:51], %[lds] offset:26624\n\t"
      "ds_read_b128 v[52:55], %[lds] offset:27648\n\t"
      "ds_read_b128 v[56:59], %[lds] offset:28672\n\t"
      "ds_read_b128 v[60:63], %[lds] offset:29696\n\t"
      "ds_read_b128 v[64:67], %[lds] offset:30720\n\t"
      "ds_read_b128 v[68:71], %[lds] offset:31744\n\t"
      "s_waitcnt lgkmcnt(0)\n\t"
      "v_cndmask_b32 v26, 0, v38, %[m0]\n\t"
      "v_cndmask_b32 v27, 0, v39, %[m0]\n\t"
      "v_cndmask_b32 v28, 0, v24, %[m0]\n\t"
      "v_cndmask_b32 v29, 0, v25, %[m0]\n\t"
      "v_cndmask_b32 v30, 0, v38, %[m1]\n\t"
      "v_cndmask_b32 v31, 0, v39, %[m1]\n\t"
      "v_cndmask_b32 v32, 0, v24, %[m1]\n\t"
      "v_cndmask_b32 v33, 0, v25, %[m1]\n\t"
      "v_mfma_f32_16x16x32_bf16 v[12:15], v[40:43], v[26:29], v[12:15]\n\t"
      "v_mfma_f32_16x16x32_bf16 v[16:19], v[56:59], v[26:29], v[16:19]\n\t"
      "v_cndmask_b32 v26, 0, v38, %[m2]\n\t"
      "v_cndmask_b32 v27, 0, v39, %[m2]\n\t"
      "v_cndmask_b32 v28, 0, v24, %[m2]\n\t"
      "v_cndmask_b32 v29, 0, v25, %[m2]\n\t"
      "v_mfma_f32_16x16x32_bf16 v[12:15], v[44:47], v[30:33], v[12:15]\n\t"
      "v_mfma_f32_16x16x32_bf16 v[16:19], v[60:63], v[30:33], v[16:19]\n\t"
      "v_cndmask_b32 v30, 0, v38, %[m3]\n\t"
      "v_cndmask_b32 v31, 0, v39, %[m3]\n\t"
      "v_cndmask_b32 v32, 0, v24, %[m3]\n\t"
      "v_cndmask_b32 v33, 0, v25, %[m3]\n\t"
      "v_mfma_f32_16x16x32_bf16 v[12:15], v[48:51], v[26:29], v[12:15]\n\t"
      "v_mfma_f32_16x16x32_bf16 v[16:19], v[64:67], v[26:29], v[16:19]\n\t"
      "v_mfma_f32_16x16x32_bf16 v[12:15], v[52:55], v[30:33], v[12:15]\n\t"
      "v_mfma_f32_16x16x32_bf16 v[16:19], v[68:71], v[30:33], v[16:19]\n\t"
      "1:\n\t"
      "s_memrealtime s[96:97]\n\t"
      "s_waitcnt lgkmcnt(0)\n\t"
      "s_sub_u32 %[so0], s96, %[due]\n\t"
      "s_cmp_lt_i32 %[so0], 0\n\t"
      "s_cbranch_scc0 2f\n\t"
      "s_sleep 1\n\t"
      "s_branch 1b\n\t"
      "2:\n\t"
      "s_add_u32 %[due], %[due], %[dl]\n\t"
      "s_add_u32 %[so0], %[rot], 0\n\t"
      "s_and_b32 %[so0], %[so0], 0x1ffff\n\t"
      "s_add_u32 %[so1], %[so0], 4096\n\t"
      "buffer_load_dwordx4 v[192:195], %[voff], %[rv], %[so0] offen offset:0 nt\n\t"
      "buffer_load_dwordx4 v[196:199], %[voff], %[rv], %[so0] offen offset:1024 nt\n\t"
      "buffer_load_dwordx4 v[200:203], %[voff], %[rv], %[so0] offen offset:2048 nt\n\t"
      "buffer_load_dwordx4 v[204:207], %[voff], %[rv], %[so0] offen offset:3072 nt\n\t"
      "buffer_load_dwordx4 v[208:211], %[voff], %[rv], %[so1] offen offset:0 nt\n\t"
      "buffer_load_dwordx4 v[212:215], %[voff], %[rv], %[so1] offen offset:1024 nt\n\t"
      "buffer_load_dwordx4 v[216:219], %[voff], %[rv], %[so1] offen offset:2048 nt\n\t"
      "buffer_load_dwordx4 v[220:223], %[voff], %[rv], %[so1] offen offset:3072 nt\n\t"
      "s_add_u32 %[so0], %[rot], 8192\n\t"
      "s_and_b32 %[so0], %[so0], 0x1ffff\n\t"
      "s_add_u32 %[so1], %[so0], 4096\n\t"
      "buffer_load_dwordx4 v[128:131], %[voff], %[rv], %[so0] offen offset:0 nt\n\t"
      "buffer_load_dwordx4 v[132:135], %[voff], %[rv], %[so0] offen offset:1024 nt\n\t"
      "buffer_load_dwordx4 v[136:139], %[voff], %[rv], %[so0] offen offset:2048 nt\n\t"
      "buffer_load_dwordx4 v[140:143], %[voff], %[rv], %[so0] offen offset:3072 nt\n\t"
      "buffer_load_dwordx4 v[144:147], %[voff], %[rv], %[so1] offen offset:0 nt\n\t"
      "buffer_load_dwordx4 v[148:151], %[voff], %[rv], %[so1] offen offset:1024 nt\n\t"
      "buffer_load_dwordx4 v[152:155], %[voff], %[rv], %[so1] offen offset:2048 nt\n\t"
      "buffer_load_dwordx4 v[156:159], %[voff], %[rv], %[so1] offen offset:3072 nt\n\t"
      "v_mfma_f32_16x16x32_bf16 v[40:43], a[192:195], v[8:11], 0\n\t"
      "v_mfma_f32_16x16x32_bf16 v[44:47], a[196:199], v[8:11], 0\n\t"
      "v_mfma_f32_16x16x32_bf16 v[48:51], a[200:203], v[8:11], 0\n\t"
      "v_mfma_f32_16x16x32_bf16 v[52:55], a[204:207], v[8:11], 0\n\t"
      "v_mfma_f32_16x16x32_bf16 v[56:59], a[208:211], v[8:11], 0\n\t"
      "v_mfma_f32_16x16x32_bf16 v[60:63], a[212:215], v[8:11], 0\n\t"
      "v_mfma_f32_16x16x32_bf16 v[64:67], a[216:219], v[8:11], 0\n\t"
      "v_mfma_f32_16x16x32_bf16 v[68:71], a[220:223], v[8:11], 0\n\t"
      "s_nop 1\n\t"
      "v_mov_b32_dpp v26, v40 quad_perm:[0,0,0,0] row_mask:0xf bank_mask:0xf\n\t"
      "v_mov_b32_dpp v27, v44 quad_perm:[0,0,0,0] row_mask:0xf bank_mask:0xf\n\t"
      "v_mov_b32_dpp v28, v48 quad_perm:[0,0,0,0] row_mask:0xf bank_mask:0xf\n\t"
      "v_mov_b32_dpp v29, v52 quad_perm:[0,0,0,0] row_mask:0xf bank_mask:0xf\n\t"
      "v_add_f32_dpp v26, v41, v26 quad_perm:[1,1,1,1] row_mask:0xf bank_mask:0xf\n\t"
      "v_add_f32_dpp v27, v45, v27 quad_perm:[1,1,1,1] row_mask:0xf bank_mask:0xf\n\t"
      "v_add_f32_dpp v28, v49, v28 quad_perm:[1,1,1,1] row_mask:0xf bank_mask:0xf\n\t"
      "v_add_f32_dpp v29, v53, v29 quad_perm:[1,1,1,1] row_mask:0xf bank_mask:0xf\n\t"
      "v_add_f32_dpp v26, v42, v26 quad_perm:[2,2,2,2] row_mask:0xf bank_mask:0xf\n\t"
      "v_add_f32_dpp v27, v46, v27 quad_perm:[2,2,2,2] row_mask:0xf bank_mask:0xf\n\t"
      "v_add_f32_dpp v28, v50, v28 quad_perm:[2,2,2,2] row_mask:0xf bank_mask:0xf\n\t"
      "v_add_f32_dpp v29, v54, v29 quad_perm:[2,2,2,2] row_mask:0xf bank_mask:0xf\n\t"
      "v_add_f32_dpp v26, v43, v26 quad_perm:[3,3,3,3] row_mask:0xf bank_mask:0xf\n\t"
      "v_add_f32_dpp v27, v47, v27 quad_perm:[3,3,3,3] row_mask:0xf bank_mask:0xf\n\t"
      "v_add_f32_dpp v28, v51, v28 quad_perm:[3,3,3,3] row_mask:0xf bank_mask:0xf\n\t"
      "v_add_f32_dpp v29, v55, v29 quad_perm:[3,3,3,3] row_mask:0xf bank_mask:0xf\n\t"
      "v_mov_b32_dpp v30, v56 quad_perm:[0,0,0,0] row_mask:0xf bank_mask:0xf\n\t"
      "v_mov_b32_dpp v31, v60 quad_perm:[0,0,0,0] row_mask:0xf bank_mask:0xf\n\t"
      "v_mov_b32_dpp v32, v64 quad_perm:[0,0,0,0] row_mask:0xf bank_mask:0xf\n\t"
      "v_mov_b32_dpp v33, v68 quad_perm:[0,0,0,0] row_mask:0xf bank_mask:0xf\n\t"
      "v_add_f32_dpp v30, v57, v30 quad_perm:[1,1,1,1] row_mask:0xf bank_mask:0xf\n\t"
      "v_add_f32_dpp v31, v61, v31 quad_perm:[1,1,1,1] row_mask:0xf bank_mask:0xf\n\t"
      "v_add_f32_dpp v32, v65, v32 quad_perm:[1,1,1,1] row_mask:0xf bank_mask:0xf\n\t"
      "v_add_f32_dpp v33, v69, v33 quad_perm:[1,1,1,1] row_mask:0xf bank_mask:0xf\n\t"
      "v_add_f32_dpp v30, v58, v30 quad_perm:[2,2,2,2] row_mask:0xf bank_mask:0xf\n\t"
      "v_add_f32_dpp v31, v62, v31 quad_perm:[2,2,2,2] row_mask:0xf bank_mask:0xf\n\t"
      "v_add_f32_dpp v32, v66, v32 quad_perm:[2,2,2,2] row_mask:0xf bank_mask:0xf\n\t"
      "v_add_f32_dpp v33, v70, v33 quad_perm:[2,2,2,2] row_mask:0xf bank_mask:0xf\n\t"
      "v_add_f32_dpp v30, v59, v30 quad_perm:[3,3,3,3] row_mask:0xf bank_mask:0xf\n\t"
      "v_add_f32_dpp v31, v63, v31 quad_perm:[3,3,3,3] row_mask:0xf bank_mask:0xf\n\t"
      "v_add_f32_dpp v32, v67, v32 quad_perm:[3,3,3,3] row_mask:0xf bank_mask:0xf\n\t"
      "v_add_f32_dpp v33, v71, v33 quad_perm:[3,3,3,3] row_mask:0xf bank_mask:0xf\n\t"
      "v_mfma_f32_16x16x32_bf16 v[40:43], a[224:227], v[8:11], 0\n\t"
      "v_mfma_f32_16x16x32_bf16 v[44:47], a[228:231], v[8:11], 0\n\t"
      "v_mfma_f32_16x16x32_bf16 v[48:51], a[232:235], v[8:11], 0\n\t"
      "v_mfma_f32_16x16x32_bf16 v[52:55], a[236:239], v[8:11], 0\n\t"
      "v_mfma_f32_16x16x32_bf16 v[56:59], a[240:243], v[8:11], 0\n\t"
      "v_mfma_f32_16x16x32_bf16 v[60:63], a[244:247], v[8:11], 0\n\t"
      "v_mfma_f32_16x16x32_bf16 v[64:67], a[248:251], v[8:11], 0\n\t"
      "v_mfma_f32_16x16x32_bf16 v[68:71], a[252:255], v[8:11], 0\n\t"
      "s_nop 1\n\t"
      "v_mov_b32_dpp v40, v40 quad_perm:[0,0,0,0] row_mask:0xf bank_mask:0xf\n\t"
      "v_mov_b32_dpp v44, v44 quad_perm:[0,0,0,0] row_mask:0xf bank_mask:0xf\n\t"
      "v_mov_b32_dpp v48, v48 quad_perm:[0,0,0,0] row_mask:0xf bank_mask:0xf\n\t"
      "v_mov_b32_dpp v52, v52 quad_perm:[0,0,0,0] row_mask:0xf bank_mask:0xf\n\t"
      "v_add_f32_dpp v40, v41, v40 quad_perm:[1,1,1,1] row_mask:0xf bank_mask:0xf\n\t"
      "v_add_f32_dpp v44, v45, v44 quad_perm:[1,1,1,1] row_mask:0xf bank_mask:0xf\n\t"
      "v_add_f32_dpp v48, v49, v48 quad_perm:[1,1,1,1] row_mask:0xf bank_mask:0xf\n\t"
      "v_add_f32_dpp v52, v53, v52 quad_perm:[1,1,1,1] row_mask:0xf bank_mask:0xf\n\t"
      "v_add_f32_dpp v40, v42, v40 quad_perm:[2,2,2,2] row_mask:0xf bank_mask:0xf\n\t"
      "v_add_f32_dpp v44, v46, v44 quad_perm:[2,2,2,2] row_mask:0xf bank_mask:0xf\n\t"
      "v_add_f32_dpp v48, v50, v48 quad_perm:[2,2,2,2] row_mask:0xf bank_mask:0xf\n\t"
      "v_add_f32_dpp v52, v54, v52 quad_perm:[2,2,2,2] row_mask:0xf bank_mask:0xf\n\t"
      "v_add_f32_dpp v40, v43, v40 quad_perm:[3,3,3,3] row_mask:0xf bank_mask:0xf\n\t"
      "v_add_f32_dpp v44, v47, v44 quad_perm:[3,3,3,3] row_mask:0xf bank_mask:0xf\n\t"
      "v_add_f32_dpp v48, v51, v48 quad_perm:[3,3,3,3] row_mask:0xf bank_mask:0xf\n\t"
      "v_add_f32_dpp v52, v55, v52 quad_perm:[3,3,3,3] row_mask:0xf bank_mask:0xf\n\t"
      "v_mov_b32_dpp v56, v56 quad_perm:[0,0,0,0] row_mask:0xf bank_mask:0xf\n\t"
      "v_mov_b32_dpp v60, v60 quad_perm:[0,0,0,0] row_mask:0xf bank_mask:0xf\n\t"
      "v_mov_b32_dpp v64, v64 quad_perm:[0,0,0,0] row_mask:0xf bank_mask:0xf\n\t"
      "v_mov_b32_dpp v68, v68 quad_perm:[0,0,0,0] row_mask:0xf bank_mask:0xf\n\t"
      "v_add_f32_dpp v56, v57, v56 quad_perm:[1,1,1,1] row_mask:0xf bank_mask:0xf\n\t"
      "v_add_f32_dpp v60, v61, v60 quad_perm:[1,1,1,1] row_mask:0xf bank_mask:0xf\n\t"
      "v_add_f32_dpp v64, v65, v64 quad_perm:[1,1,1,1] row_mask:0xf bank_mask:0xf\n\t"
      "v_add_f32_dpp v68, v69, v68 quad_perm:[1,1,1,1] row_mask:0xf bank_mask:0xf\n\t"
      "v_add_f32_dpp v56, v58, v56 quad_perm:[2,2,2,2] row_mask:0xf bank_mask:0xf\n\t"
      "v_add_f32_dpp v60, v62, v60 quad_perm:[2,2,2,2] row_mask:0xf bank_mask:0xf\n\t"
      "v_add_f32_dpp v64, v66, v64 quad_perm:[2,2,2,2] row_mask:0xf bank_mask:0xf\n\t"
      "v_add_f32_dpp v68, v70, v68 quad_perm:[2,2,2,2] row_mask:0xf bank_mask:0xf\n\t"
      "v_add_f32_dpp v56, v59, v56 quad_perm:[3,3,3,3] row_mask:0xf bank_mask:0xf\n\t"
      "v_add_f32_dpp v60, v63, v60 quad_perm:[3,3,3,3] row_mask:0xf bank_mask:0xf\n\t"
      "v_add_f32_dpp v64, v67, v64 quad_perm:[3,3,3,3] row_mask:0xf bank_mask:0xf\n\t"
      "v_add_f32_dpp v68, v71, v68 quad_perm:[3,3,3,3] row_mask:0xf bank_mask:0xf\n\t"
      "v_max3_f32 v22, v26, v27, v20\n\t"
      "v_max3_f32 v23, v28, v29, v30\n\t"
      "v_max3_f32 v22, v22, v31, v32\n\t"
      "v_max3_f32 v23, v23, v33, v40\n\t"
      "v_max3_f32 v22, v22, v44, v48\n\t"
      "v_max3_f32 v23, v23, v52, v56\n\t"
      "v_max3_f32 v22, v22, v60, v64\n\t"
      "v_max3_f32 v22, v22, v23, v68\n\t"
      "v_mov_b32 v23, v22\n\t"
      "s_nop 1\n\t"
      "v_permlane16_swap_b32 v22, v23\n\t"
      "s_nop 0\n\t"
      "v_max_f32 v22, v22, v23\n\t"
      "v_mov_b32 v23, v22\n\t"
      "s_nop 1\n\t"
      "v_permlane32_swap_b32 v22, v23\n\t"
      "s_nop 0\n\t"
      "v_max_f32 v22, v22, v23\n\t"
      "v_sub_f32 v23, v20, v22\n\t"
      "v_mul_f32 v24, %[nc2], v22\n\t"
      "v_mul_f32 v23, %[c2], v23\n\t"
      "v_mov_b32 v20, v22\n\t"
      "v_exp_f32 v23, v23\n\t"
      "v_fma_f32 v26, v26, %[c2], v24\n\t"
      "v_fma_f32 v27, v27, %[c2], v24\n\t"
      "v_fma_f32 v28, v28, %[c2], v24\n\t"
      "v_fma_f32 v29, v29, %[c2], v24\n\t"
      "v_fma_f32 v30, v30, %[c2], v24\n\t"
      "v_fma_f32 v31, v31, %[c2], v24\n\t"
      "v_fma_f32 v32, v32, %[c2], v24\n\t"
      "v_fma_f32 v33, v33, %[c2], v24\n\t"
      "v_fma_f32 v40, v40, %[c2], v24\n\t"
      "v_fma_f32 v44, v44, %[c2], v24\n\t"
      "v_fma_f32 v48, v48, %[c2], v24\n\t"
      "v_fma_f32 v52, v52, %[c2], v24\n\t"
      "v_fma_f32 v56, v56, %[c2], v24\n\t"
      "v_fma_f32 v60, v60, %[c2], v24\n\t"
      "v_fma_f32 v64, v64, %[c2], v24\n\t"
      "v_fma_f32 v68, v68, %[c2], v24\n\t"
      "v_exp_f32 v26, v26\n\t"
      "v_mul_f32 v12, v23, v12\n\t"
      "v_exp_f32 v27, v27\n\t"
      "v_mul_f32 v13, v23, v13\n\t"
      "v_exp_f32 v28, v28\n\t"
      "v_mul_f32 v14, v23, v14\n\t"
      "v_exp_f32 v29, v29\n\t"
      "v_mul_f32 v15, v23, v15\n\t"
      "v_exp_f32 v30, v30\n\t"
      "v_mul_f32 v16, v23, v16\n\t"
      "v_exp_f32 v31, v31\n\t"
      "v_mul_f32 v17, v23, v17\n\t"
      "v_exp_f32 v32, v32\n\t"
      "v_mul_f32 v18, v23, v18\n\t"
      "v_exp_f32 v33, v33\n\t"
      "v_mul_f32 v19, v23, v19\n\t"
      "v_exp_f32 v40, v40\n\t"
      "v_exp_f32 v44, v44\n\t"
      "v_exp_f32 v48, v48\n\t"
      "v_exp_f32 v52, v52\n\t"
      "v_exp_f32 v56, v56\n\t"
      "v_exp_f32 v60, v60\n\t"
      "v_exp_f32 v64, v64\n\t"
      "v_exp_f32 v68, v68\n\t"
      "v_mul_f32 v21, v21, v23\n\t"
      "v_add_f32 v22, v26, v27\n\t"
      "v_add_f32 v23, v28, v29\n\t"
      "v_add_f32 v22, v22, v30\n\t"
      "v_add_f32 v23, v23, v31\n\t"
      "v_add_f32 v22, v22, v32\n\t"
      "v_add_f32 v23, v23, v33\n\t"
      "v_add_f32 v22, v22, v40\n\t"
      "v_add_f32 v23, v23, v44\n\t"
      "v_add_f32 v22, v22, v48\n\t"
      "v_add_f32 v23, v23, v52\n\t"
      "v_add_f32 v22, v22, v56\n\t"
      "v_add_f32 v23, v23, v60\n\t"
      "v_add_f32 v22, v22, v64\n\t"
      "v_add_f32 v23, v23, v68\n\t"
      "v_add_f32 v21, v21, v22\n\t"
      "v_cvt_pk_bf16_f32 v34, v26, v27\n\t"
      "v_cvt_pk_bf16_f32 v35, v28, v29\n\t"
      "v_cvt_pk_bf16_f32 v36, v30, v31\n\t"
      "v_cvt_pk_bf16_f32 v37, v32, v33\n\t"
      "v_add_f32 v21, v21, v23\n\t"
      "v_cvt_pk_bf16_f32 v38, v40, v44\n\t"
      "v_cvt_pk_bf16_f32 v39, v48, v52\n\t"
      "v_cvt_pk_bf16_f32 v24, v56, v60\n\t"
      "v_cvt_pk_bf16_f32 v25, v64, v68\n\t"
      "ds_read_b128 v[40:43], %[lds] offset:0\n\t"
      "ds_read_b128 v[44:47], %[lds] offset:1024\n\t"
      "ds_read_b128 v[48:51], %[lds] offset:2048\n\t"
      "ds_read_b128 v[52:55], %[lds] offset:3072\n\t"
      "ds_read_b128 v[56:59], %[lds] offset:4096\n\t"
      "ds_read_b128 v[60:63], %[lds] offset:5120\n\t"
      "ds_read_b128 v[64:67], %[lds] offset:6144\n\t"
      "ds_read_b128 v[68:71], %[lds] offset:7168\n\t"
      "s_waitcnt lgkmcnt(0)\n\t"
      "v_cndmask_b32 v26, 0, v34, %[m0]\n\t"
      "v_cndmask_b32 v27, 0, v35, %[m0]\n\t"
      "v_cndmask_b32 v28, 0, v36, %[m0]\n\t"
      "v_cndmask_b32 v29, 0, v37, %[m0]\n\t"
      "v_cndmask_b32 v30, 0, v34, %[m1]\n\t"
      "v_cndmask_b32 v31, 0, v35, %[m1]\n\t"
      "v_cndmask_b32 v32, 0, v36, %[m1]\n\t"
      "v_cndmask_b32 v33, 0, v37, %[m1]\n\t"
      "v_mfma_f32_16x16x32_bf16 v[12:15], v[40:43], v[26:29], v[12:15]\n\t"
      "v_mfma_f32_16x16x32_bf16 v[16:19], v[56:59], v[26:29], v[16:19]\n\t"
      "v_cndmask_b32 v26, 0, v34, %[m2]\n\t"
      "v_cndmask_b32 v27, 0, v35, %[m2]\n\t"
      "v_cndmask_b32 v28, 0, v36, %[m2]\n\t"
      "v_cndmask_b32 v29, 0, v37, %[m2]\n\t"
      "v_mfma_f32_16x16x32_bf16 v[12:15], v[44:47], v[30:33], v[12:15]\n\t"
      "v_mfma_f32_16x16x32_bf16 v[16:19], v[60:63], v[30:33], v[16:19]\n\t"
      "v_cndmask_b32 v30, 0, v34, %[m3]\n\t"
      "v_cndmask_b32 v31, 0, v35, %[m3]\n\t"
      "v_cndmask_b32 v32, 0, v36, %[m3]\n\t"
      "v_cndmask_b32 v33, 0, v37, %[m3]\n\t"
      "v_mfma_f32_16x16x32_bf16 v[12:15], v[48:51], v[26:29], v[12:15]\n\t"
      "v_mfma_f32_16x16x32_bf16 v[16:19], v[64:67], v[26:29], v[16:19]\n\t"
      "v_mfma_f32_16x16x32_bf16 v[12:15], v[52:55], v[30:33], v[12:15]\n\t"
      "v_mfma_f32_16x16x32_bf16 v[16:19], v[68:71], v[30:33], v[16:19]\n\t"
      "ds_read_b128 v[40:43], %[lds] offset:8192\n\t"
      "ds_read_b128 v[44:47], %[lds] offset:9216\n\t"
      "ds_read_b128 v[48:51], %[lds] offset:10240\n\t"
      "ds_read_b128 v[52:55], %[lds] offset:11264\n\t"
      "ds_read_b128 v[56:59], %[lds] offset:12288\n\t"
      "ds_read_b128 v[60:63], %[lds] offset:13312\n\t"
      "ds_read_b128 v[64:67], %[lds] offset:14336\n\t"
      "ds_read_b128 v[68:71], %[lds] offset:15360\n\t"
      "s_waitcnt lgkmcnt(0)\n\t"
      "v_cndmask_b32 v26, 0, v38, %[m0]\n\t"
      "v_cndmask_b32 v27, 0, v39, %[m0]\n\t"
      "v_cndmask_b32 v28, 0, v24, %[m0]\n\t"
      "v_cndmask_b32 v29, 0, v25, %[m0]\n\t"
      "v_cndmask_b32 v30, 0, v38, %[m1]\n\t"
      "v_cndmask_b32 v31, 0, v39, %[m1]\n\t"
      "v_cndmask_b32 v32, 0, v24, %[m1]\n\t"
      "v_cndmask_b32 v33, 0, v25, %[m1]\n\t"
      "v_mfma_f32_16x16x32_bf16 v[12:15], v[40:43], v[26:29], v[12:15]\n\t"
      "v_mfma_f32_16x16x32_bf16 v[16:19], v[56:59], v[26:29], v[16:19]\n\t"
      "v_cndmask_b32 v26, 0, v38, %[m2]\n\t"
      "v_cndmask_b32 v27, 0, v39, %[m2]\n\t"
      "v_cndmask_b32 v28, 0, v24, %[m2]\n\t"
      "v_cndmask_b32 v29, 0, v25, %[m2]\n\t"
      "v_mfma_f32_16x16x32_bf16 v[12:15], v[44:47], v[30:33], v[12:15]\n\t"
      "v_mfma_f32_16x16x32_bf16 v[16:19], v[60:63], v[30:33], v[16:19]\n\t"
      "v_cndmask_b32 v30, 0, v38, %[m3]\n\t"
      "v_cndmask_b32 v31, 0, v39, %[m3]\n\t"
      "v_cndmask_b32 v32, 0, v24, %[m3]\n\t"
      "v_cndmask_b32 v33, 0, v25, %[m3]\n\t"
      "v_mfma_f32_16x16x32_bf16 v[12:15], v[48:51], v[26:29], v[12:15]\n\t"
      "v_mfma_f32_16x16x32_bf16 v[16:19], v[64:67], v[26:29], v[16:19]\n\t"
      "v_mfma_f32_16x16x32_bf16 v[12:15], v[52:55], v[30:33], v[12:15]\n\t"
      "v_mfma_f32_16x16x32_bf16 v[16:19], v[68:71], v[30:33], v[16:19]\n\t"
      "s_waitcnt vmcnt(32)\n\t"
      "s_waitcnt lgkmcnt(0)\n\t"
      "v_mfma_f32_16x16x32_bf16 v[40:43], a[64:67], v[8:11], 0\n\t"
      "v_mfma_f32_16x16x32_bf16 v[44:47], a[68:71], v[8:11], 0\n\t"
      "v_mfma_f32_16x16x32_bf16 v[48:51], a[72:75], v[8:11], 0\n\t"
      "v_mfma_f32_16x16x32_bf16 v[52:55], a[76:79], v[8:11], 0\n\t"
      "v_mfma_f32_16x16x32_bf16 v[56:59], a[80:83], v[8:11], 0\n\t"
      "v_mfma_f32_16x16x32_bf16 v[60:63], a[84:87], v[8:11], 0\n\t"
      "v_mfma_f32_16x16x32_bf16 v[64:67], a[88:91], v[8:11], 0\n\t"
      "v_mfma_f32_16x16x32_bf16 v[68:71], a[92:95], v[8:11], 0\n\t"
      "s_nop 1\n\t"
      "v_mov_b32_dpp v26, v40 quad_perm:[0,0,0,0] row_mask:0xf bank_mask:0xf\n\t"
      "v_mov_b32_dpp v27, v44 quad_perm:[0,0,0,0] row_mask:0xf bank_mask:0xf\n\t"
      "v_mov_b32_dpp v28, v48 quad_perm:[0,0,0,0] row_mask:0xf bank_mask:0xf\n\t"
      "v_mov_b32_dpp v29, v52 quad_perm:[0,0,0,0] row_mask:0xf bank_mask:0xf\n\t"
      "v_add_f32_dpp v26, v41, v26 quad_perm:[1,1,1,1] row_mask:0xf bank_mask:0xf\n\t"
      "v_add_f32_dpp v27, v45, v27 quad_perm:[1,1,1,1] row_mask:0xf bank_mask:0xf\n\t"
      "v_add_f32_dpp v28, v49, v28 quad_perm:[1,1,1,1] row_mask:0xf bank_mask:0xf\n\t"
      "v_add_f32_dpp v29, v53, v29 quad_perm:[1,1,1,1] row_mask:0xf bank_mask:0xf\n\t"
      "v_add_f32_dpp v26, v42, v26 quad_perm:[2,2,2,2] row_mask:0xf bank_mask:0xf\n\t"
      "v_add_f32_dpp v27, v46, v27 quad_perm:[2,2,2,2] row_mask:0xf bank_mask:0xf\n\t"
      "v_add_f32_dpp v28, v50, v28 quad_perm:[2,2,2,2] row_mask:0xf bank_mask:0xf\n\t"
      "v_add_f32_dpp v29, v54, v29 quad_perm:[2,2,2,2] row_mask:0xf bank_mask:0xf\n\t"
      "v_add_f32_dpp v26, v43, v26 quad_perm:[3,3,3,3] row_mask:0xf bank_mask:0xf\n\t"
      "v_add_f32_dpp v27, v47, v27 quad_perm:[3,3,3,3] row_mask:0xf bank_mask:0xf\n\t"
      "v_add_f32_dpp v28, v51, v28 quad_perm:[3,3,3,3] row_mask:0xf bank_mask:0xf\n\t"
      "v_add_f32_dpp v29, v55, v29 quad_perm:[3,3,3,3] row_mask:0xf bank_mask:0xf\n\t"
      "v_mov_b32_dpp v30, v56 quad_perm:[0,0,0,0] row_mask:0xf bank_mask:0xf\n\t"
      "v_mov_b32_dpp v31, v60 quad_perm:[0,0,0,0] row_mask:0xf bank_mask:0xf\n\t"
      "v_mov_b32_dpp v32, v64 quad_perm:[0,0,0,0] row_mask:0xf bank_mask:0xf\n\t"
      "v_mov_b32_dpp v33, v68 quad_perm:[0,0,0,0] row_mask:0xf bank_mask:0xf\n\t"
      "v_add_f32_dpp v30, v57, v30 quad_perm:[1,1,1,1] row_mask:0xf bank_mask:0xf\n\t"
      "v_add_f32_dpp v31, v61, v31 quad_perm:[1,1,1,1] row_mask:0xf bank_mask:0xf\n\t"
      "v_add_f32_dpp v32, v65, v32 quad_perm:[1,1,1,1] row_mask:0xf bank_mask:0xf\n\t"
      "v_add_f32_dpp v33, v69, v33 quad_perm:[1,1,1,1] row_mask:0xf bank_mask:0xf\n\t"
      "v_add_f32_dpp v30, v58, v30 quad_perm:[2,2,2,2] row_mask:0xf bank_mask:0xf\n\t"
      "v_add_f32_dpp v31, v62, v31 quad_perm:[2,2,2,2] row_mask:0xf bank_mask:0xf\n\t"
      "v_add_f32_dpp v32, v66, v32 quad_perm:[2,2,2,2] row_mask:0xf bank_mask:0xf\n\t"
      "v_add_f32_dpp v33, v70, v33 quad_perm:[2,2,2,2] row_mask:0xf bank_mask:0xf\n\t"
      "v_add_f32_dpp v30, v59, v30 quad_perm:[3,3,3,3] row_mask:0xf bank_mask:0xf\n\t"
      "v_add_f32_dpp v31, v63, v31 quad_perm:[3,3,3,3] row_mask:0xf bank_mask:0xf\n\t"
      "v_add_f32_dpp v32, v67, v32 quad_perm:[3,3,3,3] row_mask:0xf bank_mask:0xf\n\t"
      "v_add_f32_dpp v33, v71, v33 quad_perm:[3,3,3,3] row_mask:0xf bank_mask:0xf\n\t"
      "v_mfma_f32_16x16x32_bf16 v[40:43], a[96:99], v[8:11], 0\n\t"
      "v_mfma_f32_16x16x32_bf16 v[44:47], a[100:103], v[8:11], 0\n\t"
      "v_mfma_f32_16x16x32_bf16 v[48:51], a[104:107], v[8:11], 0\n\t"
      "v_mfma_f32_16x16x32_bf16 v[52:55], a[108:111], v[8:11], 0\n\t"
      "v_mfma_f32_16x16x32_bf16 v[56:59], a[112:115], v[8:11], 0\n\t"
      "v_mfma_f32_16x16x32_bf16 v[60:63], a[116:119], v[8:11], 0\n\t"
      "v_mfma_f32_16x16x32_bf16 v[64:67], a[120:123], v[8:11], 0\n\t"
      "v_mfma_f32_16x16x32_bf16 v[68:71], a[124:127], v[8:11], 0\n\t"
      "s_nop 1\n\t"
      "v_mov_b32_dpp v40, v40 quad_perm:[0,0,0,0] row_mask:0xf bank_mask:0xf\n\t"
      "v_mov_b32_dpp v44, v44 quad_perm:[0,0,0,0] row_mask:0xf bank_mask:0xf\n\t"
      "v_mov_b32_dpp v48, v48 quad_perm:[0,0,0,0] row_mask:0xf bank_mask:0xf\n\t"
      "v_mov_b32_dpp v52, v52 quad_perm:[0,0,0,0] row_mask:0xf bank_mask:0xf\n\t"
      "v_add_f32_dpp v40, v41, v40 quad_perm:[1,1,1,1] row_mask:0xf bank_mask:0xf\n\t"
      "v_add_f32_dpp v44, v45, v44 quad_perm:[1,1,1,1] row_mask:0xf bank_mask:0xf\n\t"
      "v_add_f32_dpp v48, v49, v48 quad_perm:[1,1,1,1] row_mask:0xf bank_mask:0xf\n\t"
      "v_add_f32_dpp v52, v53, v52 quad_perm:[1,1,1,1] row_mask:0xf bank_mask:0xf\n\t"
      "v_add_f32_dpp v40, v42, v40 quad_perm:[2,2,2,2] row_mask:0xf bank_mask:0xf\n\t"
      "v_add_f32_dpp v44, v46, v44 quad_perm:[2,2,2,2] row_mask:0xf bank_mask:0xf\n\t"
      "v_add_f32_dpp v48, v50, v48 quad_perm:[2,2,2,2] row_mask:0xf bank_mask:0xf\n\t"
      "v_add_f32_dpp v52, v54, v52 quad_perm:[2,2,2,2] row_mask:0xf bank_mask:0xf\n\t"
      "v_add_f32_dpp v40, v43, v40 quad_perm:[3,3,3,3] row_mask:0xf bank_mask:0xf\n\t"
      "v_add_f32_dpp v44, v47, v44 quad_perm:[3,3,3,3] row_mask:0xf bank_mask:0xf\n\t"
      "v_add_f32_dpp v48, v51, v48 quad_perm:[3,3,3,3] row_mask:0xf bank_mask:0xf\n\t"
      "v_add_f32_dpp v52, v55, v52 quad_perm:[3,3,3,3] row_mask:0xf bank_mask:0xf\n\t"
      "v_mov_b32_dpp v56, v56 quad_perm:[0,0,0,0] row_mask:0xf bank_mask:0xf\n\t"
      "v_mov_b32_dpp v60, v60 quad_perm:[0,0,0,0] row_mask:0xf bank_mask:0xf\n\t"
      "v_mov_b32_dpp v64, v64 quad_perm:[0,0,0,0] row_mask:0xf bank_mask:0xf\n\t"
      "v_mov_b32_dpp v68, v68 quad_perm:[0,0,0,0] row_mask:0xf bank_mask:0xf\n\t"
      "v_add_f32_dpp v56, v57, v56 quad_perm:[1,1,1,1] row_mask:0xf bank_mask:0xf\n\t"
      "v_add_f32_dpp v60, v61, v60 quad_perm:[1,1,1,1] row_mask:0xf bank_mask:0xf\n\t"
      "v_add_f32_dpp v64, v65, v64 quad_perm:[1,1,1,1] row_mask:0xf bank_mask:0xf\n\t"
      "v_add_f32_dpp v68, v69, v68 quad_perm:[1,1,1,1] row_mask:0xf bank_mask:0xf\n\t"
      "v_add_f32_dpp v56, v58, v56 quad_perm:[2,2,2,2] row_mask:0xf bank_mask:0xf\n\t"
      "v_add_f32_dpp v60, v62, v60 quad_perm:[2,2,2,2] row_mask:0xf bank_mask:0xf\n\t"
      "v_add_f32_dpp v64, v66, v64 quad_perm:[2,2,2,2] row_mask:0xf bank_mask:0xf\n\t"
      "v_add_f32_dpp v68, v70, v68 quad_perm:[2,2,2,2] row_mask:0xf bank_mask:0xf\n\t"
      "v_add_f32_dpp v56, v59, v56 quad_perm:[3,3,3,3] row_mask:0xf bank_mask:0xf\n\t"
      "v_add_f32_dpp v60, v63, v60 quad_perm:[3,3,3,3] row_mask:0xf bank_mask:0xf\n\t"
      "v_add_f32_dpp v64, v67, v64 quad_perm:[3,3,3,3] row_mask:0xf bank_mask:0xf\n\t"
      "v_add_f32_dpp v68, v71, v68 quad_perm:[3,3,3,3] row_mask:0xf bank_mask:0xf\n\t"
      "v_max3_f32 v22, v26, v27, v20\n\t"
      "v_max3_f32 v23, v28, v29, v30\n\t"
      "v_max3_f32 v22, v22, v31, v32\n\t"
      "v_max3_f32 v23, v23, v33, v40\n\t"
      "v_max3_f32 v22, v22, v44, v48\n\t"
      "v_max3_f32 v23, v23, v52, v56\n\t"
      "v_max3_f32 v22, v22, v60, v64\n\t"
      "v_max3_f32 v22, v22, v23, v68\n\t"
      "v_mov_b32 v23, v22\n\t"
      "s_nop 1\n\t"
      "v_permlane16_swap_b32 v22, v23\n\t"
      "s_nop 0\n\t"
      "v_max_f32 v22, v22, v23\n\t"
      "v_mov_b32 v23, v22\n\t"
      "s_nop 1\n\t"
      "v_permlane32_swap_b32 v22, v23\n\t"
      "s_nop 0\n\t"
      "v_max_f32 v22, v22, v23\n\t"
      "v_sub_f32 v23, v20, v22\n\t"
      "v_mul_f32 v24, %[nc2], v22\n\t"
      "v_mul_f32 v23, %[c2], v23\n\t"
      "v_mov_b32 v20, v22\n\t"
      "v_exp_f32 v23, v23\n\t"
      "v_fma_f32 v26, v26, %[c2], v24\n\t"
      "v_fma_f32 v27, v27, %[c2], v24\n\t"
      "v_fma_f32 v28, v28, %[c2], v24\n\t"
      "v_fma_f32 v29, v29, %[c2], v24\n\t"
      "v_fma_f32 v30, v30, %[c2], v24\n\t"
      "v_fma_f32 v31, v31, %[c2], v24\n\t"
      "v_fma_f32 v32, v32, %[c2], v24\n\t"
      "v_fma_f32 v33, v33, %[c2], v24\n\t"
      "v_fma_f32 v40, v40, %[c2], v24\n\t"
      "v_fma_f32 v44, v44, %[c2], v24\n\t"
      "v_fma_f32 v48, v48, %[c2], v24\n\t"
      "v_fma_f32 v52, v52, %[c2], v24\n\t"
      "v_fma_f32 v56, v56, %[c2], v24\n\t"
      "v_fma_f32 v60, v60, %[c2], v24\n\t"
      "v_fma_f32 v64, v64, %[c2], v24\n\t"
      "v_fma_f32 v68, v68, %[c2], v24\n\t"
      "v_exp_f32 v26, v26\n\t"
      "v_mul_f32 v12, v23, v12\n\t"
      "v_exp_f32 v27, v27\n\t"
      "v_mul_f32 v13, v23, v13\n\t"
      "v_exp_f32 v28, v28\n\t"
      "v_mul_f32 v14, v23, v14\n\t"
      "v_exp_f32 v29, v29\n\t"
      "v_mul_f32 v15, v23, v15\n\t"
      "v_exp_f32 v30, v30\n\t"
      "v_mul_f32 v16, v23, v16\n\t"
      "v_exp_f32 v31, v31\n\t"
      "v_mul_f32 v17, v23, v17\n\t"
      "v_exp_f32 v32, v32\n\t"
      "v_mul_f32 v18, v23, v18\n\t"
      "v_exp_f32 v33, v33\n\t"
      "v_mul_f32 v19, v23, v19\n\t"
      "v_exp_f32 v40, v40\n\t"
      "v_exp_f32 v44, v44\n\t"
      "v_exp_f32 v48, v48\n\t"
      "v_exp_f32 v52, v52\n\t"
      "v_exp_f32 v56, v56\n\t"
      "v_exp_f32 v60, v60\n\t"
      "v_exp_f32 v64, v64\n\t"
      "v_exp_f32 v68, v68\n\t"
      "v_mul_f32 v21, v21, v23\n\t"
      "v_add_f32 v22, v26, v27\n\t"
      "v_add_f32 v23, v28, v29\n\t"
      "v_add_f32 v22, v22, v30\n\t"
      "v_add_f32 v23, v23, v31\n\t"
      "v_add_f32 v22, v22, v32\n\t"
      "v_add_f32 v23, v23, v33\n\t"
      "v_add_f32 v22, v22, v40\n\t"
      "v_add_f32 v23, v23, v44\n\t"
      "v_add_f32 v22, v22, v48\n\t"
      "v_add_f32 v23, v23, v52\n\t"
      "v_add_f32 v22, v22, v56\n\t"
      "v_add_f32 v23, v23, v60\n\t"
      "v_add_f32 v22, v22, v64\n\t"
      "v_add_f32 v23, v23, v68\n\t"
      "v_add_f32 v21, v21, v22\n\t"
      "v_cvt_pk_bf16_f32 v34, v26, v27\n\t"
      "v_cvt_pk_bf16_f32 v35, v28, v29\n\t"
      "v_cvt_pk_bf16_f32 v36, v30, v31\n\t"
      "v_cvt_pk_bf16_f32 v37, v32, v33\n\t"
      "v_add_f32 v21, v21, v23\n\t"
      "v_cvt_pk_bf16_f32 v38, v40, v44\n\t"
      "v_cvt_pk_bf16_f32 v39, v48, v52\n\t"
      "v_cvt_pk_bf16_f32 v24, v56, v60\n\t"
      "v_cvt_pk_bf16_f32 v25, v64, v68\n\t"
      "s_waitcnt vmcnt(24)\n\t"
      "v_perm_b32 v40, v228, v224, %[slo]\n\t"
      "v_perm_b32 v44, v228, v224, %[shi]\n\t"
      "v_perm_b32 v41, v236, v232, %[slo]\n\t"
      "v_perm_b32 v45, v236, v232, %[shi]\n\t"
      "v_perm_b32 v42, v244, v240, %[slo]\n\t"
      "v_perm_b32 v46, v244, v240, %[shi]\n\t"
      "v_perm_b32 v43, v252, v248, %[slo]\n\t"
      "v_perm_b32 v47, v252, v248, %[shi]\n\t"
      "v_perm_b32 v48, v229, v225, %[slo]\n\t"
      "v_perm_b32 v52, v229, v225, %[shi]\n\t"
      "v_perm_b32 v49, v237, v233, %[slo]\n\t"
      "v_perm_b32 v53, v237, v233, %[shi]\n\t"
      "v_perm_b32 v50, v245, v241, %[slo]\n\t"
      "v_perm_b32 v54, v245, v241, %[shi]\n\t"
      "v_perm_b32 v51, v253, v249, %[slo]\n\t"
      "v_perm_b32 v55, v253, v249, %[shi]\n\t"
      "v_perm_b32 v56, v230, v226, %[slo]\n\t"
      "v_perm_b32 v60, v230, v226, %[shi]\n\t"
      "v_perm_b32 v57, v238, v234, %[slo]\n\t"
      "v_perm_b32 v61, v238, v234, %[shi]\n\t"
      "v_perm_b32 v58, v246, v242, %[slo]\n\t"
      "v_perm_b32 v62, v246, v242, %[shi]\n\t"
      "v_perm_b32 v59, v254, v250, %[slo]\n\t"
      "v_perm_b32 v63, v254, v250, %[shi]\n\t"
      "v_perm_b32 v64, v231, v227, %[slo]\n\t"
      "v_perm_b32 v68, v231, v227, %[shi]\n\t"
      "v_perm_b32 v65, v239, v235, %[slo]\n\t"
      "v_perm_b32 v69, v239, v235, %[shi]\n\t"
      "v_perm_b32 v66, v247, v243, %[slo]\n\t"
      "v_perm_b32 v70, v247, v243, %[shi]\n\t"
      "v_perm_b32 v67, v255, v251, %[slo]\n\t"
      "v_perm_b32 v71, v255, v251, %[shi]\n\t"
      "v_cndmask_b32 v26, 0, v34, %[m0]\n\t"
      "v_cndmask_b32 v27, 0, v35, %[m0]\n\t"
      "v_cndmask_b32 v28, 0, v36, %[m0]\n\t"
      "v_cndmask_b32 v29, 0, v37, %[m0]\n\t"
      "v_cndmask_b32 v30, 0, v34, %[m1]\n\t"
      "v_cndmask_b32 v31, 0, v35, %[m1]\n\t"
      "v_cndmask_b32 v32, 0, v36, %[m1]\n\t"
      "v_cndmask_b32 v33, 0, v37, %[m1]\n\t"
      "v_mfma_f32_16x16x32_bf16 v[12:15], v[40:43], v[26:29], v[12:15]\n\t"
      "v_mfma_f32_16x16x32_bf16 v[16:19], v[56:59], v[26:29], v[16:19]\n\t"
      "v_cndmask_b32 v26, 0, v34, %[m2]\n\t"
      "v_cndmask_b32 v27, 0, v35, %[m2]\n\t"
      "v_cndmask_b32 v28, 0, v36, %[m2]\n\t"
      "v_cndmask_b32 v29, 0, v37, %[m2]\n\t"
      "v_mfma_f32_16x16x32_bf16 v[12:15], v[44:47], v[30:33], v[12:15]\n\t"
      "v_mfma_f32_16x16x32_bf16 v[16:19], v[60:63], v[30:33], v[16:19]\n\t"
      "v_cndmask_b32 v30, 0, v34, %[m3]\n\t"
      "v_cndmask_b32 v31, 0, v35, %[m3]\n\t"
      "v_cndmask_b32 v32, 0, v36, %[m3]\n\t"
      "v_cndmask_b32 v33, 0, v37, %[m3]\n\t"
      "v_mfma_f32_16x16x32_bf16 v[12:15], v[48:51], v[26:29], v[12:15]\n\t"
      "v_mfma_f32_16x16x32_bf16 v[16:19], v[64:67], v[26:29], v[16:19]\n\t"
      "v_mfma_f32_16x16x32_bf16 v[12:15], v[52:55], v[30:33], v[12:15]\n\t"
      "v_mfma_f32_16x16x32_bf16 v[16:19], v[68:71], v[30:33], v[16:19]\n\t"
      "s_waitcnt vmcnt(16)\n\t"
      "v_perm_b32 v40, v164, v160, %[slo]\n\t"
      "v_perm_b32 v44, v164, v160, %[shi]\n\t"
      "v_perm_b32 v41, v172, v168, %[slo]\n\t"
      "v_perm_b32 v45, v172, v168, %[shi]\n\t"
      "v_perm_b32 v42, v180, v176, %[slo]\n\t"
      "v_perm_b32 v46, v180, v176, %[shi]\n\t"
      "v_perm_b32 v43, v188, v184, %[slo]\n\t"
      "v_perm_b32 v47, v188, v184, %[shi]\n\t"
      "v_perm_b32 v48, v165, v161, %[slo]\n\t"
      "v_perm_b32 v52, v165, v161, %[shi]\n\t"
      "v_perm_b32 v49, v173, v169, %[slo]\n\t"
      "v_perm_b32 v53, v173, v169, %[shi]\n\t"
      "v_perm_b32 v50, v181, v177, %[slo]\n\t"
      "v_perm_b32 v54, v181, v177, %[shi]\n\t"
      "v_perm_b32 v51, v189, v185, %[slo]\n\t"
      "v_perm_b32 v55, v189, v185, %[shi]\n\t"
      "v_perm_b32 v56, v166, v162, %[slo]\n\t"
      "v_perm_b32 v60, v166, v162, %[shi]\n\t"
      "v_perm_b32 v57, v174, v170, %[slo]\n\t"
      "v_perm_b32 v61, v174, v170, %[shi]\n\t"
      "v_perm_b32 v58, v182, v178, %[slo]\n\t"
      "v_perm_b32 v62, v182, v178, %[shi]\n\t"
      "v_perm_b32 v59, v190, v186, %[slo]\n\t"
      "v_perm_b32 v63, v190, v186, %[shi]\n\t"
      "v_perm_b32 v64, v167, v163, %[slo]\n\t"
      "v_perm_b32 v68, v167, v163, %[shi]\n\t"
      "v_perm_b32 v65, v175, v171, %[slo]\n\t"
      "v_perm_b32 v69, v175, v171, %[shi]\n\t"
      "v_perm_b32 v66, v183, v179, %[slo]\n\t"
      "v_perm_b32 v70, v183, v179, %[shi]\n\t"
      "v_perm_b32 v67, v191, v187, %[slo]\n\t"
      "v_perm_b32 v71, v191, v187, %[shi]\n\t"
      "v_cndmask_b32 v26, 0, v38, %[m0]\n\t"
      "v_cndmask_b32 v27, 0, v39, %[m0]\n\t"
      "v_cndmask_b32 v28, 0, v24, %[m0]\n\t"
      "v_cndmask_b32 v29, 0, v25, %[m0]\n\t"
      "v_cndmask_b32 v30, 0, v38, %[m1]\n\t"
      "v_cndmask_b32 v31, 0, v39, %[m1]\n\t"
      "v_cndmask_b32 v32, 0, v24, %[m1]\n\t"
      "v_cndmask_b32 v33, 0, v25, %[m1]\n\t"
      "v_mfma_f32_16x16x32_bf16 v[12:15], v[40:43], v[26:29], v[12:15]\n\t"
      "v_mfma_f32_16x16x32_bf16 v[16:19], v[56:59], v[26:29], v[16:19]\n\t"
      "v_cndmask_b32 v26, 0, v38, %[m2]\n\t"
      "v_cndmask_b32 v27, 0, v39, %[m2]\n\t"
      "v_cndmask_b32 v28, 0, v24, %[m2]\n\t"
      "v_cndmask_b32 v29, 0, v25, %[m2]\n\t"
      "v_mfma_f32_16x16x32_bf16 v[12:15], v[44:47], v[30:33], v[12:15]\n\t"
      "v_mfma_f32_16x16x32_bf16 v[16:19], v[60:63], v[30:33], v[16:19]\n\t"
      "v_cndmask_b32 v30, 0, v38, %[m3]\n\t"
      "v_cndmask_b32 v31, 0, v39, %[m3]\n\t"
      "v_cndmask_b32 v32, 0, v24, %[m3]\n\t"
      "v_cndmask_b32 v33, 0, v25, %[m3]\n\t"
      "v_mfma_f32_16x16x32_bf16 v[12:15], v[48:51], v[26:29], v[12:15]\n\t"
      "v_mfma_f32_16x16x32_bf16 v[16:19], v[64:67], v[26:29], v[16:19]\n\t"
      "v_mfma_f32_16x16x32_bf16 v[12:15], v[52:55], v[30:33], v[12:15]\n\t"
      "v_mfma_f32_16x16x32_bf16 v[16:19], v[68:71], v[30:33], v[16:19]\n\t"
      "1:\n\t"
      "s_memrealtime s[96:97]\n\t"
      "s_waitcnt lgkmcnt(0)\n\t"
      "s_sub_u32 %[so0], s96, %[due]\n\t"
      "s_cmp_lt_i32 %[so0], 0\n\t"
      "s_cbranch_scc0 2f\n\t"
      "s_sleep 1\n\t"
      "s_branch 1b\n\t"
      "2:\n\t"
      "s_add_u32 %[due], %[due], %[dl]\n\t"
      "s_add_u32 %[so0], %[rot], 16384\n\t"
      "s_and_b32 %[so0], %[so0], 0x1ffff\n\t"
      "s_add_u32 %[so1], %[so0], 4096\n\t"
      "buffer_load_dwordx4 a[0:3], %[koff], %[rk], %[so0] offen offset:0 nt\n\t"
      "buffer_load_dwordx4 a[4:7], %[koff], %[rk], %[so0] offen offset:1024 nt\n\t"
      "buffer_load_dwordx4 a[8:11], %[koff], %[rk], %[so0] offen offset:2048 nt\n\t"
      "buffer_load_dwordx4 a[12:15], %[koff], %[rk], %[so0] offen offset:3072 nt\n\t"
      "buffer_load_dwordx4 a[16:19], %[koff], %[rk], %[so1] offen offset:0 nt\n\t"
      "buffer_load_dwordx4 a[20:23], %[koff], %[rk], %[so1] offen offset:1024 nt\n\t"
      "buffer_load_dwordx4 a[24:27], %[koff], %[rk], %[so1] offen offset:2048 nt\n\t"
      "buffer_load_dwordx4 a[28:31], %[koff], %[rk], %[so1] offen offset:3072 nt\n\t"
      "s_add_u32 %[so0], %[rot], 24576\n\t"
      "s_and_b32 %[so0], %[so0], 0x1ffff\n\t"
      "s_add_u32 %[so1], %[so0], 4096\n\t"
      "buffer_load_dwordx4 a[32:35], %[koff], %[rk], %[so0] offen offset:0 nt\n\t"
      "buffer_load_dwordx4 a[36:39], %[koff], %[rk], %[so0] offen offset:1024 nt\n\t"
      "buffer_load_dwordx4 a[40:43], %[koff], %[rk], %[so0] offen offset:2048 nt\n\t"
      "buffer_load_dwordx4 a[44:47], %[koff], %[rk], %[so0] offen offset:3072 nt\n\t"
      "buffer_load_dwordx4 a[48:51], %[koff], %[rk], %[so1] offen offset:0 nt\n\t"
      "buffer_load_dwordx4 a[52:55], %[koff], %[rk], %[so1] offen offset:1024 nt\n\t"
      "buffer_load_dwordx4 a[56:59], %[koff], %[rk], %[so1] offen offset:2048 nt\n\t"
      "buffer_load_dwordx4 a[60:63], %[koff], %[rk], %[so1] offen offset:3072 nt\n\t"
      "s_waitcnt vmcnt(63)\n\t"
      "s_waitcnt lgkmcnt(0)\n\t"
      "v_mfma_f32_16x16x32_bf16 v[40:43], a[128:131], v[8:11], 0\n\t"
      "v_mfma_f32_16x16x32_bf16 v[44:47], a[132:135], v[8:11], 0\n\t"
      "v_mfma_f32_16x16x32_bf16 v[48:51], a[136:139], v[8:11], 0\n\t"
      "v_mfma_f32_16x16x32_bf16 v[52:55], a[140:143], v[8:11], 0\n\t"
      "v_mfma_f32_16x16x32_bf16 v[56:59], a[144:147], v[8:11], 0\n\t"
      "v_mfma_f32_16x16x32_bf16 v[60:63], a[148:151], v[8:11], 0\n\t"
      "v_mfma_f32_16x16x32_bf16 v[64:67], a[152:155], v[8:11], 0\n\t"
      "v_mfma_f32_16x16x32_bf16 v[68:71], a[156:159], v[8:11], 0\n\t"
      "s_nop 1\n\t"
      "v_mov_b32_dpp v26, v40 quad_perm:[0,0,0,0] row_mask:0xf bank_mask:0xf\n\t"
      "v_mov_b32_dpp v27, v44 quad_perm:[0,0,0,0] row_mask:0xf bank_mask:0xf\n\t"
      "v_mov_b32_dpp v28, v48 quad_perm:[0,0,0,0] row_mask:0xf bank_mask:0xf\n\t"
      "v_mov_b32_dpp v29, v52 quad_perm:[0,0,0,0] row_mask:0xf bank_mask:0xf\n\t"
      "v_add_f32_dpp v26, v41, v26 quad_perm:[1,1,1,1] row_mask:0xf bank_mask:0xf\n\t"
      "v_add_f32_dpp v27, v45, v27 quad_perm:[1,1,1,1] row_mask:0xf bank_mask:0xf\n\t"
      "v_add_f32_dpp v28, v49, v28 quad_perm:[1,1,1,1] row_mask:0xf bank_mask:0xf\n\t"
      "v_add_f32_dpp v29, v53, v29 quad_perm:[1,1,1,1] row_mask:0xf bank_mask:0xf\n\t"
      "v_add_f32_dpp v26, v42, v26 quad_perm:[2,2,2,2] row_mask:0xf bank_mask:0xf\n\t"
      "v_add_f32_dpp v27, v46, v27 quad_perm:[2,2,2,2] row_mask:0xf bank_mask:0xf\n\t"
      "v_add_f32_dpp v28, v50, v28 quad_perm:[2,2,2,2] row_mask:0xf bank_mask:0xf\n\t"
      "v_add_f32_dpp v29, v54, v29 quad_perm:[2,2,2,2] row_mask:0xf bank_mask:0xf\n\t"
      "v_add_f32_dpp v26, v43, v26 quad_perm:[3,3,3,3] row_mask:0xf bank_mask:0xf\n\t"
      "v_add_f32_dpp v27, v47, v27 quad_perm:[3,3,3,3] row_mask:0xf bank_mask:0xf\n\t"
      "v_add_f32_dpp v28, v51, v28 quad_perm:[3,3,3,3] row_mask:0xf bank_mask:0xf\n\t"
      "v_add_f32_dpp v29, v55, v29 quad_perm:[3,3,3,3] row_mask:0xf bank_mask:0xf\n\t"
      "v_mov_b32_dpp v30, v56 quad_perm:[0,0,0,0] row_mask:0xf bank_mask:0xf\n\t"
      "v_mov_b32_dpp v31, v60 quad_perm:[0,0,0,0] row_mask:0xf bank_mask:0xf\n\t"
      "v_mov_b32_dpp v32, v64 quad_perm:[0,0,0,0] row_mask:0xf bank_mask:0xf\n\t"
      "v_mov_b32_dpp v33, v68 quad_perm:[0,0,0,0] row_mask:0xf bank_mask:0xf\n\t"
      "v_add_f32_dpp v30, v57, v30 quad_perm:[1,1,1,1] row_mask:0xf bank_mask:0xf\n\t"
      "v_add_f32_dpp v31, v61, v31 quad_perm:[1,1,1,1] row_mask:0xf bank_mask:0xf\n\t"
      "v_add_f32_dpp v32, v65, v32 quad_perm:[1,1,1,1] row_mask:0xf bank_mask:0xf\n\t"
      "v_add_f32_dpp v33, v69, v33 quad_perm:[1,1,1,1] row_mask:0xf bank_mask:0xf\n\t"
      "v_add_f32_dpp v30, v58, v30 quad_perm:[2,2,2,2] row_mask:0xf bank_mask:0xf\n\t"
      "v_add_f32_dpp v31, v62, v31 quad_perm:[2,2,2,2] row_mask:0xf bank_mask:0xf\n\t"
      "v_add_f32_dpp v32, v66, v32 quad_perm:[2,2,2,2] row_mask:0xf bank_mask:0xf\n\t"
      "v_add_f32_dpp v33, v70, v33 quad_perm:[2,2,2,2] row_mask:0xf bank_mask:0xf\n\t"
      "v_add_f32_dpp v30, v59, v30 quad_perm:[3,3,3,3] row_mask:0xf bank_mask:0xf\n\t"
      "v_add_f32_dpp v31, v63, v31 quad_perm:[3,3,3,3] row_mask:0xf bank_mask:0xf\n\t"
      "v_add_f32_dpp v32, v67, v32 quad_perm:[3,3,3,3] row_mask:0xf bank_mask:0xf\n\t"
      "v_add_f32_dpp v33, v71, v33 quad_perm:[3,3,3,3] row_mask:0xf bank_mask:0xf\n\t"
      "v_mfma_f32_16x16x32_bf16 v[40:43], a[160:163], v[8:11], 0\n\t"
      "v_mfma_f32_16x16x32_bf16 v[44:47], a[164:167], v[8:11], 0\n\t"
      "v_mfma_f32_16x16x32_bf16 v[48:51], a[168:171], v[8:11], 0\n\t"
      "v_mfma_f32_16x16x32_bf16 v[52:55], a[172:175], v[8:11], 0\n\t"
      "v_mfma_f32_16x16x32_bf16 v[56:59], a[176:179], v[8:11], 0\n\t"
      "v_mfma_f32_16x16x32_bf16 v[60:63], a[180:183], v[8:11], 0\n\t"
      "v_mfma_f32_16x16x32_bf16 v[64:67], a[184:187], v[8:11], 0\n\t"
      "v_mfma_f32_16x16x32_bf16 v[68:71], a[188:191], v[8:11], 0\n\t"
      "s_nop 1\n\t"
      "v_mov_b32_dpp v40, v40 quad_perm:[0,0,0,0] row_mask:0xf bank_mask:0xf\n\t"
      "v_mov_b32_dpp v44, v44 quad_perm:[0,0,0,0] row_mask:0xf bank_mask:0xf\n\t"
      "v_mov_b32_dpp v48, v48 quad_perm:[0,0,0,0] row_mask:0xf bank_mask:0xf\n\t"
      "v_mov_b32_dpp v52, v52 quad_perm:[0,0,0,0] row_mask:0xf bank_mask:0xf\n\t"
      "v_add_f32_dpp v40, v41, v40 quad_perm:[1,1,1,1] row_mask:0xf bank_mask:0xf\n\t"
      "v_add_f32_dpp v44, v45, v44 quad_perm:[1,1,1,1] row_mask:0xf bank_mask:0xf\n\t"
      "v_add_f32_dpp v48, v49, v48 quad_perm:[1,1,1,1] row_mask:0xf bank_mask:0xf\n\t"
      "v_add_f32_dpp v52, v53, v52 quad_perm:[1,1,1,1] row_mask:0xf bank_mask:0xf\n\t"
      "v_add_f32_dpp v40, v42, v40 quad_perm:[2,2,2,2] row_mask:0xf bank_mask:0xf\n\t"
      "v_add_f32_dpp v44, v46, v44 quad_perm:[2,2,2,2] row_mask:0xf bank_mask:0xf\n\t"
      "v_add_f32_dpp v48, v50, v48 quad_perm:[2,2,2,2] row_mask:0xf bank_mask:0xf\n\t"
      "v_add_f32_dpp v52, v54, v52 quad_perm:[2,2,2,2] row_mask:0xf bank_mask:0xf\n\t"
      "v_add_f32_dpp v40, v43, v40 quad_perm:[3,3,3,3] row_mask:0xf bank_mask:0xf\n\t"
      "v_add_f32_dpp v44, v47, v44 quad_perm:[3,3,3,3] row_mask:0xf bank_mask:0xf\n\t"
      "v_add_f32_dpp v48, v51, v48 quad_perm:[3,3,3,3] row_mask:0xf bank_mask:0xf\n\t"
      "v_add_f32_dpp v52, v55, v52 quad_perm:[3,3,3,3] row_mask:0xf bank_mask:0xf\n\t"
      "v_mov_b32_dpp v56, v56 quad_perm:[0,0,0,0] row_mask:0xf bank_mask:0xf\n\t"
      "v_mov_b32_dpp v60, v60 quad_perm:[0,0,0,0] row_mask:0xf bank_mask:0xf\n\t"
      "v_mov_b32_dpp v64, v64 quad_perm:[0,0,0,0] row_mask:0xf bank_mask:0xf\n\t"
      "v_mov_b32_dpp v68, v68 quad_perm:[0,0,0,0] row_mask:0xf bank_mask:0xf\n\t"
      "v_add_f32_dpp v56, v57, v56 quad_perm:[1,1,1,1] row_mask:0xf bank_mask:0xf\n\t"
      "v_add_f32_dpp v60, v61, v60 quad_perm:[1,1,1,1] row_mask:0xf bank_mask:0xf\n\t"
      "v_add_f32_dpp v64, v65, v64 quad_perm:[1,1,1,1] row_mask:0xf bank_mask:0xf\n\t"
      "v_add_f32_dpp v68, v69, v68 quad_perm:[1,1,1,1] row_mask:0xf bank_mask:0xf\n\t"
      "v_add_f32_dpp v56, v58, v56 quad_perm:[2,2,2,2] row_mask:0xf bank_mask:0xf\n\t"
      "v_add_f32_dpp v60, v62, v60 quad_perm:[2,2,2,2] row_mask:0xf bank_mask:0xf\n\t"
      "v_add_f32_dpp v64, v66, v64 quad_perm:[2,2,2,2] row_mask:0xf bank_mask:0xf\n\t"
      "v_add_f32_dpp v68, v70, v68 quad_perm:[2,2,2,2] row_mask:0xf bank_mask:0xf\n\t"
      "v_add_f32_dpp v56, v59, v56 quad_perm:[3,3,3,3] row_mask:0xf bank_mask:0xf\n\t"
      "v_add_f32_dpp v60, v63, v60 quad_perm:[3,3,3,3] row_mask:0xf bank_mask:0xf\n\t"
      "v_add_f32_dpp v64, v67, v64 quad_perm:[3,3,3,3] row_mask:0xf bank_mask:0xf\n\t"
      "v_add_f32_dpp v68, v71, v68 quad_perm:[3,3,3,3] row_mask:0xf bank_mask:0xf\n\t"
      "v_max3_f32 v22, v26, v27, v20\n\t"
      "v_max3_f32 v23, v28, v29, v30\n\t"
      "v_max3_f32 v22, v22, v31, v32\n\t"
      "v_max3_f32 v23, v23, v33, v40\n\t"
      "v_max3_f32 v22, v22, v44, v48\n\t"
      "v_max3_f32 v23, v23, v52, v56\n\t"
      "v_max3_f32 v22, v22, v60, v64\n\t"
      "v_max3_f32 v22, v22, v23, v68\n\t"
      "v_mov_b32 v23, v22\n\t"
      "s_nop 1\n\t"
      "v_permlane16_swap_b32 v22, v23\n\t"
      "s_nop 0\n\t"
      "v_max_f32 v22, v22, v23\n\t"
      "v_mov_b32 v23, v22\n\t"
      "s_nop 1\n\t"
      "v_permlane32_swap_b32 v22, v23\n\t"
      "s_nop 0\n\t"
      "v_max_f32 v22, v22, v23\n\t"
      "v_sub_f32 v23, v20, v22\n\t"
      "v_mul_f32 v24, %[nc2], v22\n\t"
      "v_mul_f32 v23, %[c2], v23\n\t"
      "v_mov_b32 v20, v22\n\t"
      "v_exp_f32 v23, v23\n\t"
      "v_fma_f32 v26, v26, %[c2], v24\n\t"
      "v_fma_f32 v27, v27, %[c2], v24\n\t"
      "v_fma_f32 v28, v28, %[c2], v24\n\t"
      "v_fma_f32 v29, v29, %[c2], v24\n\t"
      "v_fma_f32 v30, v30, %[c2], v24\n\t"
      "v_fma_f32 v31, v31, %[c2], v24\n\t"
      "v_fma_f32 v32, v32, %[c2], v24\n\t"
      "v_fma_f32 v33, v33, %[c2], v24\n\t"
      "v_fma_f32 v40, v40, %[c2], v24\n\t"
      "v_fma_f32 v44, v44, %[c2], v24\n\t"
      "v_fma_f32 v48, v48, %[c2], v24\n\t"
      "v_fma_f32 v52, v52, %[c2], v24\n\t"
      "v_fma_f32 v56, v56, %[c2], v24\n\t"
      "v_fma_f32 v60, v60, %[c2], v24\n\t"
      "v_fma_f32 v64, v64, %[c2], v24\n\t"
      "v_fma_f32 v68, v68, %[c2], v24\n\t"
      "v_exp_f32 v26, v26\n\t"
      "v_mul_f32 v12, v23, v12\n\t"
      "v_exp_f32 v27, v27\n\t"
      "v_mul_f32 v13, v23, v13\n\t"
      "v_exp_f32 v28, v28\n\t"
      "v_mul_f32 v14, v23, v14\n\t"
      "v_exp_f32 v29, v29\n\t"
      "v_mul_f32 v15, v23, v15\n\t"
      "v_exp_f32 v30, v30\n\t"
      "v_mul_f32 v16, v23, v16\n\t"
      "v_exp_f32 v31, v31\n\t"
      "v_mul_f32 v17, v23, v17\n\t"
      "v_exp_f32 v32, v32\n\t"
      "v_mul_f32 v18, v23, v18\n\t"
      "v_exp_f32 v33, v33\n\t"
      "v_mul_f32 v19, v23, v19\n\t"
      "v_exp_f32 v40, v40\n\t"
      "v_exp_f32 v44, v44\n\t"
      "v_exp_f32 v48, v48\n\t"
      "v_exp_f32 v52, v52\n\t"
      "v_exp_f32 v56, v56\n\t"
      "v_exp_f32 v60, v60\n\t"
      "v_exp_f32 v64, v64\n\t"
      "v_exp_f32 v68, v68\n\t"
      "v_mul_f32 v21, v21, v23\n\t"
      "v_add_f32 v22, v26, v27\n\t"
      "v_add_f32 v23, v28, v29\n\t"
      "v_add_f32 v22, v22, v30\n\t"
      "v_add_f32 v23, v23, v31\n\t"
      "v_add_f32 v22, v22, v32\n\t"
      "v_add_f32 v23, v23, v33\n\t"
      "v_add_f32 v22, v22, v40\n\t"
      "v_add_f32 v23, v23, v44\n\t"
      "v_add_f32 v22, v22, v48\n\t"
      "v_add_f32 v23, v23, v52\n\t"
      "v_add_f32 v22, v22, v56\n\t"
      "v_add_f32 v23, v23, v60\n\t"
      "v_add_f32 v22, v22, v64\n\t"
      "v_add_f32 v23, v23, v68\n\t"
      "v_add_f32 v21, v21, v22\n\t"
      "v_cvt_pk_bf16_f32 v34, v26, v27\n\t"
      "v_cvt_pk_bf16_f32 v35, v28, v29\n\t"
      "v_cvt_pk_bf16_f32 v36, v30, v31\n\t"
      "v_cvt_pk_bf16_f32 v37, v32, v33\n\t"
      "v_add_f32 v21, v21, v23\n\t"
      "v_cvt_pk_bf16_f32 v38, v40, v44\n\t"
      "v_cvt_pk_bf16_f32 v39, v48, v52\n\t"
      "v_cvt_pk_bf16_f32 v24, v56, v60\n\t"
      "v_cvt_pk_bf16_f32 v25, v64, v68\n\t"
      "s_waitcnt vmcnt(24)\n\t"
      "v_perm_b32 v40, v196, v192, %[slo]\n\t"
      "v_perm_b32 v44, v196, v192, %[shi]\n\t"
      "v_perm_b32 v41, v204, v200, %[slo]\n\t"
      "v_perm_b32 v45, v204, v200, %[shi]\n\t"
      "v_perm_b32 v42, v212, v208, %[slo]\n\t"
      "v_perm_b32 v46, v212, v208, %[shi]\n\t"
      "v_perm_b32 v43, v220, v216, %[slo]\n\t"
      "v_perm_b32 v47, v220, v216, %[shi]\n\t"
      "v_perm_b32 v48, v197, v193, %[slo]\n\t"
      "v_perm_b32 v52, v197, v193, %[shi]\n\t"
      "v_perm_b32 v49, v205, v201, %[slo]\n\t"
      "v_perm_b32 v53, v205, v201, %[shi]\n\t"
      "v_perm_b32 v50, v213, v209, %[slo]\n\t"
      "v_perm_b32 v54, v213, v209, %[shi]\n\t"
      "v_perm_b32 v51, v221, v217, %[slo]\n\t"
      "v_perm_b32 v55, v221, v217, %[shi]\n\t"
      "v_perm_b32 v56, v198, v194, %[slo]\n\t"
      "v_perm_b32 v60, v198, v194, %[shi]\n\t"
      "v_perm_b32 v57, v206, v202, %[slo]\n\t"
      "v_perm_b32 v61, v206, v202, %[shi]\n\t"
      "v_perm_b32 v58, v214, v210, %[slo]\n\t"
      "v_perm_b32 v62, v214, v210, %[shi]\n\t"
      "v_perm_b32 v59, v222, v218, %[slo]\n\t"
      "v_perm_b32 v63, v222, v218, %[shi]\n\t"
      "v_perm_b32 v64, v199, v195, %[slo]\n\t"
      "v_perm_b32 v68, v199, v195, %[shi]\n\t"
      "v_perm_b32 v65, v207, v203, %[slo]\n\t"
      "v_perm_b32 v69, v207, v203, %[shi]\n\t"
      "v_perm_b32 v66, v215, v211, %[slo]\n\t"
      "v_perm_b32 v70, v215, v211, %[shi]\n\t"
      "v_perm_b32 v67, v223, v219, %[slo]\n\t"
      "v_perm_b32 v71, v223, v219, %[shi]\n\t"
      "v_cndmask_b32 v26, 0, v34, %[m0]\n\t"
      "v_cndmask_b32 v27, 0, v35, %[m0]\n\t"
      "v_cndmask_b32 v28, 0, v36, %[m0]\n\t"
      "v_cndmask_b32 v29, 0, v37, %[m0]\n\t"
      "v_cndmask_b32 v30, 0, v34, %[m1]\n\t"
      "v_cndmask_b32 v31, 0, v35, %[m1]\n\t"
      "v_cndmask_b32 v32, 0, v36, %[m1]\n\t"
      "v_cndmask_b32 v33, 0, v37, %[m1]\n\t"
      "v_mfma_f32_16x16x32_bf16 v[12:15], v[40:43], v[26:29], v[12:15]\n\t"
      "v_mfma_f32_16x16x32_bf16 v[16:19], v[56:59], v[26:29], v[16:19]\n\t"
      "v_cndmask_b32 v26, 0, v34, %[m2]\n\t"
      "v_cndmask_b32 v27, 0, v35, %[m2]\n\t"
      "v_cndmask_b32 v28, 0, v36, %[m2]\n\t"
      "v_cndmask_b32 v29, 0, v37, %[m2]\n\t"
      "v_mfma_f32_16x16x32_bf16 v[12:15], v[44:47], v[30:33], v[12:15]\n\t"
      "v_mfma_f32_16x16x32_bf16 v[16:19], v[60:63], v[30:33], v[16:19]\n\t"
      "v_cndmask_b32 v30, 0, v34, %[m3]\n\t"
      "v_cndmask_b32 v31, 0, v35, %[m3]\n\t"
      "v_cndmask_b32 v32, 0, v36, %[m3]\n\t"
      "v_cndmask_b32 v33, 0, v37, %[m3]\n\t"
      "v_mfma_f32_16x16x32_bf16 v[12:15], v[48:51], v[26:29], v[12:15]\n\t"
      "v_mfma_f32_16x16x32_bf16 v[16:19], v[64:67], v[26:29], v[16:19]\n\t"
      "v_mfma_f32_16x16x32_bf16 v[12:15], v[52:55], v[30:33], v[12:15]\n\t"
      "v_mfma_f32_16x16x32_bf16 v[16:19], v[68:71], v[30:33], v[16:19]\n\t"
      "s_waitcnt vmcnt(16)\n\t"
      "v_perm_b32 v40, v132, v128, %[slo]\n\t"
      "v_perm_b32 v44, v132, v128, %[shi]\n\t"
      "v_perm_b32 v41, v140, v136, %[slo]\n\t"
      "v_perm_b32 v45, v140, v136, %[shi]\n\t"
      "v_perm_b32 v42, v148, v144, %[slo]\n\t"
      "v_perm_b32 v46, v148, v144, %[shi]\n\t"
      "v_perm_b32 v43, v156, v152, %[slo]\n\t"
      "v_perm_b32 v47, v156, v152, %[shi]\n\t"
      "v_perm_b32 v48, v133, v129, %[slo]\n\t"
      "v_perm_b32 v52, v133, v129, %[shi]\n\t"
      "v_perm_b32 v49, v141, v137, %[slo]\n\t"
      "v_perm_b32 v53, v141, v137, %[shi]\n\t"
      "v_perm_b32 v50, v149, v145, %[slo]\n\t"
      "v_perm_b32 v54, v149, v145, %[shi]\n\t"
      "v_perm_b32 v51, v157, v153, %[slo]\n\t"
      "v_perm_b32 v55, v157, v153, %[shi]\n\t"
      "v_perm_b32 v56, v134, v130, %[slo]\n\t"
      "v_perm_b32 v60, v134, v130, %[shi]\n\t"
      "v_perm_b32 v57, v142, v138, %[slo]\n\t"
      "v_perm_b32 v61, v142, v138, %[shi]\n\t"
      "v_perm_b32 v58, v150, v146, %[slo]\n\t"
      "v_perm_b32 v62, v150, v146, %[shi]\n\t"
      "v_perm_b32 v59, v158, v154, %[slo]\n\t"
      "v_perm_b32 v63, v158, v154, %[shi]\n\t"
      "v_perm_b32 v64, v135, v131, %[slo]\n\t"
      "v_perm_b32 v68, v135, v131, %[shi]\n\t"
      "v_perm_b32 v65, v143, v139, %[slo]\n\t"
      "v_perm_b32 v69, v143, v139, %[shi]\n\t"
      "v_perm_b32 v66, v151, v147, %[slo]\n\t"
      "v_perm_b32 v70, v151, v147, %[shi]\n\t"
      "v_perm_b32 v67, v159, v155, %[slo]\n\t"
      "v_perm_b32 v71, v159, v155, %[shi]\n\t"
      "v_cndmask_b32 v26, 0, v38, %[m0]\n\t"
      "v_cndmask_b32 v27, 0, v39, %[m0]\n\t"
      "v_cndmask_b32 v28, 0, v24, %[m0]\n\t"
      "v_cndmask_b32 v29, 0, v25, %[m0]\n\t"
      "v_cndmask_b32 v30, 0, v38, %[m1]\n\t"
      "v_cndmask_b32 v31, 0, v39, %[m1]\n\t"
      "v_cndmask_b32 v32, 0, v24, %[m1]\n\t"
      "v_cndmask_b32 v33, 0, v25, %[m1]\n\t"
      "v_mfma_f32_16x16x32_bf16 v[12:15], v[40:43], v[26:29], v[12:15]\n\t"
      "v_mfma_f32_16x16x32_bf16 v[16:19], v[56:59], v[26:29], v[16:19]\n\t"
      "v_cndmask_b32 v26, 0, v38, %[m2]\n\t"
      "v_cndmask_b32 v27, 0, v39, %[m2]\n\t"
      "v_cndmask_b32 v28, 0, v24, %[m2]\n\t"
      "v_cndmask_b32 v29, 0, v25, %[m2]\n\t"
      "v_mfma_f32_16x16x32_bf16 v[12:15], v[44:47], v[30:33], v[12:15]\n\t"
      "v_mfma_f32_16x16x32_bf16 v[16:19], v[60:63], v[30:33], v[16:19]\n\t"
      "v_cndmask_b32 v30, 0, v38, %[m3]\n\t"
      "v_cndmask_b32 v31, 0, v39, %[m3]\n\t"
      "v_cndmask_b32 v32, 0, v24, %[m3]\n\t"
      "v_cndmask_b32 v33, 0, v25, %[m3]\n\t"
      "v_mfma_f32_16x16x32_bf16 v[12:15], v[48:51], v[26:29], v[12:15]\n\t"
      "v_mfma_f32_16x16x32_bf16 v[16:19], v[64:67], v[26:29], v[16:19]\n\t"
      "v_mfma_f32_16x16x32_bf16 v[12:15], v[52:55], v[30:33], v[12:15]\n\t"
      "v_mfma_f32_16x16x32_bf16 v[16:19], v[68:71], v[30:33], v[16:19]\n\t"
      "1:\n\t"
      "s_memrealtime s[96:97]\n\t"
      "s_waitcnt lgkmcnt(0)\n\t"
      "s_sub_u32 %[so0], s96, %[due]\n\t"
      "s_cmp_lt_i32 %[so0], 0\n\t"
      "s_cbranch_scc0 2f\n\t"
      "s_sleep 1\n\t"
      "s_branch 1b\n\t"
      "2:\n\t"
      "s_add_u32 %[due], %[due], %[dl]\n\t"
      "s_add_u32 %[so0], %[rot], 16384\n\t"
      "s_and_b32 %[so0], %[so0], 0x1ffff\n\t"
      "s_add_u32 %[so1], %[so0], 4096\n\t"
      "buffer_load_dwordx4 v[224:227], %[voff], %[rv], %[so0] offen offset:0 nt\n\t"
      "buffer_load_dwordx4 v[228:231], %[voff], %[rv], %[so0] offen offset:1024 nt\n\t"
      "buffer_load_dwordx4 v[232:235], %[voff], %[rv], %[so0] offen offset:2048 nt\n\t"
      "buffer_load_dwordx4 v[236:239], %[voff], %[rv], %[so0] offen offset:3072 nt\n\t"
      "buffer_load_dwordx4 v[240:243], %[voff], %[rv], %[so1] offen offset:0 nt\n\t"
      "buffer_load_dwordx4 v[244:247], %[voff], %[rv], %[so1] offen offset:1024 nt\n\t"
      "buffer_load_dwordx4 v[248:251], %[voff], %[rv], %[so1] offen offset:2048 nt\n\t"
      "buffer_load_dwordx4 v[252:255], %[voff], %[rv], %[so1] offen offset:3072 nt\n\t"
      "s_add_u32 %[so0], %[rot], 24576\n\t"
      "s_and_b32 %[so0], %[so0], 0x1ffff\n\t"
      "s_add_u32 %[so1], %[so0], 4096\n\t"
      "buffer_load_dwordx4 v[160:163], %[voff], %[rv], %[so0] offen offset:0 nt\n\t"
      "buffer_load_dwordx4 v[164:167], %[voff], %[rv], %[so0] offen offset:1024 nt\n\t"
      "buffer_load_dwordx4 v[168:171], %[voff], %[rv], %[so0] offen offset:2048 nt\n\t"
      "buffer_load_dwordx4 v[172:175], %[voff], %[rv], %[so0] offen offset:3072 nt\n\t"
      "buffer_load_dwordx4 v[176:179], %[voff], %[rv], %[so1] offen offset:0 nt\n\t"
      "buffer_load_dwordx4 v[180:183], %[voff], %[rv], %[so1] offen offset:1024 nt\n\t"
      "buffer_load_dwordx4 v[184:187], %[voff], %[rv], %[so1] offen offset:2048 nt\n\t"
      "buffer_load_dwordx4 v[188:191], %[voff], %[rv], %[so1] offen offset:3072 nt\n\t"
      "s_waitcnt vmcnt(16)\n\t"
      "s_waitcnt lgkmcnt(0)\n\t"
      "v_mfma_f32_16x16x32_bf16 v[40:43], a[0:3], v[8:11], 0\n\t"
      "v_mfma_f32_16x16x32_bf16 v[44:47], a[4:7], v[8:11], 0\n\t"
      "v_mfma_f32_16x16x32_bf16 v[48:51], a[8:11], v[8:11], 0\n\t"
      "v_mfma_f32_16x16x32_bf16 v[52:55], a[12:15], v[8:11], 0\n\t"
      "v_mfma_f32_16x16x32_bf16 v[56:59], a[16:19], v[8:11], 0\n\t"
      "v_mfma_f32_16x16x32_bf16 v[60:63], a[20:23], v[8:11], 0\n\t"
      "v_mfma_f32_16x16x32_bf16 v[64:67], a[24:27], v[8:11], 0\n\t"
      "v_mfma_f32_16x16x32_bf16 v[68:71], a[28:31], v[8:11], 0\n\t"
      "s_nop 1\n\t"
      "v_mov_b32_dpp v26, v40 quad_perm:[0,0,0,0] row_mask:0xf bank_mask:0xf\n\t"
      "v_mov_b32_dpp v27, v44 quad_perm:[0,0,0,0] row_mask:0xf bank_mask:0xf\n\t"
      "v_mov_b32_dpp v28, v48 quad_perm:[0,0,0,0] row_mask:0xf bank_mask:0xf\n\t"
      "v_mov_b32_dpp v29, v52 quad_perm:[0,0,0,0] row_mask:0xf bank_mask:0xf\n\t"
      "v_add_f32_dpp v26, v41, v26 quad_perm:[1,1,1,1] row_mask:0xf bank_mask:0xf\n\t"
      "v_add_f32_dpp v27, v45, v27 quad_perm:[1,1,1,1] row_mask:0xf bank_mask:0xf\n\t"
      "v_add_f32_dpp v28, v49, v28 quad_perm:[1,1,1,1] row_mask:0xf bank_mask:0xf\n\t"
      "v_add_f32_dpp v29, v53, v29 quad_perm:[1,1,1,1] row_mask:0xf bank_mask:0xf\n\t"
      "v_add_f32_dpp v26, v42, v26 quad_perm:[2,2,2,2] row_mask:0xf bank_mask:0xf\n\t"
      "v_add_f32_dpp v27, v46, v27 quad_perm:[2,2,2,2] row_mask:0xf bank_mask:0xf\n\t"
      "v_add_f32_dpp v28, v50, v28 quad_perm:[2,2,2,2] row_mask:0xf bank_mask:0xf\n\t"
      "v_add_f32_dpp v29, v54, v29 quad_perm:[2,2,2,2] row_mask:0xf bank_mask:0xf\n\t"
      "v_add_f32_dpp v26, v43, v26 quad_perm:[3,3,3,3] row_mask:0xf bank_mask:0xf\n\t"
      "v_add_f32_dpp v27, v47, v27 quad_perm:[3,3,3,3] row_mask:0xf bank_mask:0xf\n\t"
      "v_add_f32_dpp v28, v51, v28 quad_perm:[3,3,3,3] row_mask:0xf bank_mask:0xf\n\t"
      "v_add_f32_dpp v29, v55, v29 quad_perm:[3,3,3,3] row_mask:0xf bank_mask:0xf\n\t"
      "v_mov_b32_dpp v30, v56 quad_perm:[0,0,0,0] row_mask:0xf bank_mask:0xf\n\t"
      "v_mov_b32_dpp v31, v60 quad_perm:[0,0,0,0] row_mask:0xf bank_mask:0xf\n\t"
      "v_mov_b32_dpp v32, v64 quad_perm:[0,0,0,0] row_mask:0xf bank_mask:0xf\n\t"
      "v_mov_b32_dpp v33, v68 quad_perm:[0,0,0,0] row_mask:0xf bank_mask:0xf\n\t"
      "v_add_f32_dpp v30, v57, v30 quad_perm:[1,1,1,1] row_mask:0xf bank_mask:0xf\n\t"
      "v_add_f32_dpp v31, v61, v31 quad_perm:[1,1,1,1] row_mask:0xf bank_mask:0xf\n\t"
      "v_add_f32_dpp v32, v65, v32 quad_perm:[1,1,1,1] row_mask:0xf bank_mask:0xf\n\t"
      "v_add_f32_dpp v33, v69, v33 quad_perm:[1,1,1,1] row_mask:0xf bank_mask:0xf\n\t"
      "v_add_f32_dpp v30, v58, v30 quad_perm:[2,2,2,2] row_mask:0xf bank_mask:0xf\n\t"
      "v_add_f32_dpp v31, v62, v31 quad_perm:[2,2,2,2] row_mask:0xf bank_mask:0xf\n\t"
      "v_add_f32_dpp v32, v66, v32 quad_perm:[2,2,2,2] row_mask:0xf bank_mask:0xf\n\t"
      "v_add_f32_dpp v33, v70, v33 quad_perm:[2,2,2,2] row_mask:0xf bank_mask:0xf\n\t"
      "v_add_f32_dpp v30, v59, v30 quad_perm:[3,3,3,3] row_mask:0xf bank_mask:0xf\n\t"
      "v_add_f32_dpp v31, v63, v31 quad_perm:[3,3,3,3] row_mask:0xf bank_mask:0xf\n\t"
      "v_add_f32_dpp v32, v67, v32 quad_perm:[3,3,3,3] row_mask:0xf bank_mask:0xf\n\t"
      "v_add_f32_dpp v33, v71, v33 quad_perm:[3,3,3,3] row_mask:0xf bank_mask:0xf\n\t"
      "v_mfma_f32_16x16x32_bf16 v[40:43], a[32:35], v[8:11], 0\n\t"
      "v_mfma_f32_16x16x32_bf16 v[44:47], a[36:39], v[8:11], 0\n\t"
      "v_mfma_f32_16x16x32_bf16 v[48:51], a[40:43], v[8:11], 0\n\t"
      "v_mfma_f32_16x16x32_bf16 v[52:55], a[44:47], v[8:11], 0\n\t"
      "v_mfma_f32_16x16x32_bf16 v[56:59], a[48:51], v[8:11], 0\n\t"
      "v_mfma_f32_16x16x32_bf16 v[60:63], a[52:55], v[8:11], 0\n\t"
      "v_mfma_f32_16x16x32_bf16 v[64:67], a[56:59], v[8:11], 0\n\t"
      "v_mfma_f32_16x16x32_bf16 v[68:71], a[60:63], v[8:11], 0\n\t"
      "s_nop 1\n\t"
      "v_mov_b32_dpp v40, v40 quad_perm:[0,0,0,0] row_mask:0xf bank_mask:0xf\n\t"
      "v_mov_b32_dpp v44, v44 quad_perm:[0,0,0,0] row_mask:0xf bank_mask:0xf\n\t"
      "v_mov_b32_dpp v48, v48 quad_perm:[0,0,0,0] row_mask:0xf bank_mask:0xf\n\t"
      "v_mov_b32_dpp v52, v52 quad_perm:[0,0,0,0] row_mask:0xf bank_mask:0xf\n\t"
      "v_add_f32_dpp v40, v41, v40 quad_perm:[1,1,1,1] row_mask:0xf bank_mask:0xf\n\t"
      "v_add_f32_dpp v44, v45, v44 quad_perm:[1,1,1,1] row_mask:0xf bank_mask:0xf\n\t"
      "v_add_f32_dpp v48, v49, v48 quad_perm:[1,1,1,1] row_mask:0xf bank_mask:0xf\n\t"
      "v_add_f32_dpp v52, v53, v52 quad_perm:[1,1,1,1] row_mask:0xf bank_mask:0xf\n\t"
      "v_add_f32_dpp v40, v42, v40 quad_perm:[2,2,2,2] row_mask:0xf bank_mask:0xf\n\t"
      "v_add_f32_dpp v44, v46, v44 quad_perm:[2,2,2,2] row_mask:0xf bank_mask:0xf\n\t"
      "v_add_f32_dpp v48, v50, v48 quad_perm:[2,2,2,2] row_mask:0xf bank_mask:0xf\n\t"
      "v_add_f32_dpp v52, v54, v52 quad_perm:[2,2,2,2] row_mask:0xf bank_mask:0xf\n\t"
      "v_add_f32_dpp v40, v43, v40 quad_perm:[3,3,3,3] row_mask:0xf bank_mask:0xf\n\t"
      "v_add_f32_dpp v44, v47, v44 quad_perm:[3,3,3,3] row_mask:0xf bank_mask:0xf\n\t"
      "v_add_f32_dpp v48, v51, v48 quad_perm:[3,3,3,3] row_mask:0xf bank_mask:0xf\n\t"
      "v_add_f32_dpp v52, v55, v52 quad_perm:[3,3,3,3] row_mask:0xf bank_mask:0xf\n\t"
      "v_mov_b32_dpp v56, v56 quad_perm:[0,0,0,0] row_mask:0xf bank_mask:0xf\n\t"
      "v_mov_b32_dpp v60, v60 quad_perm:[0,0,0,0] row_mask:0xf bank_mask:0xf\n\t"
      "v_mov_b32_dpp v64, v64 quad_perm:[0,0,0,0] row_mask:0xf bank_mask:0xf\n\t"
      "v_mov_b32_dpp v68, v68 quad_perm:[0,0,0,0] row_mask:0xf bank_mask:0xf\n\t"
      "v_add_f32_dpp v56, v57, v56 quad_perm:[1,1,1,1] row_mask:0xf bank_mask:0xf\n\t"
      "v_add_f32_dpp v60, v61, v60 quad_perm:[1,1,1,1] row_mask:0xf bank_mask:0xf\n\t"
      "v_add_f32_dpp v64, v65, v64 quad_perm:[1,1,1,1] row_mask:0xf bank_mask:0xf\n\t"
      "v_add_f32_dpp v68, v69, v68 quad_perm:[1,1,1,1] row_mask:0xf bank_mask:0xf\n\t"
      "v_add_f32_dpp v56, v58, v56 quad_perm:[2,2,2,2] row_mask:0xf bank_mask:0xf\n\t"
      "v_add_f32_dpp v60, v62, v60 quad_perm:[2,2,2,2] row_mask:0xf bank_mask:0xf\n\t"
      "v_add_f32_dpp v64, v66, v64 quad_perm:[2,2,2,2] row_mask:0xf bank_mask:0xf\n\t"
      "v_add_f32_dpp v68, v70, v68 quad_perm:[2,2,2,2] row_mask:0xf bank_mask:0xf\n\t"
      "v_add_f32_dpp v56, v59, v56 quad_perm:[3,3,3,3] row_mask:0xf bank_mask:0xf\n\t"
      "v_add_f32_dpp v60, v63, v60 quad_perm:[3,3,3,3] row_mask:0xf bank_mask:0xf\n\t"
      "v_add_f32_dpp v64, v67, v64 quad_perm:[3,3,3,3] row_mask:0xf bank_mask:0xf\n\t"
      "v_add_f32_dpp v68, v71, v68 quad_perm:[3,3,3,3] row_mask:0xf bank_mask:0xf\n\t"
      "v_max3_f32 v22, v26, v27, v20\n\t"
      "v_max3_f32 v23, v28, v29, v30\n\t"
      "v_max3_f32 v22, v22, v31, v32\n\t"
      "v_max3_f32 v23, v23, v33, v40\n\t"
      "v_max3_f32 v22, v22, v44, v48\n\t"
      "v_max3_f32 v23, v23, v52, v56\n\t"
      "v_max3_f32 v22, v22, v60, v64\n\t"
      "v_max3_f32 v22, v22, v23, v68\n\t"
      "v_mov_b32 v23, v22\n\t"
      "s_nop 1\n\t"
      "v_permlane16_swap_b32 v22, v23\n\t"
      "s_nop 0\n\t"
      "v_max_f32 v22, v22, v23\n\t"
      "v_mov_b32 v23, v22\n\t"
      "s_nop 1\n\t"
      "v_permlane32_swap_b32 v22, v23\n\t"
      "s_nop 0\n\t"
      "v_max_f32 v22, v22, v23\n\t"
      "v_sub_f32 v23, v20, v22\n\t"
      "v_mul_f32 v24, %[nc2], v22\n\t"
      "v_mul_f32 v23, %[c2], v23\n\t"
      "v_mov_b32 v20, v22\n\t"
      "v_exp_f32 v23, v23\n\t"
      "v_fma_f32 v26, v26, %[c2], v24\n\t"
      "v_fma_f32 v27, v27, %[c2], v24\n\t"
      "v_fma_f32 v28, v28, %[c2], v24\n\t"
      "v_fma_f32 v29, v29, %[c2], v24\n\t"
      "v_fma_f32 v30, v30, %[c2], v24\n\t"
      "v_fma_f32 v31, v31, %[c2], v24\n\t"
      "v_fma_f32 v32, v32, %[c2], v24\n\t"
      "v_fma_f32 v33, v33, %[c2], v24\n\t"
      "v_fma_f32 v40, v40, %[c2], v24\n\t"
      "v_fma_f32 v44, v44, %[c2], v24\n\t"
      "v_fma_f32 v48, v48, %[c2], v24\n\t"
      "v_fma_f32 v52, v52, %[c2], v24\n\t"
      "v_fma_f32 v56, v56, %[c2], v24\n\t"
      "v_fma_f32 v60, v60, %[c2], v24\n\t"
      "v_fma_f32 v64, v64, %[c2], v24\n\t"
      "v_fma_f32 v68, v68, %[c2], v24\n\t"
      "v_exp_f32 v26, v26\n\t"
      "v_mul_f32 v12, v23, v12\n\t"
      "v_exp_f32 v27, v27\n\t"
      "v_mul_f32 v13, v23, v13\n\t"
      "v_exp_f32 v28, v28\n\t"
      "v_mul_f32 v14, v23, v14\n\t"
      "v_exp_f32 v29, v29\n\t"
      "v_mul_f32 v15, v23, v15\n\t"
      "v_exp_f32 v30, v30\n\t"
      "v_mul_f32 v16, v23, v16\n\t"
      "v_exp_f32 v31, v31\n\t"
      "v_mul_f32 v17, v23, v17\n\t"
      "v_exp_f32 v32, v32\n\t"
      "v_mul_f32 v18, v23, v18\n\t"
      "v_exp_f32 v33, v33\n\t"
      "v_mul_f32 v19, v23, v19\n\t"
      "v_exp_f32 v40, v40\n\t"
      "v_exp_f32 v44, v44\n\t"
      "v_exp_f32 v48, v48\n\t"
      "v_exp_f32 v52, v52\n\t"
      "v_exp_f32 v56, v56\n\t"
      "v_exp_f32 v60, v60\n\t"
      "v_exp_f32 v64, v64\n\t"
      "v_exp_f32 v68, v68\n\t"
      "v_mul_f32 v21, v21, v23\n\t"
      "v_add_f32 v22, v26, v27\n\t"
      "v_add_f32 v23, v28, v29\n\t"
      "v_add_f32 v22, v22, v30\n\t"
      "v_add_f32 v23, v23, v31\n\t"
      "v_add_f32 v22, v22, v32\n\t"
      "v_add_f32 v23, v23, v33\n\t"
      "v_add_f32 v22, v22, v40\n\t"
      "v_add_f32 v23, v23, v44\n\t"
      "v_add_f32 v22, v22, v48\n\t"
      "v_add_f32 v23, v23, v52\n\t"
      "v_add_f32 v22, v22, v56\n\t"
      "v_add_f32 v23, v23, v60\n\t"
      "v_add_f32 v22, v22, v64\n\t"
      "v_add_f32 v23, v23, v68\n\t"
      "v_add_f32 v21, v21, v22\n\t"
      "v_cvt_pk_bf16_f32 v34, v26, v27\n\t"
      "v_cvt_pk_bf16_f32 v35, v28, v29\n\t"
      "v_cvt_pk_bf16_f32 v36, v30, v31\n\t"
      "v_cvt_pk_bf16_f32 v37, v32, v33\n\t"
      "v_add_f32 v21, v21, v23\n\t"
      "v_cvt_pk_bf16_f32 v38, v40, v44\n\t"
      "v_cvt_pk_bf16_f32 v39, v48, v52\n\t"
      "v_cvt_pk_bf16_f32 v24, v56, v60\n\t"
      "v_cvt_pk_bf16_f32 v25, v64, v68\n\t"
      "1:\n\t"
      "s_memrealtime s[96:97]\n\t"
      "s_waitcnt lgkmcnt(0)\n\t"
      "s_sub_u32 %[so0], s96, %[due]\n\t"
      "s_cmp_lt_i32 %[so0], 0\n\t"
      "s_cbranch_scc0 2f\n\t"
      "s_sleep 1\n\t"
      "s_branch 1b\n\t"
      "2:\n\t"
      "s_add_u32 %[due], %[due], %[dl]\n\t"
      "s_add_u32 %[so0], %[rot], 32768\n\t"
      "s_and_b32 %[so0], %[so0], 0x1ffff\n\t"
      "s_add_u32 %[so1], %[so0], 4096\n\t"
      "buffer_load_dwordx4 a[192:195], %[koff], %[rk], %[so0] offen offset:0 nt\n\t"
      "buffer_load_dwordx4 a[196:199], %[koff], %[rk], %[so0] offen offset:1024 nt\n\t"
      "buffer_load_dwordx4 a[200:203], %[koff], %[rk], %[so0] offen offset:2048 nt\n\t"
      "buffer_load_dwordx4 a[204:207], %[koff], %[rk], %[so0] offen offset:3072 nt\n\t"
      "buffer_load_dwordx4 a[208:211], %[koff], %[rk], %[so1] offen offset:0 nt\n\t"
      "buffer_load_dwordx4 a[212:215], %[koff], %[rk], %[so1] offen offset:1024 nt\n\t"
      "buffer_load_dwordx4 a[216:219], %[koff], %[rk], %[so1] offen offset:2048 nt\n\t"
      "buffer_load_dwordx4 a[220:223], %[koff], %[rk], %[so1] offen offset:3072 nt\n\t"
      "s_add_u32 %[so0], %[rot], 40960\n\t"
      "s_and_b32 %[so0], %[so0], 0x1ffff\n\t"
      "s_add_u32 %[so1], %[so0], 4096\n\t"
      "buffer_load_dwordx4 a[224:227], %[koff], %[rk], %[so0] offen offset:0 nt\n\t"
      "buffer_load_dwordx4 a[228:231], %[koff], %[rk], %[so0] offen offset:1024 nt\n\t"
      "buffer_load_dwordx4 a[232:235], %[koff], %[rk], %[so0] offen offset:2048 nt\n\t"
      "buffer_load_dwordx4 a[236:239], %[koff], %[rk], %[so0] offen offset:3072 nt\n\t"
      "buffer_load_dwordx4 a[240:243], %[koff], %[rk], %[so1] offen offset:0 nt\n\t"
      "buffer_load_dwordx4 a[244:247], %[koff], %[rk], %[so1] offen offset:1024 nt\n\t"
      "buffer_load_dwordx4 a[248:251], %[koff], %[rk], %[so1] offen offset:2048 nt\n\t"
      "buffer_load_dwordx4 a[252:255], %[koff], %[rk], %[so1] offen offset:3072 nt\n\t"
      "s_waitcnt vmcnt(24)\n\t"
      "v_perm_b32 v40, v228, v224, %[slo]\n\t"
      "v_perm_b32 v44, v228, v224, %[shi]\n\t"
      "v_perm_b32 v41, v236, v232, %[slo]\n\t"
      "v_perm_b32 v45, v236, v232, %[shi]\n\t"
      "v_perm_b32 v42, v244, v240, %[slo]\n\t"
      "v_perm_b32 v46, v244, v240, %[shi]\n\t"
      "v_perm_b32 v43, v252, v248, %[slo]\n\t"
      "v_perm_b32 v47, v252, v248, %[shi]\n\t"
      "v_perm_b32 v48, v229, v225, %[slo]\n\t"
      "v_perm_b32 v52, v229, v225, %[shi]\n\t"
      "v_perm_b32 v49, v237, v233, %[slo]\n\t"
      "v_perm_b32 v53, v237, v233, %[shi]\n\t"
      "v_perm_b32 v50, v245, v241, %[slo]\n\t"
      "v_perm_b32 v54, v245, v241, %[shi]\n\t"
      "v_perm_b32 v51, v253, v249, %[slo]\n\t"
      "v_perm_b32 v55, v253, v249, %[shi]\n\t"
      "v_perm_b32 v56, v230, v226, %[slo]\n\t"
      "v_perm_b32 v60, v230, v226, %[shi]\n\t"
      "v_perm_b32 v57, v238, v234, %[slo]\n\t"
      "v_perm_b32 v61, v238, v234, %[shi]\n\t"
      "v_perm_b32 v58, v246, v242, %[slo]\n\t"
      "v_perm_b32 v62, v246, v242, %[shi]\n\t"
      "v_perm_b32 v59, v254, v250, %[slo]\n\t"
      "v_perm_b32 v63, v254, v250, %[shi]\n\t"
      "v_perm_b32 v64, v231, v227, %[slo]\n\t"
      "v_perm_b32 v68, v231, v227, %[shi]\n\t"
      "v_perm_b32 v65, v239, v235, %[slo]\n\t"
      "v_perm_b32 v69, v239, v235, %[shi]\n\t"
      "v_perm_b32 v66, v247, v243, %[slo]\n\t"
      "v_perm_b32 v70, v247, v243, %[shi]\n\t"
      "v_perm_b32 v67, v255, v251, %[slo]\n\t"
      "v_perm_b32 v71, v255, v251, %[shi]\n\t"
      "v_cndmask_b32 v26, 0, v34, %[m0]\n\t"
      "v_cndmask_b32 v27, 0, v35, %[m0]\n\t"
      "v_cndmask_b32 v28, 0, v36, %[m0]\n\t"
      "v_cndmask_b32 v29, 0, v37, %[m0]\n\t"
      "v_cndmask_b32 v30, 0, v34, %[m1]\n\t"
      "v_cndmask_b32 v31, 0, v35, %[m1]\n\t"
      "v_cndmask_b32 v32, 0, v36, %[m1]\n\t"
      "v_cndmask_b32 v33, 0, v37, %[m1]\n\t"
      "v_mfma_f32_16x16x32_bf16 v[12:15], v[40:43], v[26:29], v[12:15]\n\t"
      "v_mfma_f32_16x16x32_bf16 v[16:19], v[56:59], v[26:29], v[16:19]\n\t"
      "v_cndmask_b32 v26, 0, v34, %[m2]\n\t"
      "v_cndmask_b32 v27, 0, v35, %[m2]\n\t"
      "v_cndmask_b32 v28, 0, v36, %[m2]\n\t"
      "v_cndmask_b32 v29, 0, v37, %[m2]\n\t"
      "v_mfma_f32_16x16x32_bf16 v[12:15], v[44:47], v[30:33], v[12:15]\n\t"
      "v_mfma_f32_16x16x32_bf16 v[16:19], v[60:63], v[30:33], v[16:19]\n\t"
      "v_cndmask_b32 v30, 0, v34, %[m3]\n\t"
      "v_cndmask_b32 v31, 0, v35, %[m3]\n\t"
      "v_cndmask_b32 v32, 0, v36, %[m3]\n\t"
      "v_cndmask_b32 v33, 0, v37, %[m3]\n\t"
      "v_mfma_f32_16x16x32_bf16 v[12:15], v[48:51], v[26:29], v[12:15]\n\t"
      "v_mfma_f32_16x16x32_bf16 v[16:19], v[64:67], v[26:29], v[16:19]\n\t"
      "v_mfma_f32_16x16x32_bf16 v[12:15], v[52:55], v[30:33], v[12:15]\n\t"
      "v_mfma_f32_16x16x32_bf16 v[16:19], v[68:71], v[30:33], v[16:19]\n\t"
      "s_waitcnt vmcnt(16)\n\t"
      "v_perm_b32 v40, v164, v160, %[slo]\n\t"
      "v_perm_b32 v44, v164, v160, %[shi]\n\t"
      "v_perm_b32 v41, v172, v168, %[slo]\n\t"
      "v_perm_b32 v45, v172, v168, %[shi]\n\t"
      "v_perm_b32 v42, v180, v176, %[slo]\n\t"
      "v_perm_b32 v46, v180, v176, %[shi]\n\t"
      "v_perm_b32 v43, v188, v184, %[slo]\n\t"
      "v_perm_b32 v47, v188, v184, %[shi]\n\t"
      "v_perm_b32 v48, v165, v161, %[slo]\n\t"
      "v_perm_b32 v52, v165, v161, %[shi]\n\t"
      "v_perm_b32 v49, v173, v169, %[slo]\n\t"
      "v_perm_b32 v53, v173, v169, %[shi]\n\t"
      "v_perm_b32 v50, v181, v177, %[slo]\n\t"
      "v_perm_b32 v54, v181, v177, %[shi]\n\t"
      "v_perm_b32 v51, v189, v185, %[slo]\n\t"
      "v_perm_b32 v55, v189, v185, %[shi]\n\t"
      "v_perm_b32 v56, v166, v162, %[slo]\n\t"
      "v_perm_b32 v60, v166, v162, %[shi]\n\t"
      "v_perm_b32 v57, v174, v170, %[slo]\n\t"
      "v_perm_b32 v61, v174, v170, %[shi]\n\t"
      "v_perm_b32 v58, v182, v178, %[slo]\n\t"
      "v_perm_b32 v62, v182, v178, %[shi]\n\t"
      "v_perm_b32 v59, v190, v186, %[slo]\n\t"
      "v_perm_b32 v63, v190, v186, %[shi]\n\t"
      "v_perm_b32 v64, v167, v163, %[slo]\n\t"
      "v_perm_b32 v68, v167, v163, %[shi]\n\t"
      "v_perm_b32 v65, v175, v171, %[slo]\n\t"
      "v_perm_b32 v69, v175, v171, %[shi]\n\t"
      "v_perm_b32 v66, v183, v179, %[slo]\n\t"
      "v_perm_b32 v70, v183, v179, %[shi]\n\t"
      "v_perm_b32 v67, v191, v187, %[slo]\n\t"
      "v_perm_b32 v71, v191, v187, %[shi]\n\t"
      "v_cndmask_b32 v26, 0, v38, %[m0]\n\t"
      "v_cndmask_b32 v27, 0, v39, %[m0]\n\t"
      "v_cndmask_b32 v28, 0, v24, %[m0]\n\t"
      "v_cndmask_b32 v29, 0, v25, %[m0]\n\t"
      "v_cndmask_b32 v30, 0, v38, %[m1]\n\t"
      "v_cndmask_b32 v31, 0, v39, %[m1]\n\t"
      "v_cndmask_b32 v32, 0, v24, %[m1]\n\t"
      "v_cndmask_b32 v33, 0, v25, %[m1]\n\t"
      "v_mfma_f32_16x16x32_bf16 v[12:15], v[40:43], v[26:29], v[12:15]\n\t"
      "v_mfma_f32_16x16x32_bf16 v[16:19], v[56:59], v[26:29], v[16:19]\n\t"
      "v_cndmask_b32 v26, 0, v38, %[m2]\n\t"
      "v_cndmask_b32 v27, 0, v39, %[m2]\n\t"
      "v_cndmask_b32 v28, 0, v24, %[m2]\n\t"
      "v_cndmask_b32 v29, 0, v25, %[m2]\n\t"
      "v_mfma_f32_16x16x32_bf16 v[12:15], v[44:47], v[30:33], v[12:15]\n\t"
      "v_mfma_f32_16x16x32_bf16 v[16:19], v[60:63], v[30:33], v[16:19]\n\t"
      "v_cndmask_b32 v30, 0, v38, %[m3]\n\t"
      "v_cndmask_b32 v31, 0, v39, %[m3]\n\t"
      "v_cndmask_b32 v32, 0, v24, %[m3]\n\t"
      "v_cndmask_b32 v33, 0, v25, %[m3]\n\t"
      "v_mfma_f32_16x16x32_bf16 v[12:15], v[48:51], v[26:29], v[12:15]\n\t"
      "v_mfma_f32_16x16x32_bf16 v[16:19], v[64:67], v[26:29], v[16:19]\n\t"
      "v_mfma_f32_16x16x32_bf16 v[12:15], v[52:55], v[30:33], v[12:15]\n\t"
      "v_mfma_f32_16x16x32_bf16 v[16:19], v[68:71], v[30:33], v[16:19]\n\t"
      "1:\n\t"
      "s_memrealtime s[96:97]\n\t"
      "s_waitcnt lgkmcnt(0)\n\t"
      "s_sub_u32 %[so0], s96, %[due]\n\t"
      "s_cmp_lt_i32 %[so0], 0\n\t"
      "s_cbranch_scc0 2f\n\t"
      "s_sleep 1\n\t"
      "s_branch 1b\n\t"
      "2:\n\t"
      "s_add_u32 %[due], %[due], %[dl]\n\t"
      "s_add_u32 %[so0], %[rot], 32768\n\t"
      "s_and_b32 %[so0], %[so0], 0x1ffff\n\t"
      "s_add_u32 %[so1], %[so0], 4096\n\t"
      "buffer_load_dwordx4 v[192:195], %[voff], %[rv], %[so0] offen offset:0 nt\n\t"
      "buffer_load_dwordx4 v[196:199], %[voff], %[rv], %[so0] offen offset:1024 nt\n\t"
      "buffer_load_dwordx4 v[200:203], %[voff], %[rv], %[so0] offen offset:2048 nt\n\t"
      "buffer_load_dwordx4 v[204:207], %[voff], %[rv], %[so0] offen offset:3072 nt\n\t"
      "buffer_load_dwordx4 v[208:211], %[voff], %[rv], %[so1] offen offset:0 nt\n\t"
      "buffer_load_dwordx4 v[212:215], %[voff], %[rv], %[so1] offen offset:1024 nt\n\t"
      "buffer_load_dwordx4 v[216:219], %[voff], %[rv], %[so1] offen offset:2048 nt\n\t"
      "buffer_load_dwordx4 v[220:223], %[voff], %[rv], %[so1] offen offset:3072 nt\n\t"
      "s_add_u32 %[so0], %[rot], 40960\n\t"
      "s_and_b32 %[so0], %[so0], 0x1ffff\n\t"
      "s_add_u32 %[so1], %[so0], 4096\n\t"
      "buffer_load_dwordx4 v[128:131], %[voff], %[rv], %[so0] offen offset:0 nt\n\t"
      "buffer_load_dwordx4 v[132:135], %[voff], %[rv], %[so0] offen offset:1024 nt\n\t"
      "buffer_load_dwordx4 v[136:139], %[voff], %[rv], %[so0] offen offset:2048 nt\n\t"
      "buffer_load_dwordx4 v[140:143], %[voff], %[rv], %[so0] offen offset:3072 nt\n\t"
      "buffer_load_dwordx4 v[144:147], %[voff], %[rv], %[so1] offen offset:0 nt\n\t"
      "buffer_load_dwordx4 v[148:151], %[voff], %[rv], %[so1] offen offset:1024 nt\n\t"
      "buffer_load_dwordx4 v[152:155], %[voff], %[rv], %[so1] offen offset:2048 nt\n\t"
      "buffer_load_dwordx4 v[156:159], %[voff], %[rv], %[so1] offen offset:3072 nt\n\t"
      "s_waitcnt vmcnt(16)\n\t"
      "s_waitcnt lgkmcnt(0)\n\t"
      "v_mfma_f32_16x16x32_bf16 v[40:43], a[192:195], v[8:11], 0\n\t"
      "v_mfma_f32_16x16x32_bf16 v[44:47], a[196:199], v[8:11], 0\n\t"
      "v_mfma_f32_16x16x32_bf16 v[48:51], a[200:203], v[8:11], 0\n\t"
      "v_mfma_f32_16x16x32_bf16 v[52:55], a[204:207], v[8:11], 0\n\t"
      "v_mfma_f32_16x16x32_bf16 v[56:59], a[208:211], v[8:11], 0\n\t"
      "v_mfma_f32_16x16x32_bf16 v[60:63], a[212:215], v[8:11], 0\n\t"
      "v_mfma_f32_16x16x32_bf16 v[64:67], a[216:219], v[8:11], 0\n\t"
      "v_mfma_f32_16x16x32_bf16 v[68:71], a[220:223], v[8:11], 0\n\t"
      "s_nop 1\n\t"
      "v_mov_b32_dpp v26, v40 quad_perm:[0,0,0,0] row_mask:0xf bank_mask:0xf\n\t"
      "v_mov_b32_dpp v27, v44 quad_perm:[0,0,0,0] row_mask:0xf bank_mask:0xf\n\t"
      "v_mov_b32_dpp v28, v48 quad_perm:[0,0,0,0] row_mask:0xf bank_mask:0xf\n\t"
      "v_mov_b32_dpp v29, v52 quad_perm:[0,0,0,0] row_mask:0xf bank_mask:0xf\n\t"
      "v_add_f32_dpp v26, v41, v26 quad_perm:[1,1,1,1] row_mask:0xf bank_mask:0xf\n\t"
      "v_add_f32_dpp v27, v45, v27 quad_perm:[1,1,1,1] row_mask:0xf bank_mask:0xf\n\t"
      "v_add_f32_dpp v28, v49, v28 quad_perm:[1,1,1,1] row_mask:0xf bank_mask:0xf\n\t"
      "v_add_f32_dpp v29, v53, v29 quad_perm:[1,1,1,1] row_mask:0xf bank_mask:0xf\n\t"
      "v_add_f32_dpp v26, v42, v26 quad_perm:[2,2,2,2] row_mask:0xf bank_mask:0xf\n\t"
      "v_add_f32_dpp v27, v46, v27 quad_perm:[2,2,2,2] row_mask:0xf bank_mask:0xf\n\t"
      "v_add_f32_dpp v28, v50, v28 quad_perm:[2,2,2,2] row_mask:0xf bank_mask:0xf\n\t"
      "v_add_f32_dpp v29, v54, v29 quad_perm:[2,2,2,2] row_mask:0xf bank_mask:0xf\n\t"
      "v_add_f32_dpp v26, v43, v26 quad_perm:[3,3,3,3] row_mask:0xf bank_mask:0xf\n\t"
      "v_add_f32_dpp v27, v47, v27 quad_perm:[3,3,3,3] row_mask:0xf bank_mask:0xf\n\t"
      "v_add_f32_dpp v28, v51, v28 quad_perm:[3,3,3,3] row_mask:0xf bank_mask:0xf\n\t"
      "v_add_f32_dpp v29, v55, v29 quad_perm:[3,3,3,3] row_mask:0xf bank_mask:0xf\n\t"
      "v_mov_b32_dpp v30, v56 quad_perm:[0,0,0,0] row_mask:0xf bank_mask:0xf\n\t"
      "v_mov_b32_dpp v31, v60 quad_perm:[0,0,0,0] row_mask:0xf bank_mask:0xf\n\t"
      "v_mov_b32_dpp v32, v64 quad_perm:[0,0,0,0] row_mask:0xf bank_mask:0xf\n\t"
      "v_mov_b32_dpp v33, v68 quad_perm:[0,0,0,0] row_mask:0xf bank_mask:0xf\n\t"
      "v_add_f32_dpp v30, v57, v30 quad_perm:[1,1,1,1] row_mask:0xf bank_mask:0xf\n\t"
      "v_add_f32_dpp v31, v61, v31 quad_perm:[1,1,1,1] row_mask:0xf bank_mask:0xf\n\t"
      "v_add_f32_dpp v32, v65, v32 quad_perm:[1,1,1,1] row_mask:0xf bank_mask:0xf\n\t"
      "v_add_f32_dpp v33, v69, v33 quad_perm:[1,1,1,1] row_mask:0xf bank_mask:0xf\n\t"
      "v_add_f32_dpp v30, v58, v30 quad_perm:[2,2,2,2] row_mask:0xf bank_mask:0xf\n\t"
      "v_add_f32_dpp v31, v62, v31 quad_perm:[2,2,2,2] row_mask:0xf bank_mask:0xf\n\t"
      "v_add_f32_dpp v32, v66, v32 quad_perm:[2,2,2,2] row_mask:0xf bank_mask:0xf\n\t"
      "v_add_f32_dpp v33, v70, v33 quad_perm:[2,2,2,2] row_mask:0xf bank_mask:0xf\n\t"
      "v_add_f32_dpp v30, v59, v30 quad_perm:[3,3,3,3] row_mask:0xf bank_mask:0xf\n\t"
      "v_add_f32_dpp v31, v63, v31 quad_perm:[3,3,3,3] row_mask:0xf bank_mask:0xf\n\t"
      "v_add_f32_dpp v32, v67, v32 quad_perm:[3,3,3,3] row_mask:0xf bank_mask:0xf\n\t"
      "v_add_f32_dpp v33, v71, v33 quad_perm:[3,3,3,3] row_mask:0xf bank_mask:0xf\n\t"
      "v_mfma_f32_16x16x32_bf16 v[40:43], a[224:227], v[8:11], 0\n\t"
      "v_mfma_f32_16x16x32_bf16 v[44:47], a[228:231], v[8:11], 0\n\t"
      "v_mfma_f32_16x16x32_bf16 v[48:51], a[232:235], v[8:11], 0\n\t"
      "v_mfma_f32_16x16x32_bf16 v[52:55], a[236:239], v[8:11], 0\n\t"
      "v_mfma_f32_16x16x32_bf16 v[56:59], a[240:243], v[8:11], 0\n\t"
      "v_mfma_f32_16x16x32_bf16 v[60:63], a[244:247], v[8:11], 0\n\t"
      "v_mfma_f32_16x16x32_bf16 v[64:67], a[248:251], v[8:11], 0\n\t"
      "v_mfma_f32_16x16x32_bf16 v[68:71], a[252:255], v[8:11], 0\n\t"
      "s_nop 1\n\t"
      "v_mov_b32_dpp v40, v40 quad_perm:[0,0,0,0] row_mask:0xf bank_mask:0xf\n\t"
      "v_mov_b32_dpp v44, v44 quad_perm:[0,0,0,0] row_mask:0xf bank_mask:0xf\n\t"
      "v_mov_b32_dpp v48, v48 quad_perm:[0,0,0,0] row_mask:0xf bank_mask:0xf\n\t"
      "v_mov_b32_dpp v52, v52 quad_perm:[0,0,0,0] row_mask:0xf bank_mask:0xf\n\t"
      "v_add_f32_dpp v40, v41, v40 quad_perm:[1,1,1,1] row_mask:0xf bank_mask:0xf\n\t"
      "v_add_f32_dpp v44, v45, v44 quad_perm:[1,1,1,1] row_mask:0xf bank_mask:0xf\n\t"
      "v_add_f32_dpp v48, v49, v48 quad_perm:[1,1,1,1] row_mask:0xf bank_mask:0xf\n\t"
      "v_add_f32_dpp v52, v53, v52 quad_perm:[1,1,1,1] row_mask:0xf bank_mask:0xf\n\t"
      "v_add_f32_dpp v40, v42, v40 quad_perm:[2,2,2,2] row_mask:0xf bank_mask:0xf\n\t"
      "v_add_f32_dpp v44, v46, v44 quad_perm:[2,2,2,2] row_mask:0xf bank_mask:0xf\n\t"
      "v_add_f32_dpp v48, v50, v48 quad_perm:[2,2,2,2] row_mask:0xf bank_mask:0xf\n\t"
      "v_add_f32_dpp v52, v54, v52 quad_perm:[2,2,2,2] row_mask:0xf bank_mask:0xf\n\t"
      "v_add_f32_dpp v40, v43, v40 quad_perm:[3,3,3,3] row_mask:0xf bank_mask:0xf\n\t"
      "v_add_f32_dpp v44, v47, v44 quad_perm:[3,3,3,3] row_mask:0xf bank_mask:0xf\n\t"
      "v_add_f32_dpp v48, v51, v48 quad_perm:[3,3,3,3] row_mask:0xf bank_mask:0xf\n\t"
      "v_add_f32_dpp v52, v55, v52 quad_perm:[3,3,3,3] row_mask:0xf bank_mask:0xf\n\t"
      "v_mov_b32_dpp v56, v56 quad_perm:[0,0,0,0] row_mask:0xf bank_mask:0xf\n\t"
      "v_mov_b32_dpp v60, v60 quad_perm:[0,0,0,0] row_mask:0xf bank_mask:0xf\n\t"
      "v_mov_b32_dpp v64, v64 quad_perm:[0,0,0,0] row_mask:0xf bank_mask:0xf\n\t"
      "v_mov_b32_dpp v68, v68 quad_perm:[0,0,0,0] row_mask:0xf bank_mask:0xf\n\t"
      "v_add_f32_dpp v56, v57, v56 quad_perm:[1,1,1,1] row_mask:0xf bank_mask:0xf\n\t"
      "v_add_f32_dpp v60, v61, v60 quad_perm:[1,1,1,1] row_mask:0xf bank_mask:0xf\n\t"
      "v_add_f32_dpp v64, v65, v64 quad_perm:[1,1,1,1] row_mask:0xf bank_mask:0xf\n\t"
      "v_add_f32_dpp v68, v69, v68 quad_perm:[1,1,1,1] row_mask:0xf bank_mask:0xf\n\t"
      "v_add_f32_dpp v56, v58, v56 quad_perm:[2,2,2,2] row_mask:0xf bank_mask:0xf\n\t"
      "v_add_f32_dpp v60, v62, v60 quad_perm:[2,2,2,2] row_mask:0xf bank_mask:0xf\n\t"
      "v_add_f32_dpp v64, v66, v64 quad_perm:[2,2,2,2] row_mask:0xf bank_mask:0xf\n\t"
      "v_add_f32_dpp v68, v70, v68 quad_perm:[2,2,2,2] row_mask:0xf bank_mask:0xf\n\t"
      "v_add_f32_dpp v56, v59, v56 quad_perm:[3,3,3,3] row_mask:0xf bank_mask:0xf\n\t"
      "v_add_f32_dpp v60, v63, v60 quad_perm:[3,3,3,3] row_mask:0xf bank_mask:0xf\n\t"
      "v_add_f32_dpp v64, v67, v64 quad_perm:[3,3,3,3] row_mask:0xf bank_mask:0xf\n\t"
      "v_add_f32_dpp v68, v71, v68 quad_perm:[3,3,3,3] row_mask:0xf bank_mask:0xf\n\t"
      "v_max3_f32 v22, v26, v27, v20\n\t"
      "v_max3_f32 v23, v28, v29, v30\n\t"
      "v_max3_f32 v22, v22, v31, v32\n\t"
      "v_max3_f32 v23, v23, v33, v40\n\t"
      "v_max3_f32 v22, v22, v44, v48\n\t"
      "v_max3_f32 v23, v23, v52, v56\n\t"
      "v_max3_f32 v22, v22, v60, v64\n\t"
      "v_max3_f32 v22, v22, v23, v68\n\t"
      "v_mov_b32 v23, v22\n\t"
      "s_nop 1\n\t"
      "v_permlane16_swap_b32 v22, v23\n\t"
      "s_nop 0\n\t"
      "v_max_f32 v22, v22, v23\n\t"
      "v_mov_b32 v23, v22\n\t"
      "s_nop 1\n\t"
      "v_permlane32_swap_b32 v22, v23\n\t"
      "s_nop 0\n\t"
      "v_max_f32 v22, v22, v23\n\t"
      "v_sub_f32 v23, v20, v22\n\t"
      "v_mul_f32 v24, %[nc2], v22\n\t"
      "v_mul_f32 v23, %[c2], v23\n\t"
      "v_mov_b32 v20, v22\n\t"
      "v_exp_f32 v23, v23\n\t"
      "v_fma_f32 v26, v26, %[c2], v24\n\t"
      "v_fma_f32 v27, v27, %[c2], v24\n\t"
      "v_fma_f32 v28, v28, %[c2], v24\n\t"
      "v_fma_f32 v29, v29, %[c2], v24\n\t"
      "v_fma_f32 v30, v30, %[c2], v24\n\t"
      "v_fma_f32 v31, v31, %[c2], v24\n\t"
      "v_fma_f32 v32, v32, %[c2], v24\n\t"
      "v_fma_f32 v33, v33, %[c2], v24\n\t"
      "v_fma_f32 v40, v40, %[c2], v24\n\t"
      "v_fma_f32 v44, v44, %[c2], v24\n\t"
      "v_fma_f32 v48, v48, %[c2], v24\n\t"
      "v_fma_f32 v52, v52, %[c2], v24\n\t"
      "v_fma_f32 v56, v56, %[c2], v24\n\t"
      "v_fma_f32 v60, v60, %[c2], v24\n\t"
      "v_fma_f32 v64, v64, %[c2], v24\n\t"
      "v_fma_f32 v68, v68, %[c2], v24\n\t"
      "v_exp_f32 v26, v26\n\t"
      "v_mul_f32 v12, v23, v12\n\t"
      "v_exp_f32 v27, v27\n\t"
      "v_mul_f32 v13, v23, v13\n\t"
      "v_exp_f32 v28, v28\n\t"
      "v_mul_f32 v14, v23, v14\n\t"
      "v_exp_f32 v29, v29\n\t"
      "v_mul_f32 v15, v23, v15\n\t"
      "v_exp_f32 v30, v30\n\t"
      "v_mul_f32 v16, v23, v16\n\t"
      "v_exp_f32 v31, v31\n\t"
      "v_mul_f32 v17, v23, v17\n\t"
      "v_exp_f32 v32, v32\n\t"
      "v_mul_f32 v18, v23, v18\n\t"
      "v_exp_f32 v33, v33\n\t"
      "v_mul_f32 v19, v23, v19\n\t"
      "v_exp_f32 v40, v40\n\t"
      "v_exp_f32 v44, v44\n\t"
      "v_exp_f32 v48, v48\n\t"
      "v_exp_f32 v52, v52\n\t"
      "v_exp_f32 v56, v56\n\t"
      "v_exp_f32 v60, v60\n\t"
      "v_exp_f32 v64, v64\n\t"
      "v_exp_f32 v68, v68\n\t"
      "v_mul_f32 v21, v21, v23\n\t"
      "v_add_f32 v22, v26, v27\n\t"
      "v_add_f32 v23, v28, v29\n\t"
      "v_add_f32 v22, v22, v30\n\t"
      "v_add_f32 v23, v23, v31\n\t"
      "v_add_f32 v22, v22, v32\n\t"
      "v_add_f32 v23, v23, v33\n\t"
      "v_add_f32 v22, v22, v40\n\t"
      "v_add_f32 v23, v23, v44\n\t"
      "v_add_f32 v22, v22, v48\n\t"
      "v_add_f32 v23, v23, v52\n\t"
      "v_add_f32 v22, v22, v56\n\t"
      "v_add_f32 v23, v23, v60\n\t"
      "v_add_f32 v22, v22, v64\n\t"
      "v_add_f32 v23, v23, v68\n\t"
      "v_add_f32 v21, v21, v22\n\t"
      "v_cvt_pk_bf16_f32 v34, v26, v27\n\t"
      "v_cvt_pk_bf16_f32 v35, v28, v29\n\t"
      "v_cvt_pk_bf16_f32 v36, v30, v31\n\t"
      "v_cvt_pk_bf16_f32 v37, v32, v33\n\t"
      "v_add_f32 v21, v21, v23\n\t"
      "v_cvt_pk_bf16_f32 v38, v40, v44\n\t"
      "v_cvt_pk_bf16_f32 v39, v48, v52\n\t"
      "v_cvt_pk_bf16_f32 v24, v56, v60\n\t"
      "v_cvt_pk_bf16_f32 v25, v64, v68\n\t"
      "s_waitcnt vmcnt(8)\n\t"
      "v_perm_b32 v40, v196, v192, %[slo]\n\t"
      "v_perm_b32 v44, v196, v192, %[shi]\n\t"
      "v_perm_b32 v41, v204, v200, %[slo]\n\t"
      "v_perm_b32 v45, v204, v200, %[shi]\n\t"
      "v_perm_b32 v42, v212, v208, %[slo]\n\t"
      "v_perm_b32 v46, v212, v208, %[shi]\n\t"
      "v_perm_b32 v43, v220, v216, %[slo]\n\t"
      "v_perm_b32 v47, v220, v216, %[shi]\n\t"
      "v_perm_b32 v48, v197, v193, %[slo]\n\t"
      "v_perm_b32 v52, v197, v193, %[shi]\n\t"
      "v_perm_b32 v49, v205, v201, %[slo]\n\t"
      "v_perm_b32 v53, v205, v201, %[shi]\n\t"
      "v_perm_b32 v50, v213, v209, %[slo]\n\t"
      "v_perm_b32 v54, v213, v209, %[shi]\n\t"
      "v_perm_b32 v51, v221, v217, %[slo]\n\t"
      "v_perm_b32 v55, v221, v217, %[shi]\n\t"
      "v_perm_b32 v56, v198, v194, %[slo]\n\t"
      "v_perm_b32 v60, v198, v194, %[shi]\n\t"
      "v_perm_b32 v57, v206, v202, %[slo]\n\t"
      "v_perm_b32 v61, v206, v202, %[shi]\n\t"
      "v_perm_b32 v58, v214, v210, %[slo]\n\t"
      "v_perm_b32 v62, v214, v210, %[shi]\n\t"
      "v_perm_b32 v59, v222, v218, %[slo]\n\t"
      "v_perm_b32 v63, v222, v218, %[shi]\n\t"
      "v_perm_b32 v64, v199, v195, %[slo]\n\t"
      "v_perm_b32 v68, v199, v195, %[shi]\n\t"
      "v_perm_b32 v65, v207, v203, %[slo]\n\t"
      "v_perm_b32 v69, v207, v203, %[shi]\n\t"
      "v_perm_b32 v66, v215, v211, %[slo]\n\t"
      "v_perm_b32 v70, v215, v211, %[shi]\n\t"
      "v_perm_b32 v67, v223, v219, %[slo]\n\t"
      "v_perm_b32 v71, v223, v219, %[shi]\n\t"
      "v_cndmask_b32 v26, 0, v34, %[m0]\n\t"
      "v_cndmask_b32 v27, 0, v35, %[m0]\n\t"
      "v_cndmask_b32 v28, 0, v36, %[m0]\n\t"
      "v_cndmask_b32 v29, 0, v37, %[m0]\n\t"
      "v_cndmask_b32 v30, 0, v34, %[m1]\n\t"
      "v_cndmask_b32 v31, 0, v35, %[m1]\n\t"
      "v_cndmask_b32 v32, 0, v36, %[m1]\n\t"
      "v_cndmask_b32 v33, 0, v37, %[m1]\n\t"
      "v_mfma_f32_16x16x32_bf16 v[12:15], v[40:43], v[26:29], v[12:15]\n\t"
      "v_mfma_f32_16x16x32_bf16 v[16:19], v[56:59], v[26:29], v[16:19]\n\t"
      "v_cndmask_b32 v26, 0, v34, %[m2]\n\t"
      "v_cndmask_b32 v27, 0, v35, %[m2]\n\t"
      "v_cndmask_b32 v28, 0, v36, %[m2]\n\t"
      "v_cndmask_b32 v29, 0, v37, %[m2]\n\t"
      "v_mfma_f32_16x16x32_bf16 v[12:15], v[44:47], v[30:33], v[12:15]\n\t"
      "v_mfma_f32_16x16x32_bf16 v[16:19], v[60:63], v[30:33], v[16:19]\n\t"
      "v_cndmask_b32 v30, 0, v34, %[m3]\n\t"
      "v_cndmask_b32 v31, 0, v35, %[m3]\n\t"
      "v_cndmask_b32 v32, 0, v36, %[m3]\n\t"
      "v_cndmask_b32 v33, 0, v37, %[m3]\n\t"
      "v_mfma_f32_16x16x32_bf16 v[12:15], v[48:51], v[26:29], v[12:15]\n\t"
      "v_mfma_f32_16x16x32_bf16 v[16:19], v[64:67], v[26:29], v[16:19]\n\t"
      "v_mfma_f32_16x16x32_bf16 v[12:15], v[52:55], v[30:33], v[12:15]\n\t"
      "v_mfma_f32_16x16x32_bf16 v[16:19], v[68:71], v[30:33], v[16:19]\n\t"
      "s_waitcnt vmcnt(0)\n\t"
      "v_perm_b32 v40, v132, v128, %[slo]\n\t"
      "v_perm_b32 v44, v132, v128, %[shi]\n\t"
      "v_perm_b32 v41, v140, v136, %[slo]\n\t"
      "v_perm_b32 v45, v140, v136, %[shi]\n\t"
      "v_perm_b32 v42, v148, v144, %[slo]\n\t"
      "v_perm_b32 v46, v148, v144, %[shi]\n\t"
      "v_perm_b32 v43, v156, v152, %[slo]\n\t"
      "v_perm_b32 v47, v156, v152, %[shi]\n\t"
      "v_perm_b32 v48, v133, v129, %[slo]\n\t"
      "v_perm_b32 v52, v133, v129, %[shi]\n\t"
      "v_perm_b32 v49, v141, v137, %[slo]\n\t"
      "v_perm_b32 v53, v141, v137, %[shi]\n\t"
      "v_perm_b32 v50, v149, v145, %[slo]\n\t"
      "v_perm_b32 v54, v149, v145, %[shi]\n\t"
      "v_perm_b32 v51, v157, v153, %[slo]\n\t"
      "v_perm_b32 v55, v157, v153, %[shi]\n\t"
      "v_perm_b32 v56, v134, v130, %[slo]\n\t"
      "v_perm_b32 v60, v134, v130, %[shi]\n\t"
      "v_perm_b32 v57, v142, v138, %[slo]\n\t"
      "v_perm_b32 v61, v142, v138, %[shi]\n\t"
      "v_perm_b32 v58, v150, v146, %[slo]\n\t"
      "v_perm_b32 v62, v150, v146, %[shi]\n\t"
      "v_perm_b32 v59, v158, v154, %[slo]\n\t"
      "v_perm_b32 v63, v158, v154, %[shi]\n\t"
      "v_perm_b32 v64, v135, v131, %[slo]\n\t"
      "v_perm_b32 v68, v135, v131, %[shi]\n\t"
      "v_perm_b32 v65, v143, v139, %[slo]\n\t"
      "v_perm_b32 v69, v143, v139, %[shi]\n\t"
      "v_perm_b32 v66, v151, v147, %[slo]\n\t"
      "v_perm_b32 v70, v151, v147, %[shi]\n\t"
      "v_perm_b32 v67, v159, v155, %[slo]\n\t"
      "v_perm_b32 v71, v159, v155, %[shi]\n\t"
      "v_cndmask_b32 v26, 0, v38, %[m0]\n\t"
      "v_cndmask_b32 v27, 0, v39, %[m0]\n\t"
      "v_cndmask_b32 v28, 0, v24, %[m0]\n\t"
      "v_cndmask_b32 v29, 0, v25, %[m0]\n\t"
      "v_cndmask_b32 v30, 0, v38, %[m1]\n\t"
      "v_cndmask_b32 v31, 0, v39, %[m1]\n\t"
      "v_cndmask_b32 v32, 0, v24, %[m1]\n\t"
      "v_cndmask_b32 v33, 0, v25, %[m1]\n\t"
      "v_mfma_f32_16x16x32_bf16 v[12:15], v[40:43], v[26:29], v[12:15]\n\t"
      "v_mfma_f32_16x16x32_bf16 v[16:19], v[56:59], v[26:29], v[16:19]\n\t"
      "v_cndmask_b32 v26, 0, v38, %[m2]\n\t"
      "v_cndmask_b32 v27, 0, v39, %[m2]\n\t"
      "v_cndmask_b32 v28, 0, v24, %[m2]\n\t"
      "v_cndmask_b32 v29, 0, v25, %[m2]\n\t"
      "v_mfma_f32_16x16x32_bf16 v[12:15], v[44:47], v[30:33], v[12:15]\n\t"
      "v_mfma_f32_16x16x32_bf16 v[16:19], v[60:63], v[30:33], v[16:19]\n\t"
      "v_cndmask_b32 v30, 0, v38, %[m3]\n\t"
      "v_cndmask_b32 v31, 0, v39, %[m3]\n\t"
      "v_cndmask_b32 v32, 0, v24, %[m3]\n\t"
      "v_cndmask_b32 v33, 0, v25, %[m3]\n\t"
      "v_mfma_f32_16x16x32_bf16 v[12:15], v[48:51], v[26:29], v[12:15]\n\t"
      "v_mfma_f32_16x16x32_bf16 v[16:19], v[64:67], v[26:29], v[16:19]\n\t"
      "v_mfma_f32_16x16x32_bf16 v[12:15], v[52:55], v[30:33], v[12:15]\n\t"
      "v_mfma_f32_16x16x32_bf16 v[16:19], v[68:71], v[30:33], v[16:19]\n\t"
      "s_nop 7\n\t"
      : "=&{v[12:15]}"(st.a0), "=&{v[16:19]}"(st.a1), "=&{v20}"(st.m), "=&{v21}"(st.l), [so0] "=&s"(so0), [so1] "=&s"(so1), [due] "+s"(due)
      : "{v[8:11]}"(qf), PASS_IN, PASS_CONST, [dl] "s"(DL2), [dlh] "s"(DLL), [dlx] "s"(DL2 * (16 - 6) / 16)
      : "memory", "v22", "v23", "v24", "v25", "v26", "v27", "v28", "v29", "v30", "v31", "v32", "v33", "v34", "v35", "v36", "v37", "v38", "v39", "v40", "v41", "v42", "v43", "v44", "v45", "v46", "v47", "v48", "v49", "v50", "v51", "v52", "v53", "v54", "v55", "v56", "v57", "v58", "v59", "v60", "v61", "v62", "v63", "v64", "v65", "v66", "v67", "v68", "v69", "v70", "v71", "scc", "vcc", "s96", "s97", "v72", "v73", "v74", "v75", "v76", "v77", "v78", "v79", "v80", "v81", "v82", "v83", "v84", "v85", "v86", "v87", "v88", "v89", "v90", "v91", "v92", "v93", "v94", "v95", "v96", "v97", "v98", "v99", "v100", "v101", "v102", "v103", "v104", "v105", "v106", "v107", "v108", "v109", "v110", "v111", "v112", "v113", "v114", "v115", "v116", "v117", "v118", "v119", "v120", "v121", "v122", "v123", "v124", "v125", "v126", "v127", "v128", "v129", "v130", "v131", "v132", "v133", "v134", "v135", "v136", "v137", "v138", "v139", "v140", "v141", "v142", "v143", "v144", "v145", "v146", "v147", "v148", "v149", "v150", "v151", "v152", "v153", "v154", "v155", "v156", "v157", "v158", "v159", "v160", "v161", "v162", "v163", "v164", "v165", "v166", "v167", "v168", "v169", "v170", "v171", "v172", "v173", "v174", "v175", "v176", "v177", "v178", "v179", "v180", "v181", "v182", "v183", "v184", "v185", "v186", "v187", "v188", "v189", "v190", "v191", "v192", "v193", "v194", "v195", "v196", "v197", "v198", "v199", "v200", "v201", "v202", "v203", "v204", "v205", "v206", "v207", "v208", "v209", "v210", "v211", "v212", "v213", "v214", "v215", "v216", "v217", "v218", "v219", "v220", "v221", "v222", "v223", "v224", "v225", "v226", "v227", "v228", "v229", "v230", "v231", "v232", "v233", "v234", "v235", "v236", "v237", "v238", "v239", "v240", "v241", "v242", "v243", "v244", "v245", "v246", "v247", "v248", "v249", "v250", "v251", "v252", "v253", "v254", "v255", "a0", "a1", "a2", "a3", "a4", "a5", "a6", "a7", "a8", "a9", "a10", "a11", "a12", "a13", "a14", "a15", "a16", "a17", "a18", "a19", "a20", "a21", "a22", "a23", "a24", "a25", "a26", "a27", "a28", "a29", "a30", "a31", "a32", "a33", "a34", "a35", "a36", "a37", "a38", "a39", "a40", "a41", "a42", "a43", "a44", "a45", "a46", "a47", "a48", "a49", "a50", "a51", "a52", "a53", "a54", "a55", "a56", "a57", "a58", "a59", "a60", "a61", "a62", "a63", "a64", "a65", "a66", "a67", "a68", "a69", "a70", "a71", "a72", "a73", "a74", "a75", "a76", "a77", "a78", "a79", "a80", "a81", "a82", "a83", "a84", "a85", "a86", "a87", "a88", "a89", "a90", "a91", "a92", "a93", "a94", "a95", "a96", "a97", "a98", "a99", "a100", "a101", "a102", "a103", "a104", "a105", "a106", "a107", "a108", "a109", "a110", "a111", "a112", "a113", "a114", "a115", "a116", "a117", "a118", "a119", "a120", "a121", "a122", "a123", "a124", "a125", "a126", "a127", "a128", "a129", "a130", "a131", "a132", "a133", "a134", "a135", "a136", "a137", "a138", "a139", "a140", "a141", "a142", "a143", "a144", "a145", "a146", "a147", "a148", "a149", "a150", "a151", "a152", "a153", "a154", "a155", "a156", "a157", "a158", "a159", "a160", "a161", "a162", "a163", "a164", "a165", "a166", "a167", "a168", "a169", "a170", "a171", "a172", "a173", "a174", "a175", "a176", "a177", "a178", "a179", "a180", "a181", "a182", "a183", "a184", "a185", "a186", "a187", "a188", "a189", "a190", "a191", "a192", "a193", "a194", "a195", "a196", "a197", "a198", "a199", "a200", "a201", "a202", "a203", "a204", "a205", "a206", "a207", "a208", "a209", "a210", "a211", "a212", "a213", "a214", "a215", "a216", "a217", "a218", "a219", "a220", "a221", "a222", "a223", "a224", "a225", "a226", "a227", "a228", "a229", "a230", "a231", "a232", "a233", "a234", "a235", "a236", "a237", "a238", "a239", "a240", "a241", "a242", "a243", "a244", "a245", "a246", "a247", "a248", "a249", "a250", "a251", "a252", "a253", "a254", "a255");
  LAUNDER();
  if (c.wave == 1) CODE_TOUCH("v70", "v71", c.lane + CODE_LINES - 64, c.lane + CODE_LINES - 64);
  c.tag += REUSE;
  middle<1>(c, st, xres, out, due - ((stg16 * DL2) >> 7) + TPOLL2);
}

extern "C" void kernel_launch(void* const* d_in, const int* in_sizes, int n_in, void* d_out, int out_size, void* d_ws, size_t ws_size, hipStream_t stream) {
  (void)hipFuncSetAttribute((const void*)fused_kernel, hipFuncAttributeMaxDynamicSharedMemorySize, LDS_BYTES);
  fused_kernel<<<256, 256, LDS_BYTES, stream>>>((const u16*)d_in[0], (const u16*)d_in[1], (const u16*)d_in[2], (const u16*)d_in[3], (const u16*)d_in[4],
      (const u16*)d_in[6], (const u16*)d_in[7], (const u16*)d_in[8], (u16*)d_out, (unsigned char*)d_ws);
}
```
